```python
import torch

def _softmax(x, axis=-1):
    return torch.softmax(x, dim=axis)
def _gelu(x):
    return torch.nn.functional.gelu(x, approximate="tanh")
def _silu(x):
    return torch.nn.functional.silu(x)
def _sigmoid(x):
    return torch.sigmoid(x)
def _layernorm(x, axis=-1, eps=1e-5):
    m = x.mean(dim=axis, keepdim=True)
    v = x.var(dim=axis, keepdim=True, unbiased=False)
    return (x - m) / torch.sqrt(v + eps)
def _scatter_add(tgt, idx, src, axis=0):
    return tgt.clone().index_add_(axis, idx, src)
def _segment_sum(src, seg, n):
    out = torch.zeros((n,) + src.shape[1:], dtype=src.dtype)
    return out.index_add_(0, seg, src)
def _pad(x, pw):
    flat = tuple(v for pair in reversed(pw) for v in pair)
    return torch.nn.functional.pad(x, flat)

kernel_name = 'recurrent_scan__grouped_kv_mqa__dense_activation_relu__prompt'

def setup_inputs():

    _w = (lambda a: (lambda t: t.to(torch.bfloat16) if t.is_floating_point() else t)(torch.as_tensor(a)))
    def _A_setup():
        B, S, D, H = 1, 2048, 512, 32
        Dh = D // H
        return {
            "x": _w((lambda *s: torch.randn(*s, dtype=torch.bfloat16) * 0.05)(B, S, D)),
            "wq": _w((lambda *s: torch.randn(*s, dtype=torch.bfloat16) * 0.05)(D, H * Dh)),
            "wk": _w((lambda *s: torch.randn(*s, dtype=torch.bfloat16) * 0.05)(D, Dh)),
            "wv": _w((lambda *s: torch.randn(*s, dtype=torch.bfloat16) * 0.05)(D, Dh)),
            "wo": _w((lambda *s: torch.randn(*s, dtype=torch.bfloat16) * 0.05)(H * Dh, D)),
        }
    _A_in = _A_setup()

    def _B_setup():
        B, S, D, Dff = 1, 2048, 512, 1792
        return {
            "x": _w((lambda *s: torch.randn(*s, dtype=torch.bfloat16) * 0.05)(B, S, D)),
            "w1": _w((lambda *s: torch.randn(*s, dtype=torch.bfloat16) * 0.05)(D, Dff)),
            "w2": _w((lambda *s: torch.randn(*s, dtype=torch.bfloat16) * 0.05)(Dff, D)),
        }
    _B_in = _B_setup()

    return {
        "A_x": _A_in["x"],
        "A_wq": _A_in["wq"],
        "A_wk": _A_in["wk"],
        "A_wv": _A_in["wv"],
        "A_wo": _A_in["wo"],
        "B_x": _B_in["x"],
        "B_w1": _B_in["w1"],
        "B_w2": _B_in["w2"],
    }

def reference(A_x, A_wq, A_wk, A_wv, A_wo, B_x, B_w1, B_w2):
    def _A_ref(x, wq, wk, wv, wo):
        B, S, D = x.shape
        H = 32
        Dh = D // H
        q = (lambda x, *d: x.permute(*d))((x @ wq).reshape(B, S, H, Dh), 0, 2, 1, 3)
        k = x @ wk
        v = x @ wv
        scores = torch.einsum("bhqd,bkd->bhqk", q, k) * (Dh ** -0.5)
        p = _softmax(scores, axis=-1)
        o = torch.einsum("bhqk,bkd->bhqd", p, v)
        return (lambda x, *d: x.permute(*d))(o, 0, 2, 1, 3).reshape(B, S, H * Dh) @ wo

    def _B_ref(x, w1, w2):
        h = torch.einsum("bsd,df->bsf", x, w1)
        zero = torch.zeros_like(h)
        h = torch.maximum(h, zero)
        return torch.einsum("bsf,fd->bsd", h, w2)

    x = A_x
    xn = _layernorm(x, axis=-1)
    s = _A_ref(xn, A_wq, A_wk, A_wv, A_wo)
    g = _silu(_B_ref(xn, B_w1, B_w2))
    return x + s * g
```

```cpp
#include <hip/hip_runtime.h>
#include <hip/hip_bf16.h>
typedef unsigned short u16;
__device__ __forceinline__ float bf2f(u16 v) { return __uint_as_float(((unsigned)v) << 16); }
__device__ __forceinline__ u16 f2bf(float f) { __bf16 b = (__bf16)f; return __builtin_bit_cast(u16, b); }
constexpr int S = 2048, D = 512, F = 1792;
__global__ void k_ln(const u16* x, u16* xn) {
  int r = blockIdx.x; int t = threadIdx.x;
  float v[8]; float s = 0;
  for (int i = 0; i < 8; ++i) { v[i] = bf2f(x[r * D + t * 8 + i]); s += v[i]; }
  for (int o = 32; o; o >>= 1) s += __shfl_xor(s, o);
  float m = s / D; float q = 0;
  for (int i = 0; i < 8; ++i) { float d = v[i] - m; q += d * d; }
  for (int o = 32; o; o >>= 1) q += __shfl_xor(q, o);
  float rs = rsqrtf(q / D + 1e-5f);
  for (int i = 0; i < 8; ++i) xn[r * D + t * 8 + i] = f2bf((v[i] - m) * rs);
}
template <int ACT>
__global__ void k_gemm(const u16* A, const u16* B, u16* C, int M, int N, int K) {
  __shared__ float As[16][17], Bs[16][17];
  int tx = threadIdx.x & 15, ty = threadIdx.x >> 4;
  int row = blockIdx.y * 16 + ty, col = blockIdx.x * 16 + tx;
  float acc = 0;
  for (int k0 = 0; k0 < K; k0 += 16) {
    As[ty][tx] = bf2f(A[row * K + k0 + tx]);
    Bs[ty][tx] = bf2f(B[(k0 + ty) * N + col]);
    __syncthreads();
    for (int k = 0; k < 16; ++k) acc += As[ty][k] * Bs[k][tx];
    __syncthreads();
  }
  if (ACT == 1) acc = fmaxf(acc, 0.f);
  C[row * N + col] = f2bf(acc);
}
__global__ void k_attn(const u16* q, const u16* k, const u16* v, u16* o) {
  int id = blockIdx.x * blockDim.x + threadIdx.x; int row = id >> 5, h = id & 31;
  float qv[16]; for (int d = 0; d < 16; ++d) qv[d] = bf2f(q[row * D + h * 16 + d]) * 0.25f;
  float m = -1e30f, l = 0, acc[16]; for (int d = 0; d < 16; ++d) acc[d] = 0;
  for (int j = 0; j < S; ++j) {
    float s = 0; for (int d = 0; d < 16; ++d) s += qv[d] * bf2f(k[j * 16 + d]);
    float mn = fmaxf(m, s); float a = __expf(m - mn), p = __expf(s - mn);
    l = l * a + p; for (int d = 0; d < 16; ++d) acc[d] = acc[d] * a + p * bf2f(v[j * 16 + d]);
    m = mn;
  }
  for (int d = 0; d < 16; ++d) o[row * D + h * 16 + d] = f2bf(acc[d] / l);
}
__global__ void k_fin(const u16* x, const u16* s, const u16* y, u16* out) {
  int i = blockIdx.x * blockDim.x + threadIdx.x;
  float yy = bf2f(y[i]); float g = yy / (1.f + __expf(-yy));
  out[i] = f2bf(bf2f(x[i]) + bf2f(s[i]) * g);
}
extern "C" void kernel_launch(void* const* d_in, const int* in_sizes, int n_in, void* d_out, int out_size, void* d_ws, size_t ws_size, hipStream_t stream) {
  const u16* x = (const u16*)d_in[0]; const u16* wq = (const u16*)d_in[1]; const u16* wk = (const u16*)d_in[2]; const u16* wv = (const u16*)d_in[3];
  const u16* wo = (const u16*)d_in[4]; const u16* w1 = (const u16*)d_in[6]; const u16* w2 = (const u16*)d_in[7];
  u16* ws = (u16*)d_ws; u16* xn = ws; u16* q = xn + S * D; u16* k = q + S * D; u16* v = k + S * 16; u16* o = v + S * 16; u16* s = o + S * D; u16* y = s + S * D; u16* h = y + S * D;
  k_ln<<<S, 64, 0, stream>>>(x, xn);
  k_gemm<0><<<dim3(D / 16, S / 16), 256, 0, stream>>>(xn, wq, q, S, D, D);
  k_gemm<0><<<dim3(1, S / 16), 256, 0, stream>>>(xn, wk, k, S, 16, D);
  k_gemm<0><<<dim3(1, S / 16), 256, 0, stream>>>(xn, wv, v, S, 16, D);
  k_attn<<<S * 32 / 64, 64, 0, stream>>>(q, k, v, o);
  k_gemm<0><<<dim3(D / 16, S / 16), 256, 0, stream>>>(o, wo, s, S, D, D);
  k_gemm<1><<<dim3(F / 16, S / 16), 256, 0, stream>>>(xn, w1, h, S, F, D);
  k_gemm<0><<<dim3(D / 16, S / 16), 256, 0, stream>>>(h, w2, y, S, D, F);
  k_fin<<<S * D / 256, 256, 0, stream>>>(x, s, y, (u16*)d_out);
}
```

```cpp
#include <hip/hip_runtime.h>

__global__ void __launch_bounds__(512, 2)
recurrent_scan__grouped_kv_mqa__dense_activation_relu__prompt_kernel(const void* x, const void* wq, const void* wk, const void* wv, const void* wo,
              const void* w1, const void* w2, void* out, void* ws) {
  __shared__ __attribute__((aligned(16))) char lds[163840];
  asm volatile(
    ".L_top_k:\n"
    "s_load_dwordx16 s[52:67], %0, 0\n"
    "s_load_dwordx2 s[68:69], %0, 64\n"
    "v_and_b32 v1, 63, %2\n"
    "v_lshrrev_b32 v2, 6, %2\n"
    "s_mov_b32 s98, %1\n"
    "v_readfirstlane_b32 s72, v2\n"
    "s_and_b32 s97, s98, 7\n"
    "s_lshr_b32 s77, s98, 3\n"
    "s_and_b32 s74, s77, 3\n"
    "s_lshl_b32 s74, s74, 1\n"
    "s_and_b32 s76, s97, 1\n"
    "s_add_u32 s74, s74, s76\n"
    "s_lshr_b32 s77, s77, 2\n"
    "s_and_b32 s73, s97, 6\n"
    "s_lshl_b32 s73, s73, 2\n"
    "s_add_u32 s73, s73, s77\n"
    "s_and_b32 s75, s72, 3\n"
    "s_and_b32 s79, s74, 6\n"
    "s_add_u32 s79, s79, s72\n"
    "s_and_b32 s79, s79, 7\n"
    "v_lshrrev_b32 v3, 3, v1\n"
    "v_and_b32 v4, 7, v1\n"
    "v_lshlrev_b32 v5, 10, v3\n"
    "v_lshl_or_b32 v5, v4, 4, v5\n"
    "s_lshl_b32 s76, s73, 3\n"
    "s_add_u32 s76, s76, s79\n"
    "s_lshl_b32 s76, s76, 13\n"
    "v_lshlrev_b32 v112, 2, v1\n"
    "v_add_u32 v112, 163584, v112\n"
    "v_mov_b32 v113, 0\n"
    "s_getpc_b64 s[80:81]\n"
    ".L_pc_k:\n"
    "s_lshr_b32 s77, s98, 3\n"
    "s_lshl_b32 s77, s77, 3\n"
    "s_add_u32 s77, s77, s72\n"
    "s_mul_i32 s77, s77, 2\n"
    "v_min_u32 v114, 1, v1\n"
    "v_add_u32 v114, s77, v114\n"
    "v_lshlrev_b32 v114, 7, v114\n"
    "v_min_u32 v114, .L_end_k-.L_pc_k, v114\n"
    "global_load_dword v210, v114, s[80:81]\n"
    "ds_write_b32 v112, v113\n"
    "v_xor_b32 v6, v3, v4\n"
    "v_lshlrev_b32 v6, 4, v6\n"
    "v_lshl_or_b32 v6, v4, 10, v6\n"
    "s_lshl_b32 s77, s79, 7\n"
    "v_add_u32 v6, s77, v6\n"
    "s_mov_b32 s14, s76\n"
    "s_waitcnt lgkmcnt(0)\n"
    "s_barrier\n"
    "s_load_dwordx2 s[70:71], s[68:69], 0\n"
    "s_mov_b32 s20, s52\n"
    "s_and_b32 s21, s53, 0xffff\n"
    "s_mov_b32 s22, 2097152\n"
    "s_mov_b32 s23, 0x20000\n"
    "s_cmp_ge_u32 s72, 4\n"
    "s_cbranch_scc1 .L_gemm_k\n"
    "buffer_load_dwordx4 v[8:11], v5, s[20:23], s14 offen offset:0\n"
    "buffer_load_dwordx4 v[12:15], v5, s[20:23], s14 offen offset:128\n"
    "buffer_load_dwordx4 v[16:19], v5, s[20:23], s14 offen offset:256\n"
    "buffer_load_dwordx4 v[20:23], v5, s[20:23], s14 offen offset:384\n"
    "buffer_load_dwordx4 v[24:27], v5, s[20:23], s14 offen offset:512\n"
    "buffer_load_dwordx4 v[28:31], v5, s[20:23], s14 offen offset:640\n"
    "buffer_load_dwordx4 v[32:35], v5, s[20:23], s14 offen offset:768\n"
    "buffer_load_dwordx4 v[36:39], v5, s[20:23], s14 offen offset:896\n"
    "s_bitcmp1_b32 s74, 0\n"
    "s_cselect_b32 s80, s58, s56\n"
    "s_cselect_b32 s81, s59, s57\n"
    "s_mov_b32 s28, s80\n"
    "s_and_b32 s29, s81, 0xffff\n"
    "s_mov_b32 s30, 16384\n"
    "s_mov_b32 s31, 0x20000\n"
    "v_bfe_u32 v114, v1, 3, 2\n"
    "v_lshlrev_b32 v114, 3, v114\n"
    "v_lshrrev_b32 v115, 5, v1\n"
    "v_lshl_or_b32 v114, v115, 2, v114\n"
    "v_bfe_u32 v115, v1, 1, 2\n"
    "v_or_b32 v114, v114, v115\n"
    "v_and_b32 v115, 1, v1\n"
    "v_lshlrev_b32 v115, 4, v115\n"
    "v_lshl_or_b32 v114, v114, 5, v115\n"
    "s_lshl_b32 s77, s75, 12\n"
    "s_add_u32 m0, s77, 139008\n"
    "s_nop 0\n"
    "buffer_load_dwordx4 v114, s[28:31], s77 offen offset:0 lds\n"
    "buffer_load_dwordx4 v114, s[28:31], s77 offen offset:1024 lds\n"
    "buffer_load_dwordx4 v114, s[28:31], s77 offen offset:2048 lds\n"
    "buffer_load_dwordx4 v114, s[28:31], s77 offen offset:3072 lds\n"
    "s_mov_b32 s48, s68\n"
    "s_and_b32 s49, s69, 0xffff\n"
    "s_mov_b32 s50, 12582912\n"
    "s_mov_b32 s51, 0x20000\n"
    "v_and_b32 v112, 15, v1\n"
    "v_mov_b32 v113, 0x3f803f80\n"
    "v_cmp_eq_u32 vcc, 0, v112\n"
    "s_nop 1\n"
    "v_cndmask_b32 v114, 0, v113, vcc\n"
    "v_cndmask_b32 v115, 0, v113, vcc\n"
    "v_mov_b64 v[136:137], v[114:115]\n"
    "v_mov_b64 v[138:139], v[114:115]\n"
    "v_mov_b64 v[140:141], v[114:115]\n"
    "v_mov_b64 v[142:143], v[114:115]\n"
    "v_mov_b64 v[144:145], v[114:115]\n"
    "v_mov_b64 v[146:147], v[114:115]\n"
    "v_mov_b64 v[148:149], v[114:115]\n"
    "v_mov_b64 v[150:151], v[114:115]\n"
    "v_mov_b64 v[152:153], v[114:115]\n"
    "v_mov_b64 v[154:155], v[114:115]\n"
    "v_mov_b64 v[156:157], v[114:115]\n"
    "v_mov_b64 v[158:159], v[114:115]\n"
    "v_mov_b64 v[160:161], v[114:115]\n"
    "v_mov_b64 v[162:163], v[114:115]\n"
    "v_mov_b64 v[164:165], v[114:115]\n"
    "v_mov_b64 v[166:167], v[114:115]\n"
    "v_mov_b64 v[168:169], v[114:115]\n"
    "v_mov_b64 v[170:171], v[114:115]\n"
    "v_mov_b64 v[172:173], v[114:115]\n"
    "v_mov_b64 v[174:175], v[114:115]\n"
    "v_mov_b64 v[176:177], v[114:115]\n"
    "v_mov_b64 v[178:179], v[114:115]\n"
    "v_mov_b64 v[180:181], v[114:115]\n"
    "v_mov_b64 v[182:183], v[114:115]\n"
    "v_mov_b64 v[184:185], v[114:115]\n"
    "v_mov_b64 v[186:187], v[114:115]\n"
    "v_mov_b64 v[188:189], v[114:115]\n"
    "v_mov_b64 v[190:191], v[114:115]\n"
    "v_mov_b64 v[192:193], v[114:115]\n"
    "v_mov_b64 v[194:195], v[114:115]\n"
    "v_mov_b64 v[196:197], v[114:115]\n"
    "v_mov_b64 v[198:199], v[114:115]\n"
    "v_mov_b64 v[232:233], 0\n"
    "v_mov_b64 v[234:235], 0\n"
    "v_mov_b64 v[236:237], 0\n"
    "v_mov_b64 v[238:239], 0\n"
    "v_mov_b64 v[240:241], 0\n"
    "v_mov_b64 v[242:243], 0\n"
    "v_mov_b64 v[244:245], 0\n"
    "v_mov_b64 v[246:247], 0\n"
    "v_lshrrev_b32 v112, 4, v1\n"
    "v_and_b32 v249, 15, v1\n"
    "v_xor_b32 v249, v249, v112\n"
    "v_lshlrev_b32 v249, 4, v249\n"
    "v_lshl_or_b32 v249, v112, 10, v249\n"
    "v_xor_b32 v250, 64, v249\n"
    "v_lshrrev_b32 v112, 2, v1\n"
    "v_lshlrev_b32 v251, 7, v112\n"
    "v_bfe_u32 v112, v1, 3, 2\n"
    "v_lshl_or_b32 v251, v112, 5, v251\n"
    "v_and_b32 v112, 3, v1\n"
    "v_lshl_or_b32 v251, v112, 3, v251\n"
    "v_and_b32 v248, 15, v1\n"
    "v_lshrrev_b32 v112, 5, v1\n"
    "v_lshl_or_b32 v248, v112, 5, v248\n"
    "s_and_b32 s76, s75, 1\n"
    "s_lshl_b32 s76, s76, 4\n"
    "v_or_b32 v248, s76, v248\n"
    "v_lshlrev_b32 v248, 4, v248\n"
    "v_bfe_u32 v112, v1, 4, 1\n"
    "v_lshl_or_b32 v248, v112, 3, v248\n"
    "s_lshr_b32 s76, s75, 1\n"
    "s_lshl_b32 s76, s76, 12\n"
    "s_add_u32 s76, s76, 155392\n"
    "v_add_u32 v248, s76, v248\n"
    "s_lshl_b32 s76, s75, 5\n"
    "v_xor_b32 v251, s76, v251\n"
    "v_add_u32 v251, 73472, v251\n"
    "v_and_b32 v112, 31, v1\n"
    "v_lshrrev_b32 v113, 5, v1\n"
    "v_lshlrev_b32 v253, 5, v112\n"
    "v_lshl_or_b32 v253, v113, 4, v253\n"
    "v_lshlrev_b32 v254, 4, v1\n"
    "v_add_u32 v2, 155392, v254\n"
    "s_lshl_b32 s76, s75, 11\n"
    "s_add_u32 s76, s76, 65536\n"
    "v_add_u32 v3, s76, v254\n"
    "s_lshl_b32 s76, s75, 2\n"
    "s_add_u32 s76, s76, 73728\n"
    "v_add_u32 v4, s76, v254\n"
    "v_mov_b32 v7, 163584\n"
    "v_add_u32 v5, s75, v7\n"
    "s_add_u32 s76, s98, s75\n"
    "s_cmp_eq_u32 s76, 0\n"
    "s_cselect_b32 s86, 2, 0x01010101\n"
    "s_mov_b32 s87, 0x45c80000\n"
    "s_lshl_b32 s90, s97, 12\n"
    "s_lshl_b32 s77, s75, 10\n"
    "s_add_u32 s90, s90, s77\n"
    "s_add_u32 s90, s90, 32768\n"
    "s_lshl_b32 s85, s75, 14\n"
    "s_add_u32 s85, s85, 65536\n"
    "s_lshl_b32 s93, s98, 10\n"
    "s_and_b32 s93, s93, 0x2000\n"
    "s_add_u32 s91, s85, s93\n"
    "s_add_u32 s92, s91, 65536\n"
    "v_mov_b32 v252, 0xdddd\n"
    "s_lshr_b32 s76, s74, 1\n"
    "s_lshl_b32 s83, s73, 2\n"
    "s_add_u32 s83, s83, s76\n"
    "s_lshl_b32 s84, s83, 1\n"
    "s_and_b32 s77, s74, 1\n"
    "s_add_u32 s84, s84, s77\n"
    "s_lshl_b32 s76, s76, 8\n"
    "s_lshl_b32 s77, s75, 14\n"
    "s_add_u32 s76, s76, s77\n"
    "v_add_u32 v226, s76, v249\n"
    "v_add_u32 v227, s76, v250\n"
    "v_lshlrev_b32 v228, 3, v1\n"
    "s_lshl_b32 s77, s75, 12\n"
    "s_add_u32 s77, s77, 139008\n"
    "v_add_u32 v228, s77, v228\n"
    "s_lshl_b32 s77, s75, 10\n"
    "v_add_u32 v229, s77, v2\n"
    "s_setprio 3\n"
    "s_waitcnt vmcnt(4)\n"
    "s_waitcnt lgkmcnt(0)\n"
    "s_add_u32 s70, s70, 1\n"
    "s_addc_u32 s71, s71, 0\n"
    "s_mov_b32 s76, 0xffff0000\n"
    "v_lshlrev_b32 v40, 16, v8\n"
    "v_and_b32 v41, s76, v8\n"
    "v_lshlrev_b32 v42, 16, v9\n"
    "v_and_b32 v43, s76, v9\n"
    "v_lshlrev_b32 v44, 16, v10\n"
    "v_and_b32 v45, s76, v10\n"
    "v_lshlrev_b32 v46, 16, v11\n"
    "v_and_b32 v47, s76, v11\n"
    "v_mov_b64 v[104:105], v[40:41]\n"
    "v_pk_mul_f32 v[108:109], v[40:41], v[40:41]\n"
    "v_mov_b64 v[106:107], v[42:43]\n"
    "v_pk_mul_f32 v[110:111], v[42:43], v[42:43]\n"
    "v_pk_add_f32 v[104:105], v[104:105], v[44:45]\n"
    "v_pk_fma_f32 v[108:109], v[44:45], v[44:45], v[108:109]\n"
    "v_pk_add_f32 v[106:107], v[106:107], v[46:47]\n"
    "v_pk_fma_f32 v[110:111], v[46:47], v[46:47], v[110:111]\n"
    "v_lshlrev_b32 v48, 16, v12\n"
    "v_and_b32 v49, s76, v12\n"
    "v_lshlrev_b32 v50, 16, v13\n"
    "v_and_b32 v51, s76, v13\n"
    "v_lshlrev_b32 v52, 16, v14\n"
    "v_and_b32 v53, s76, v14\n"
    "v_lshlrev_b32 v54, 16, v15\n"
    "v_and_b32 v55, s76, v15\n"
    "v_pk_add_f32 v[104:105], v[104:105], v[48:49]\n"
    "v_pk_fma_f32 v[108:109], v[48:49], v[48:49], v[108:109]\n"
    "v_pk_add_f32 v[106:107], v[106:107], v[50:51]\n"
    "v_pk_fma_f32 v[110:111], v[50:51], v[50:51], v[110:111]\n"
    "v_pk_add_f32 v[104:105], v[104:105], v[52:53]\n"
    "v_pk_fma_f32 v[108:109], v[52:53], v[52:53], v[108:109]\n"
    "v_pk_add_f32 v[106:107], v[106:107], v[54:55]\n"
    "v_pk_fma_f32 v[110:111], v[54:55], v[54:55], v[110:111]\n"
    "v_lshlrev_b32 v56, 16, v16\n"
    "v_and_b32 v57, s76, v16\n"
    "v_lshlrev_b32 v58, 16, v17\n"
    "v_and_b32 v59, s76, v17\n"
    "v_lshlrev_b32 v60, 16, v18\n"
    "v_and_b32 v61, s76, v18\n"
    "v_lshlrev_b32 v62, 16, v19\n"
    "v_and_b32 v63, s76, v19\n"
    "v_pk_add_f32 v[104:105], v[104:105], v[56:57]\n"
    "v_pk_fma_f32 v[108:109], v[56:57], v[56:57], v[108:109]\n"
    "v_pk_add_f32 v[106:107], v[106:107], v[58:59]\n"
    "v_pk_fma_f32 v[110:111], v[58:59], v[58:59], v[110:111]\n"
    "v_pk_add_f32 v[104:105], v[104:105], v[60:61]\n"
    "v_pk_fma_f32 v[108:109], v[60:61], v[60:61], v[108:109]\n"
    "v_pk_add_f32 v[106:107], v[106:107], v[62:63]\n"
    "v_pk_fma_f32 v[110:111], v[62:63], v[62:63], v[110:111]\n"
    "v_lshlrev_b32 v64, 16, v20\n"
    "v_and_b32 v65, s76, v20\n"
    "v_lshlrev_b32 v66, 16, v21\n"
    "v_and_b32 v67, s76, v21\n"
    "v_lshlrev_b32 v68, 16, v22\n"
    "v_and_b32 v69, s76, v22\n"
    "v_lshlrev_b32 v70, 16, v23\n"
    "v_and_b32 v71, s76, v23\n"
    "v_pk_add_f32 v[104:105], v[104:105], v[64:65]\n"
    "v_pk_fma_f32 v[108:109], v[64:65], v[64:65], v[108:109]\n"
    "v_pk_add_f32 v[106:107], v[106:107], v[66:67]\n"
    "v_pk_fma_f32 v[110:111], v[66:67], v[66:67], v[110:111]\n"
    "v_pk_add_f32 v[104:105], v[104:105], v[68:69]\n"
    "v_pk_fma_f32 v[108:109], v[68:69], v[68:69], v[108:109]\n"
    "v_pk_add_f32 v[106:107], v[106:107], v[70:71]\n"
    "v_pk_fma_f32 v[110:111], v[70:71], v[70:71], v[110:111]\n"
    "v_lshlrev_b32 v72, 16, v24\n"
    "v_and_b32 v73, s76, v24\n"
    "v_lshlrev_b32 v74, 16, v25\n"
    "v_and_b32 v75, s76, v25\n"
    "v_lshlrev_b32 v76, 16, v26\n"
    "v_and_b32 v77, s76, v26\n"
    "v_lshlrev_b32 v78, 16, v27\n"
    "v_and_b32 v79, s76, v27\n"
    "v_pk_add_f32 v[104:105], v[104:105], v[72:73]\n"
    "v_pk_fma_f32 v[108:109], v[72:73], v[72:73], v[108:109]\n"
    "v_pk_add_f32 v[106:107], v[106:107], v[74:75]\n"
    "v_pk_fma_f32 v[110:111], v[74:75], v[74:75], v[110:111]\n"
    "v_pk_add_f32 v[104:105], v[104:105], v[76:77]\n"
    "v_pk_fma_f32 v[108:109], v[76:77], v[76:77], v[108:109]\n"
    "v_pk_add_f32 v[106:107], v[106:107], v[78:79]\n"
    "v_pk_fma_f32 v[110:111], v[78:79], v[78:79], v[110:111]\n"
    "v_lshlrev_b32 v80, 16, v28\n"
    "v_and_b32 v81, s76, v28\n"
    "v_lshlrev_b32 v82, 16, v29\n"
    "v_and_b32 v83, s76, v29\n"
    "v_lshlrev_b32 v84, 16, v30\n"
    "v_and_b32 v85, s76, v30\n"
    "v_lshlrev_b32 v86, 16, v31\n"
    "v_and_b32 v87, s76, v31\n"
    "v_pk_add_f32 v[104:105], v[104:105], v[80:81]\n"
    "v_pk_fma_f32 v[108:109], v[80:81], v[80:81], v[108:109]\n"
    "v_pk_add_f32 v[106:107], v[106:107], v[82:83]\n"
    "v_pk_fma_f32 v[110:111], v[82:83], v[82:83], v[110:111]\n"
    "v_pk_add_f32 v[104:105], v[104:105], v[84:85]\n"
    "v_pk_fma_f32 v[108:109], v[84:85], v[84:85], v[108:109]\n"
    "v_pk_add_f32 v[106:107], v[106:107], v[86:87]\n"
    "v_pk_fma_f32 v[110:111], v[86:87], v[86:87], v[110:111]\n"
    "v_lshlrev_b32 v88, 16, v32\n"
    "v_and_b32 v89, s76, v32\n"
    "v_lshlrev_b32 v90, 16, v33\n"
    "v_and_b32 v91, s76, v33\n"
    "v_lshlrev_b32 v92, 16, v34\n"
    "v_and_b32 v93, s76, v34\n"
    "v_lshlrev_b32 v94, 16, v35\n"
    "v_and_b32 v95, s76, v35\n"
    "v_pk_add_f32 v[104:105], v[104:105], v[88:89]\n"
    "v_pk_fma_f32 v[108:109], v[88:89], v[88:89], v[108:109]\n"
    "v_pk_add_f32 v[106:107], v[106:107], v[90:91]\n"
    "v_pk_fma_f32 v[110:111], v[90:91], v[90:91], v[110:111]\n"
    "v_pk_add_f32 v[104:105], v[104:105], v[92:93]\n"
    "v_pk_fma_f32 v[108:109], v[92:93], v[92:93], v[108:109]\n"
    "v_pk_add_f32 v[106:107], v[106:107], v[94:95]\n"
    "v_pk_fma_f32 v[110:111], v[94:95], v[94:95], v[110:111]\n"
    "v_lshlrev_b32 v96, 16, v36\n"
    "v_and_b32 v97, s76, v36\n"
    "v_lshlrev_b32 v98, 16, v37\n"
    "v_and_b32 v99, s76, v37\n"
    "v_lshlrev_b32 v100, 16, v38\n"
    "v_and_b32 v101, s76, v38\n"
    "v_lshlrev_b32 v102, 16, v39\n"
    "v_and_b32 v103, s76, v39\n"
    "v_pk_add_f32 v[104:105], v[104:105], v[96:97]\n"
    "v_pk_fma_f32 v[108:109], v[96:97], v[96:97], v[108:109]\n"
    "v_pk_add_f32 v[106:107], v[106:107], v[98:99]\n"
    "v_pk_fma_f32 v[110:111], v[98:99], v[98:99], v[110:111]\n"
    "v_pk_add_f32 v[104:105], v[104:105], v[100:101]\n"
    "v_pk_fma_f32 v[108:109], v[100:101], v[100:101], v[108:109]\n"
    "v_pk_add_f32 v[106:107], v[106:107], v[102:103]\n"
    "v_pk_fma_f32 v[110:111], v[102:103], v[102:103], v[110:111]\n"
    "v_pk_add_f32 v[104:105], v[104:105], v[106:107]\n"
    "v_pk_add_f32 v[108:109], v[108:109], v[110:111]\n"
    "v_add_f32 v104, v104, v105\n"
    "v_add_f32 v108, v108, v109\n"
    "s_nop 0\n"
    "v_add_f32_dpp v104, v104, v104 quad_perm:[1,0,3,2] row_mask:0xf bank_mask:0xf\n"
    "v_add_f32_dpp v108, v108, v108 quad_perm:[1,0,3,2] row_mask:0xf bank_mask:0xf\n"
    "s_nop 0\n"
    "v_add_f32_dpp v104, v104, v104 quad_perm:[2,3,0,1] row_mask:0xf bank_mask:0xf\n"
    "v_add_f32_dpp v108, v108, v108 quad_perm:[2,3,0,1] row_mask:0xf bank_mask:0xf\n"
    "s_nop 0\n"
    "v_add_f32_dpp v104, v104, v104 row_half_mirror row_mask:0xf bank_mask:0xf\n"
    "v_add_f32_dpp v108, v108, v108 row_half_mirror row_mask:0xf bank_mask:0xf\n"
    "v_mul_f32 v104, 0x3b000000, v104\n"
    "v_mul_f32 v108, 0x3b000000, v108\n"
    "v_fma_f32 v108, -v104, v104, v108\n"
    "v_max_f32 v108, 0, v108\n"
    "v_cvt_pk_bf16_f32 v104, v104, v108\n"
    "v_and_b32 v108, s76, v104\n"
    "v_lshlrev_b32 v104, 16, v104\n"
    "v_add_f32 v108, 0x3727c5ac, v108\n"
    "v_cvt_pk_bf16_f32 v108, v108, v108\n"
    "v_and_b32 v108, s76, v108\n"
    "v_sqrt_f32 v108, v108\n"
    "s_nop 0\n"
    "v_cvt_pk_bf16_f32 v108, v108, v108\n"
    "v_and_b32 v108, s76, v108\n"
    "v_rcp_f32 v108, v108\n"
    "s_nop 0\n"
    "v_mul_f32_e64 v104, -v104, v108\n"
    "v_pk_fma_f32 v[40:41], v[40:41], v[108:109], v[104:105] op_sel_hi:[1,0,0]\n"
    "v_pk_fma_f32 v[42:43], v[42:43], v[108:109], v[104:105] op_sel_hi:[1,0,0]\n"
    "v_pk_fma_f32 v[44:45], v[44:45], v[108:109], v[104:105] op_sel_hi:[1,0,0]\n"
    "v_pk_fma_f32 v[46:47], v[46:47], v[108:109], v[104:105] op_sel_hi:[1,0,0]\n"
    "v_cvt_pk_bf16_f32 v8, v40, v41\n"
    "v_cvt_pk_bf16_f32 v9, v42, v43\n"
    "v_cvt_pk_bf16_f32 v10, v44, v45\n"
    "v_cvt_pk_bf16_f32 v11, v46, v47\n"
    "ds_write_b128 v6, v[8:11] offset:0\n"
    "v_pk_fma_f32 v[48:49], v[48:49], v[108:109], v[104:105] op_sel_hi:[1,0,0]\n"
    "v_pk_fma_f32 v[50:51], v[50:51], v[108:109], v[104:105] op_sel_hi:[1,0,0]\n"
    "v_pk_fma_f32 v[52:53], v[52:53], v[108:109], v[104:105] op_sel_hi:[1,0,0]\n"
    "v_pk_fma_f32 v[54:55], v[54:55], v[108:109], v[104:105] op_sel_hi:[1,0,0]\n"
    "v_cvt_pk_bf16_f32 v12, v48, v49\n"
    "v_cvt_pk_bf16_f32 v13, v50, v51\n"
    "v_cvt_pk_bf16_f32 v14, v52, v53\n"
    "v_cvt_pk_bf16_f32 v15, v54, v55\n"
    "ds_write_b128 v6, v[12:15] offset:8192\n"
    "v_pk_fma_f32 v[56:57], v[56:57], v[108:109], v[104:105] op_sel_hi:[1,0,0]\n"
    "v_pk_fma_f32 v[58:59], v[58:59], v[108:109], v[104:105] op_sel_hi:[1,0,0]\n"
    "v_pk_fma_f32 v[60:61], v[60:61], v[108:109], v[104:105] op_sel_hi:[1,0,0]\n"
    "v_pk_fma_f32 v[62:63], v[62:63], v[108:109], v[104:105] op_sel_hi:[1,0,0]\n"
    "v_cvt_pk_bf16_f32 v16, v56, v57\n"
    "v_cvt_pk_bf16_f32 v17, v58, v59\n"
    "v_cvt_pk_bf16_f32 v18, v60, v61\n"
    "v_cvt_pk_bf16_f32 v19, v62, v63\n"
    "ds_write_b128 v6, v[16:19] offset:16384\n"
    "v_pk_fma_f32 v[64:65], v[64:65], v[108:109], v[104:105] op_sel_hi:[1,0,0]\n"
    "v_pk_fma_f32 v[66:67], v[66:67], v[108:109], v[104:105] op_sel_hi:[1,0,0]\n"
    "v_pk_fma_f32 v[68:69], v[68:69], v[108:109], v[104:105] op_sel_hi:[1,0,0]\n"
    "v_pk_fma_f32 v[70:71], v[70:71], v[108:109], v[104:105] op_sel_hi:[1,0,0]\n"
    "v_cvt_pk_bf16_f32 v20, v64, v65\n"
    "v_cvt_pk_bf16_f32 v21, v66, v67\n"
    "v_cvt_pk_bf16_f32 v22, v68, v69\n"
    "v_cvt_pk_bf16_f32 v23, v70, v71\n"
    "ds_write_b128 v6, v[20:23] offset:24576\n"
    "v_pk_fma_f32 v[72:73], v[72:73], v[108:109], v[104:105] op_sel_hi:[1,0,0]\n"
    "v_pk_fma_f32 v[74:75], v[74:75], v[108:109], v[104:105] op_sel_hi:[1,0,0]\n"
    "v_pk_fma_f32 v[76:77], v[76:77], v[108:109], v[104:105] op_sel_hi:[1,0,0]\n"
    "v_pk_fma_f32 v[78:79], v[78:79], v[108:109], v[104:105] op_sel_hi:[1,0,0]\n"
    "v_cvt_pk_bf16_f32 v24, v72, v73\n"
    "v_cvt_pk_bf16_f32 v25, v74, v75\n"
    "v_cvt_pk_bf16_f32 v26, v76, v77\n"
    "v_cvt_pk_bf16_f32 v27, v78, v79\n"
    "ds_write_b128 v6, v[24:27] offset:32768\n"
    "v_pk_fma_f32 v[80:81], v[80:81], v[108:109], v[104:105] op_sel_hi:[1,0,0]\n"
    "v_pk_fma_f32 v[82:83], v[82:83], v[108:109], v[104:105] op_sel_hi:[1,0,0]\n"
    "v_pk_fma_f32 v[84:85], v[84:85], v[108:109], v[104:105] op_sel_hi:[1,0,0]\n"
    "v_pk_fma_f32 v[86:87], v[86:87], v[108:109], v[104:105] op_sel_hi:[1,0,0]\n"
    "v_cvt_pk_bf16_f32 v28, v80, v81\n"
    "v_cvt_pk_bf16_f32 v29, v82, v83\n"
    "v_cvt_pk_bf16_f32 v30, v84, v85\n"
    "v_cvt_pk_bf16_f32 v31, v86, v87\n"
    "ds_write_b128 v6, v[28:31] offset:40960\n"
    "v_pk_fma_f32 v[88:89], v[88:89], v[108:109], v[104:105] op_sel_hi:[1,0,0]\n"
    "v_pk_fma_f32 v[90:91], v[90:91], v[108:109], v[104:105] op_sel_hi:[1,0,0]\n"
    "v_pk_fma_f32 v[92:93], v[92:93], v[108:109], v[104:105] op_sel_hi:[1,0,0]\n"
    "v_pk_fma_f32 v[94:95], v[94:95], v[108:109], v[104:105] op_sel_hi:[1,0,0]\n"
    "v_cvt_pk_bf16_f32 v32, v88, v89\n"
    "v_cvt_pk_bf16_f32 v33, v90, v91\n"
    "v_cvt_pk_bf16_f32 v34, v92, v93\n"
    "v_cvt_pk_bf16_f32 v35, v94, v95\n"
    "ds_write_b128 v6, v[32:35] offset:49152\n"
    "v_pk_fma_f32 v[96:97], v[96:97], v[108:109], v[104:105] op_sel_hi:[1,0,0]\n"
    "v_pk_fma_f32 v[98:99], v[98:99], v[108:109], v[104:105] op_sel_hi:[1,0,0]\n"
    "v_pk_fma_f32 v[100:101], v[100:101], v[108:109], v[104:105] op_sel_hi:[1,0,0]\n"
    "v_pk_fma_f32 v[102:103], v[102:103], v[108:109], v[104:105] op_sel_hi:[1,0,0]\n"
    "v_cvt_pk_bf16_f32 v36, v96, v97\n"
    "v_cvt_pk_bf16_f32 v37, v98, v99\n"
    "v_cvt_pk_bf16_f32 v38, v100, v101\n"
    "v_cvt_pk_bf16_f32 v39, v102, v103\n"
    "ds_write_b128 v6, v[36:39] offset:57344\n"
    "v_mov_b32 v255, 1\n"
    "ds_write_b8 v5, v255 offset:80\n"
    "s_waitcnt vmcnt(0)\n"
    "ds_read_b64_tr_b16 v[40:41], v228 offset:0\n"
    "ds_read_b64_tr_b16 v[42:43], v228 offset:512\n"
    "ds_read_b64_tr_b16 v[44:45], v228 offset:1024\n"
    "ds_read_b64_tr_b16 v[46:47], v228 offset:1536\n"
    "ds_read_b64_tr_b16 v[48:49], v228 offset:2048\n"
    "ds_read_b64_tr_b16 v[50:51], v228 offset:2560\n"
    "ds_read_b64_tr_b16 v[52:53], v228 offset:3072\n"
    "ds_read_b64_tr_b16 v[54:55], v228 offset:3584\n"
    "s_mov_b32 s96, 131072\n"
    ".Lsp_1_k:\n"
    "ds_read_b32 v255, v7 offset:80\n"
    "s_waitcnt lgkmcnt(0)\n"
    "v_readfirstlane_b32 s94, v255\n"
    "s_and_b32 s94, s94, 0x0101\n"
    "s_cmp_eq_u32 s94, 0x0101\n"
    "s_cbranch_scc1 .Lspd_2_k\n"
    "s_sub_u32 s96, s96, 1\n"
    "s_cbranch_scc0 .Lsp_1_k\n"
    ".Lspd_2_k:\n"
    "ds_read_b128 v[56:59], v226 offset:0\n"
    "ds_read_b128 v[60:63], v227 offset:4096\n"
    "ds_read_b128 v[64:67], v226 offset:8192\n"
    "ds_read_b128 v[68:71], v227 offset:12288\n"
    "s_bitcmp1_b32 s74, 0\n"
    "s_cbranch_scc1 .L_vproj_k\n"
    "s_waitcnt lgkmcnt(3)\n"
    "v_mfma_f32_16x16x32_bf16 v[128:131], v[40:43], v[56:59], 0\n"
    "s_waitcnt lgkmcnt(2)\n"
    "v_mfma_f32_16x16x32_bf16 v[128:131], v[44:47], v[60:63], v[128:131]\n"
    "s_waitcnt lgkmcnt(1)\n"
    "v_mfma_f32_16x16x32_bf16 v[128:131], v[48:51], v[64:67], v[128:131]\n"
    "s_waitcnt lgkmcnt(0)\n"
    "v_mfma_f32_16x16x32_bf16 v[128:131], v[52:55], v[68:71], v[128:131]\n"
    "s_branch .L_kvp_k\n"
    ".L_vproj_k:\n"
    "s_waitcnt lgkmcnt(3)\n"
    "v_mfma_f32_16x16x32_bf16 v[128:131], v[56:59], v[40:43], 0\n"
    "s_waitcnt lgkmcnt(2)\n"
    "v_mfma_f32_16x16x32_bf16 v[128:131], v[60:63], v[44:47], v[128:131]\n"
    "s_waitcnt lgkmcnt(1)\n"
    "v_mfma_f32_16x16x32_bf16 v[128:131], v[64:67], v[48:51], v[128:131]\n"
    "s_waitcnt lgkmcnt(0)\n"
    "v_mfma_f32_16x16x32_bf16 v[128:131], v[68:71], v[52:55], v[128:131]\n"
    ".L_kvp_k:\n"
    "s_cmp_eq_u32 s75, 2\n"
    "s_cbranch_scc1 .L_kvsum_k\n"
    "s_nop 5\n"
    "ds_write_b128 v229, v[128:131]\n"
    "v_mov_b32 v255, 1\n"
    "ds_write_b8 v5, v255 offset:4\n"
    "s_branch .L_bar1_k\n"
    ".L_kvsum_k:\n"
    "v_and_b32 v116, 15, v1\n"
    "v_lshlrev_b32 v117, 5, v116\n"
    "v_lshrrev_b32 v118, 4, v1\n"
    "v_lshl_or_b32 v117, v118, 3, v117\n"
    "v_lshlrev_b32 v116, 4, v116\n"
    "v_lshrrev_b32 v118, 5, v1\n"
    "v_lshl_or_b32 v116, v118, 9, v116\n"
    "v_bfe_u32 v118, v1, 4, 1\n"
    "v_lshl_or_b32 v116, v118, 3, v116\n"
    "s_lshl_b32 s76, s83, 9\n"
    "s_add_u32 s76, s76, 65536\n"
    "s_and_b32 s77, s83, 1\n"
    "s_lshl_b32 s77, s77, 8\n"
    "s_lshr_b32 s83, s83, 1\n"
    "s_lshl_b32 s83, s83, 10\n"
    "s_add_u32 s83, s83, s77\n"
    "s_add_u32 s83, s83, 131072\n"
    "s_mov_b32 s96, 131072\n"
    ".Lsp_3_k:\n"
    "ds_read_b32 v255, v7 offset:4\n"
    "ds_read_b128 v[40:43], v2\n"
    "ds_read_b128 v[44:47], v2 offset:1024\n"
    "ds_read_b128 v[48:51], v2 offset:3072\n"
    "s_waitcnt lgkmcnt(0)\n"
    "v_readfirstlane_b32 s94, v255\n"
    "s_cmp_eq_u32 s94, 0x01000101\n"
    "s_cbranch_scc1 .Lspd_4_k\n"
    "s_sub_u32 s96, s96, 1\n"
    "s_cbranch_scc0 .Lsp_3_k\n"
    ".Lspd_4_k:\n"
    "v_pk_add_f32 v[40:41], v[40:41], v[44:45]\n"
    "v_pk_add_f32 v[42:43], v[42:43], v[46:47]\n"
    "v_pk_add_f32 v[128:129], v[128:129], v[48:49]\n"
    "v_pk_add_f32 v[130:131], v[130:131], v[50:51]\n"
    "v_pk_add_f32 v[128:129], v[128:129], v[40:41]\n"
    "v_pk_add_f32 v[130:131], v[130:131], v[42:43]\n"
    "v_cvt_pk_bf16_f32 v114, v128, v129\n"
    "v_cvt_pk_bf16_f32 v115, v130, v131\n"
    "s_bitcmp1_b32 s74, 0\n"
    "s_cbranch_scc1 .L_vst_k\n"
    "buffer_store_dwordx2 v[114:115], v117, s[48:51], s76 offen sc1\n"
    "v_mul_f32 v121, v128, v128\n"
    "v_fmac_f32 v121, v129, v129\n"
    "v_fmac_f32 v121, v130, v130\n"
    "v_fmac_f32 v121, v131, v131\n"
    "v_mov_b32 v112, v121\n"
    "s_nop 1\n"
    "v_permlane16_swap_b32 v121, v112\n"
    "v_add_f32 v121, v121, v112\n"
    "v_mov_b32 v112, v121\n"
    "s_nop 1\n"
    "v_permlane32_swap_b32 v121, v112\n"
    "v_add_f32 v121, v121, v112\n"
    "s_nop 1\n"
    "v_max_f32_dpp v121, v121, v121 row_ror:8 row_mask:0xf bank_mask:0xf\n"
    "s_nop 1\n"
    "v_max_f32_dpp v121, v121, v121 row_ror:4 row_mask:0xf bank_mask:0xf\n"
    "s_nop 1\n"
    "v_max_f32_dpp v121, v121, v121 row_ror:2 row_mask:0xf bank_mask:0xf\n"
    "s_nop 1\n"
    "v_max_f32_dpp v121, v121, v121 row_ror:1 row_mask:0xf bank_mask:0xf\n"
    "s_branch .L_kvdone_k\n"
    ".L_vst_k:\n"
    "buffer_store_dwordx2 v[114:115], v116, s[48:51], s83 offen sc1\n"
    "v_max3_f32 v121, |v128|, |v129|, |v130|\n"
    "v_max_f32_e64 v121, |v131|, v121\n"
    "s_mov_b32 s76, 0x4e800000\n"
    "v_cmp_ge_f32 vcc, s76, v121\n"
    "s_cmp_eq_u64 vcc, -1\n"
    "s_cselect_b32 s76, 0, 0x7f800000\n"
    "v_mov_b32 v121, s76\n"
    ".L_kvdone_k:\n"
    ".L_bar1_k:\n"
    "s_waitcnt lgkmcnt(0)\n"
    "s_barrier\n"
    "ds_read_b64_tr_b16 v[8:9], v251 offset:32768\n"
    "ds_read_b64_tr_b16 v[10:11], v251 offset:34816\n"
    "ds_read_b128 v[12:15], v249 offset:0\n"
    "ds_read_b128 v[16:19], v249 offset:256\n"
    "ds_read_b128 v[20:23], v249 offset:512\n"
    "ds_read_b128 v[24:27], v249 offset:768\n"
    "ds_read_b64_tr_b16 v[28:29], v251 offset:36864\n"
    "ds_read_b64_tr_b16 v[30:31], v251 offset:38912\n"
    "ds_read_b128 v[32:35], v250 offset:4096\n"
    "ds_read_b128 v[36:39], v250 offset:4352\n"
    "ds_read_b128 v[40:43], v250 offset:4608\n"
    "ds_read_b128 v[44:47], v250 offset:4864\n"
    "s_waitcnt lgkmcnt(6)\n"
    "v_mfma_f32_16x16x32_bf16 v[200:203], v[8:11], v[12:15], 0\n"
    "ds_read_b64_tr_b16 v[48:49], v251 offset:40960\n"
    "v_mfma_f32_16x16x32_bf16 v[204:207], v[8:11], v[16:19], 0\n"
    "ds_read_b64_tr_b16 v[50:51], v251 offset:43008\n"
    "v_mfma_f32_16x16x32_bf16 v[208:211], v[8:11], v[20:23], 0\n"
    "ds_read_b128 v[52:55], v249 offset:8192\n"
    "v_mfma_f32_16x16x32_bf16 v[212:215], v[8:11], v[24:27], 0\n"
    "ds_read_b128 v[56:59], v249 offset:8448\n"
    "ds_read_b128 v[60:63], v249 offset:8704\n"
    "ds_read_b128 v[64:67], v249 offset:8960\n"
    "s_waitcnt lgkmcnt(6)\n"
    "v_mfma_f32_16x16x32_bf16 v[200:203], v[28:31], v[32:35], v[200:203]\n"
    "ds_read_b64_tr_b16 v[8:9], v251 offset:45056\n"
    "v_mfma_f32_16x16x32_bf16 v[204:207], v[28:31], v[36:39], v[204:207]\n"
    "ds_read_b64_tr_b16 v[10:11], v251 offset:47104\n"
    "v_mfma_f32_16x16x32_bf16 v[208:211], v[28:31], v[40:43], v[208:211]\n"
    "ds_read_b128 v[12:15], v250 offset:12288\n"
    "v_mfma_f32_16x16x32_bf16 v[212:215], v[28:31], v[44:47], v[212:215]\n"
    "ds_read_b128 v[16:19], v250 offset:12544\n"
    "ds_read_b128 v[20:23], v250 offset:12800\n"
    "ds_read_b128 v[24:27], v250 offset:13056\n"
    "s_waitcnt lgkmcnt(6)\n"
    "v_mfma_f32_16x16x32_bf16 v[200:203], v[48:51], v[52:55], v[200:203]\n"
    "ds_read_b64_tr_b16 v[28:29], v251 offset:49152\n"
    "v_mfma_f32_16x16x32_bf16 v[204:207], v[48:51], v[56:59], v[204:207]\n"
    "ds_read_b64_tr_b16 v[30:31], v251 offset:51200\n"
    "v_mfma_f32_16x16x32_bf16 v[208:211], v[48:51], v[60:63], v[208:211]\n"
    "ds_read_b128 v[32:35], v249 offset:16384\n"
    "v_mfma_f32_16x16x32_bf16 v[212:215], v[48:51], v[64:67], v[212:215]\n"
    "ds_read_b128 v[36:39], v249 offset:16640\n"
    "ds_read_b128 v[40:43], v249 offset:16896\n"
    "ds_read_b128 v[44:47], v249 offset:17152\n"
    "s_cmp_lg_u32 s75, 2\n"
    "s_cbranch_scc1 .L_nopub_k\n"
    "v_mov_b32 v120, s70\n"
    "v_mov_b32 v122, s71\n"
    "v_mov_b32 v123, 0\n"
    "v_lshlrev_b32 v124, 12, v1\n"
    "s_lshl_b32 s77, s84, 4\n"
    "s_add_u32 s77, s77, 32768\n"
    "s_mov_b64 exec, 0xff\n"
    "s_waitcnt vmcnt(0)\n"
    "buffer_store_dwordx4 v[120:123], v124, s[48:51], s77 offen sc1\n"
    "s_mov_b64 exec, -1\n"
    ".L_nopub_k:\n"
    "s_waitcnt lgkmcnt(6)\n"
    "v_mfma_f32_16x16x32_bf16 v[200:203], v[8:11], v[12:15], v[200:203]\n"
    "ds_read_b64_tr_b16 v[48:49], v251 offset:53248\n"
    "v_mfma_f32_16x16x32_bf16 v[204:207], v[8:11], v[16:19], v[204:207]\n"
    "ds_read_b64_tr_b16 v[50:51], v251 offset:55296\n"
    "v_mfma_f32_16x16x32_bf16 v[208:211], v[8:11], v[20:23], v[208:211]\n"
    "ds_read_b128 v[52:55], v250 offset:20480\n"
    "v_mfma_f32_16x16x32_bf16 v[212:215], v[8:11], v[24:27], v[212:215]\n"
    "ds_read_b128 v[56:59], v250 offset:20736\n"
    "ds_read_b128 v[60:63], v250 offset:20992\n"
    "ds_read_b128 v[64:67], v250 offset:21248\n"
    "s_waitcnt lgkmcnt(6)\n"
    "v_mfma_f32_16x16x32_bf16 v[200:203], v[28:31], v[32:35], v[200:203]\n"
    "ds_read_b64_tr_b16 v[8:9], v251 offset:57344\n"
    "v_mfma_f32_16x16x32_bf16 v[204:207], v[28:31], v[36:39], v[204:207]\n"
    "ds_read_b64_tr_b16 v[10:11], v251 offset:59392\n"
    "v_mfma_f32_16x16x32_bf16 v[208:211], v[28:31], v[40:43], v[208:211]\n"
    "ds_read_b128 v[12:15], v249 offset:24576\n"
    "v_mfma_f32_16x16x32_bf16 v[212:215], v[28:31], v[44:47], v[212:215]\n"
    "ds_read_b128 v[16:19], v249 offset:24832\n"
    "ds_read_b128 v[20:23], v249 offset:25088\n"
    "ds_read_b128 v[24:27], v249 offset:25344\n"
    "s_waitcnt lgkmcnt(6)\n"
    "v_mfma_f32_16x16x32_bf16 v[200:203], v[48:51], v[52:55], v[200:203]\n"
    "ds_read_b64_tr_b16 v[28:29], v251 offset:61440\n"
    "v_mfma_f32_16x16x32_bf16 v[204:207], v[48:51], v[56:59], v[204:207]\n"
    "ds_read_b64_tr_b16 v[30:31], v251 offset:63488\n"
    "v_mfma_f32_16x16x32_bf16 v[208:211], v[48:51], v[60:63], v[208:211]\n"
    "ds_read_b128 v[32:35], v250 offset:28672\n"
    "v_mfma_f32_16x16x32_bf16 v[212:215], v[48:51], v[64:67], v[212:215]\n"
    "ds_read_b128 v[36:39], v250 offset:28928\n"
    "ds_read_b128 v[40:43], v250 offset:29184\n"
    "ds_read_b128 v[44:47], v250 offset:29440\n"
    "s_waitcnt lgkmcnt(6)\n"
    "v_mfma_f32_16x16x32_bf16 v[200:203], v[8:11], v[12:15], v[200:203]\n"
    "ds_read_b64_tr_b16 v[48:49], v251 offset:0\n"
    "v_mfma_f32_16x16x32_bf16 v[204:207], v[8:11], v[16:19], v[204:207]\n"
    "ds_read_b64_tr_b16 v[50:51], v251 offset:2048\n"
    "v_mfma_f32_16x16x32_bf16 v[208:211], v[8:11], v[20:23], v[208:211]\n"
    "ds_read_b128 v[52:55], v249 offset:32768\n"
    "v_mfma_f32_16x16x32_bf16 v[212:215], v[8:11], v[24:27], v[212:215]\n"
    "ds_read_b128 v[56:59], v249 offset:33024\n"
    "ds_read_b128 v[60:63], v249 offset:33280\n"
    "ds_read_b128 v[64:67], v249 offset:33536\n"
    "s_waitcnt lgkmcnt(6)\n"
    "v_mfma_f32_16x16x32_bf16 v[200:203], v[28:31], v[32:35], v[200:203]\n"
    "ds_read_b64_tr_b16 v[8:9], v251 offset:4096\n"
    "v_mfma_f32_16x16x32_bf16 v[204:207], v[28:31], v[36:39], v[204:207]\n"
    "ds_read_b64_tr_b16 v[10:11], v251 offset:6144\n"
    "v_mfma_f32_16x16x32_bf16 v[208:211], v[28:31], v[40:43], v[208:211]\n"
    "ds_read_b128 v[12:15], v250 offset:36864\n"
    "v_mfma_f32_16x16x32_bf16 v[212:215], v[28:31], v[44:47], v[212:215]\n"
    "ds_read_b128 v[16:19], v250 offset:37120\n"
    "ds_read_b128 v[20:23], v250 offset:37376\n"
    "ds_read_b128 v[24:27], v250 offset:37632\n"
    "v_mov_b32 v255, 1\n"
    "ds_write_b8 v5, v255 offset:16\n"
    "s_waitcnt lgkmcnt(6)\n"
    "v_mfma_f32_16x16x32_bf16 v[200:203], v[48:51], v[52:55], v[200:203]\n"
    "ds_read_b64_tr_b16 v[28:29], v251 offset:8192\n"
    "v_mfma_f32_16x16x32_bf16 v[204:207], v[48:51], v[56:59], v[204:207]\n"
    "ds_read_b64_tr_b16 v[30:31], v251 offset:10240\n"
    "v_mfma_f32_16x16x32_bf16 v[208:211], v[48:51], v[60:63], v[208:211]\n"
    "ds_read_b128 v[32:35], v249 offset:40960\n"
    "v_mfma_f32_16x16x32_bf16 v[212:215], v[48:51], v[64:67], v[212:215]\n"
    "ds_read_b128 v[36:39], v249 offset:41216\n"
    "ds_read_b128 v[40:43], v249 offset:41472\n"
    "ds_read_b128 v[44:47], v249 offset:41728\n"
    "s_waitcnt lgkmcnt(6)\n"
    "v_mfma_f32_16x16x32_bf16 v[200:203], v[8:11], v[12:15], v[200:203]\n"
    "ds_read_b64_tr_b16 v[48:49], v251 offset:12288\n"
    "v_mfma_f32_16x16x32_bf16 v[204:207], v[8:11], v[16:19], v[204:207]\n"
    "ds_read_b64_tr_b16 v[50:51], v251 offset:14336\n"
    "v_mfma_f32_16x16x32_bf16 v[208:211], v[8:11], v[20:23], v[208:211]\n"
    "ds_read_b128 v[52:55], v250 offset:45056\n"
    "v_mfma_f32_16x16x32_bf16 v[212:215], v[8:11], v[24:27], v[212:215]\n"
    "ds_read_b128 v[56:59], v250 offset:45312\n"
    "ds_read_b128 v[60:63], v250 offset:45568\n"
    "ds_read_b128 v[64:67], v250 offset:45824\n"
    "s_waitcnt lgkmcnt(6)\n"
    "v_mfma_f32_16x16x32_bf16 v[200:203], v[28:31], v[32:35], v[200:203]\n"
    "ds_read_b64_tr_b16 v[8:9], v251 offset:16384\n"
    "v_mfma_f32_16x16x32_bf16 v[204:207], v[28:31], v[36:39], v[204:207]\n"
    "ds_read_b64_tr_b16 v[10:11], v251 offset:18432\n"
    "v_mfma_f32_16x16x32_bf16 v[208:211], v[28:31], v[40:43], v[208:211]\n"
    "ds_read_b128 v[12:15], v249 offset:49152\n"
    "v_mfma_f32_16x16x32_bf16 v[212:215], v[28:31], v[44:47], v[212:215]\n"
    "ds_read_b128 v[16:19], v249 offset:49408\n"
    "ds_read_b128 v[20:23], v249 offset:49664\n"
    "ds_read_b128 v[24:27], v249 offset:49920\n"
    "s_waitcnt lgkmcnt(6)\n"
    "v_mfma_f32_16x16x32_bf16 v[200:203], v[48:51], v[52:55], v[200:203]\n"
    "ds_read_b64_tr_b16 v[28:29], v251 offset:20480\n"
    "v_mfma_f32_16x16x32_bf16 v[204:207], v[48:51], v[56:59], v[204:207]\n"
    "ds_read_b64_tr_b16 v[30:31], v251 offset:22528\n"
    "v_mfma_f32_16x16x32_bf16 v[208:211], v[48:51], v[60:63], v[208:211]\n"
    "ds_read_b128 v[32:35], v250 offset:53248\n"
    "v_mfma_f32_16x16x32_bf16 v[212:215], v[48:51], v[64:67], v[212:215]\n"
    "ds_read_b128 v[36:39], v250 offset:53504\n"
    "ds_read_b128 v[40:43], v250 offset:53760\n"
    "ds_read_b128 v[44:47], v250 offset:54016\n"
    "s_waitcnt lgkmcnt(6)\n"
    "v_mfma_f32_16x16x32_bf16 v[200:203], v[8:11], v[12:15], v[200:203]\n"
    "ds_read_b64_tr_b16 v[48:49], v251 offset:24576\n"
    "v_mfma_f32_16x16x32_bf16 v[204:207], v[8:11], v[16:19], v[204:207]\n"
    "ds_read_b64_tr_b16 v[50:51], v251 offset:26624\n"
    "v_mfma_f32_16x16x32_bf16 v[208:211], v[8:11], v[20:23], v[208:211]\n"
    "ds_read_b128 v[52:55], v249 offset:57344\n"
    "v_mfma_f32_16x16x32_bf16 v[212:215], v[8:11], v[24:27], v[212:215]\n"
    "ds_read_b128 v[56:59], v249 offset:57600\n"
    "ds_read_b128 v[60:63], v249 offset:57856\n"
    "ds_read_b128 v[64:67], v249 offset:58112\n"
    "buffer_load_dwordx4 v[216:219], v254, s[48:51], s90 offen sc1\n"
    "s_waitcnt lgkmcnt(6)\n"
    "v_mfma_f32_16x16x32_bf16 v[200:203], v[28:31], v[32:35], v[200:203]\n"
    "ds_read_b64_tr_b16 v[8:9], v251 offset:28672\n"
    "v_mfma_f32_16x16x32_bf16 v[204:207], v[28:31], v[36:39], v[204:207]\n"
    "ds_read_b64_tr_b16 v[10:11], v251 offset:30720\n"
    "v_mfma_f32_16x16x32_bf16 v[208:211], v[28:31], v[40:43], v[208:211]\n"
    "ds_read_b128 v[12:15], v250 offset:61440\n"
    "v_mfma_f32_16x16x32_bf16 v[212:215], v[28:31], v[44:47], v[212:215]\n"
    "ds_read_b128 v[16:19], v250 offset:61696\n"
    "ds_read_b128 v[20:23], v250 offset:61952\n"
    "ds_read_b128 v[24:27], v250 offset:62208\n"
    "s_waitcnt lgkmcnt(6)\n"
    "v_mfma_f32_16x16x32_bf16 v[200:203], v[48:51], v[52:55], v[200:203]\n"
    "v_mfma_f32_16x16x32_bf16 v[204:207], v[48:51], v[56:59], v[204:207]\n"
    "v_mfma_f32_16x16x32_bf16 v[208:211], v[48:51], v[60:63], v[208:211]\n"
    "v_mfma_f32_16x16x32_bf16 v[212:215], v[48:51], v[64:67], v[212:215]\n"
    "s_waitcnt lgkmcnt(0)\n"
    "v_mfma_f32_16x16x32_bf16 v[200:203], v[8:11], v[12:15], v[200:203]\n"
    "v_mfma_f32_16x16x32_bf16 v[204:207], v[8:11], v[16:19], v[204:207]\n"
    "v_mfma_f32_16x16x32_bf16 v[208:211], v[8:11], v[20:23], v[208:211]\n"
    "v_mfma_f32_16x16x32_bf16 v[212:215], v[8:11], v[24:27], v[212:215]\n"
    "s_mov_b32 s76, 0x3eb8aa3b\n"
    "s_nop 3\n"
    "v_mul_f32 v200, s76, v200\n"
    "v_mul_f32 v201, s76, v201\n"
    "v_mul_f32 v202, s76, v202\n"
    "v_mul_f32 v203, s76, v203\n"
    "v_cvt_pk_bf16_f32 v224, v200, v201\n"
    "v_cvt_pk_bf16_f32 v225, v202, v203\n"
    "ds_write_b64 v248, v[224:225] offset:0\n"
    "v_mul_f32 v204, s76, v204\n"
    "v_mul_f32 v205, s76, v205\n"
    "v_mul_f32 v206, s76, v206\n"
    "v_mul_f32 v207, s76, v207\n"
    "v_cvt_pk_bf16_f32 v226, v204, v205\n"
    "v_cvt_pk_bf16_f32 v227, v206, v207\n"
    "ds_write_b64 v248, v[226:227] offset:1024\n"
    "v_mul_f32 v208, s76, v208\n"
    "v_mul_f32 v209, s76, v209\n"
    "v_mul_f32 v210, s76, v210\n"
    "v_mul_f32 v211, s76, v211\n"
    "v_cvt_pk_bf16_f32 v228, v208, v209\n"
    "v_cvt_pk_bf16_f32 v229, v210, v211\n"
    "ds_write_b64 v248, v[228:229] offset:2048\n"
    "v_mul_f32 v212, s76, v212\n"
    "v_mul_f32 v213, s76, v213\n"
    "v_mul_f32 v214, s76, v214\n"
    "v_mul_f32 v215, s76, v215\n"
    "v_cvt_pk_bf16_f32 v230, v212, v213\n"
    "v_cvt_pk_bf16_f32 v231, v214, v215\n"
    "ds_write_b64 v248, v[230:231] offset:3072\n"
    "v_mov_b32 v255, 1\n"
    "ds_write_b8 v5, v255 offset:24\n"
    "s_mov_b32 s96, 8192\n"
    ".Lsp_5_k:\n"
    "s_waitcnt vmcnt(0)\n"
    "v_cmp_eq_u32 vcc, s70, v216\n"
    "v_cmp_eq_u32 s[88:89], s71, v218\n"
    "s_and_b64 vcc, vcc, s[88:89]\n"
    "s_cmp_eq_u64 vcc, -1\n"
    "s_cbranch_scc1 .Lspd_6_k\n"
    "buffer_load_dwordx4 v[216:219], v254, s[48:51], s90 offen sc1\n"
    "s_sub_u32 s96, s96, 1\n"
    "s_cbranch_scc0 .Lsp_5_k\n"
    "s_waitcnt vmcnt(0)\n"
    ".Lspd_6_k:\n"
    "buffer_load_dwordx4 v[8:11], v253, s[48:51], s91 offen offset:0 sc1\n"
    "buffer_load_dwordx4 v[72:75], v254, s[48:51], s92 offen offset:0 sc1\n"
    "buffer_load_dwordx4 v[12:15], v253, s[48:51], s91 offen offset:1024 sc1\n"
    "buffer_load_dwordx4 v[76:79], v254, s[48:51], s92 offen offset:1024 sc1\n"
    "buffer_load_dwordx4 v[16:19], v253, s[48:51], s91 offen offset:2048 sc1\n"
    "buffer_load_dwordx4 v[80:83], v254, s[48:51], s92 offen offset:2048 sc1\n"
    "buffer_load_dwordx4 v[20:23], v253, s[48:51], s91 offen offset:3072 sc1\n"
    "buffer_load_dwordx4 v[84:87], v254, s[48:51], s92 offen offset:3072 sc1\n"
    "s_add_u32 s93, s93, 4096\n"
    "s_and_b32 s93, s93, 0x3fff\n"
    "s_add_u32 s91, s85, s93\n"
    "s_add_u32 s92, s91, 65536\n"
    "buffer_load_dwordx4 v[24:27], v253, s[48:51], s91 offen offset:0 sc1\n"
    "buffer_load_dwordx4 v[88:91], v254, s[48:51], s92 offen offset:0 sc1\n"
    "buffer_load_dwordx4 v[28:31], v253, s[48:51], s91 offen offset:1024 sc1\n"
    "buffer_load_dwordx4 v[92:95], v254, s[48:51], s92 offen offset:1024 sc1\n"
    "buffer_load_dwordx4 v[32:35], v253, s[48:51], s91 offen offset:2048 sc1\n"
    "buffer_load_dwordx4 v[96:99], v254, s[48:51], s92 offen offset:2048 sc1\n"
    "buffer_load_dwordx4 v[36:39], v253, s[48:51], s91 offen offset:3072 sc1\n"
    "buffer_load_dwordx4 v[100:103], v254, s[48:51], s92 offen offset:3072 sc1\n"
    "s_add_u32 s93, s93, 4096\n"
    "s_and_b32 s93, s93, 0x3fff\n"
    "s_add_u32 s91, s85, s93\n"
    "s_add_u32 s92, s91, 65536\n"
    "v_mul_f32 v220, v200, v200\n"
    "v_fmac_f32 v220, v201, v201\n"
    "v_fmac_f32 v220, v202, v202\n"
    "v_fmac_f32 v220, v203, v203\n"
    "v_mul_f32 v221, v204, v204\n"
    "v_fmac_f32 v221, v205, v205\n"
    "v_fmac_f32 v221, v206, v206\n"
    "v_fmac_f32 v221, v207, v207\n"
    "v_mul_f32 v222, v208, v208\n"
    "v_fmac_f32 v222, v209, v209\n"
    "v_fmac_f32 v222, v210, v210\n"
    "v_fmac_f32 v222, v211, v211\n"
    "v_mul_f32 v223, v212, v212\n"
    "v_fmac_f32 v223, v213, v213\n"
    "v_fmac_f32 v223, v214, v214\n"
    "v_fmac_f32 v223, v215, v215\n"
    "v_mov_b32 v224, v220\n"
    "v_mov_b32 v225, v221\n"
    "v_mov_b32 v226, v222\n"
    "v_mov_b32 v227, v223\n"
    "v_mov_b32 v228, v217\n"
    "v_permlane16_swap_b32 v220, v224\n"
    "v_permlane16_swap_b32 v221, v225\n"
    "v_permlane16_swap_b32 v222, v226\n"
    "v_permlane16_swap_b32 v223, v227\n"
    "v_permlane16_swap_b32 v217, v228\n"
    "v_add_f32 v220, v220, v224\n"
    "v_add_f32 v221, v221, v225\n"
    "v_add_f32 v222, v222, v226\n"
    "v_add_f32 v223, v223, v227\n"
    "v_max_f32 v217, v217, v228\n"
    "v_mov_b32 v224, v220\n"
    "v_mov_b32 v225, v221\n"
    "v_mov_b32 v226, v222\n"
    "v_mov_b32 v227, v223\n"
    "v_mov_b32 v228, v217\n"
    "v_permlane32_swap_b32 v220, v224\n"
    "v_permlane32_swap_b32 v221, v225\n"
    "v_permlane32_swap_b32 v222, v226\n"
    "v_permlane32_swap_b32 v223, v227\n"
    "v_permlane32_swap_b32 v217, v228\n"
    "v_add_f32 v220, v220, v224\n"
    "v_add_f32 v221, v221, v225\n"
    "v_add_f32 v222, v222, v226\n"
    "v_add_f32 v223, v223, v227\n"
    "v_max_f32 v217, v217, v228\n"
    "v_max3_f32 v220, v220, v221, v222\n"
    "v_max_f32 v220, v220, v223\n"
    "s_nop 1\n"
    "v_max_f32_dpp v220, v220, v220 row_ror:8 row_mask:0xf bank_mask:0xf\n"
    "v_max_f32_dpp v217, v217, v217 row_ror:8 row_mask:0xf bank_mask:0xf\n"
    "s_nop 0\n"
    "v_max_f32_dpp v220, v220, v220 row_ror:4 row_mask:0xf bank_mask:0xf\n"
    "v_max_f32_dpp v217, v217, v217 row_ror:4 row_mask:0xf bank_mask:0xf\n"
    "s_nop 0\n"
    "v_max_f32_dpp v220, v220, v220 row_ror:2 row_mask:0xf bank_mask:0xf\n"
    "v_max_f32_dpp v217, v217, v217 row_ror:2 row_mask:0xf bank_mask:0xf\n"
    "s_nop 0\n"
    "v_max_f32_dpp v220, v220, v220 row_ror:1 row_mask:0xf bank_mask:0xf\n"
    "v_max_f32_dpp v217, v217, v217 row_ror:1 row_mask:0xf bank_mask:0xf\n"
    "s_mov_b64 exec, 1\n"
    "ds_max_u32 v7, v220 offset:56\n"
    "ds_max_u32 v7, v217 offset:60\n"
    "s_mov_b64 exec, -1\n"
    "v_mov_b32 v255, 1\n"
    "ds_write_b8 v5, v255 offset:88\n"
    "s_mov_b32 s96, 131072\n"
    ".Lsp_7_k:\n"
    "ds_read_b32 v255, v7 offset:24\n"
    "s_waitcnt lgkmcnt(0)\n"
    "v_readfirstlane_b32 s94, v255\n"
    "s_cmp_eq_u32 s94, 0x1010101\n"
    "s_cbranch_scc1 .Lspd_8_k\n"
    "s_sub_u32 s96, s96, 1\n"
    "s_cbranch_scc0 .Lsp_7_k\n"
    ".Lspd_8_k:\n"
    "ds_read_b128 v[248:251], v2\n"
    "s_waitcnt lgkmcnt(0)\n"
    "s_waitcnt vmcnt(15)\n"
    ".p2align 3\n"
    "v_mfma_f32_32x32x16_bf16 v[200:215], v[8:11], v[248:251], 0\n"
    "s_nop 11\n"
    "v_exp_f32 v200, v200\n"
    "v_exp_f32 v201, v201\n"
    "v_exp_f32 v202, v202\n"
    "v_exp_f32 v203, v203\n"
    "v_exp_f32 v204, v204\n"
    "s_waitcnt vmcnt(13)\n"
    "v_mfma_f32_32x32x16_bf16 v[216:231], v[12:15], v[248:251], 0\n"
    "buffer_load_dwordx4 v[40:43], v253, s[48:51], s91 offen offset:0 sc1\n"
    "buffer_load_dwordx4 v[104:107], v254, s[48:51], s92 offen offset:0 sc1\n"
    "v_permlane16_swap_b32 v72, v136\n"
    "v_permlane16_swap_b32 v73, v137\n"
    "v_permlane16_swap_b32 v74, v138\n"
    "v_permlane16_swap_b32 v75, v139\n"
    "s_setprio 1\n"
    "v_exp_f32 v205, v205\n"
    "v_exp_f32 v206, v206\n"
    "v_exp_f32 v207, v207\n"
    "v_exp_f32 v208, v208\n"
    "v_exp_f32 v209, v209\n"
    "v_smfmac_f32_32x32x32_bf16 v[232:247], v[72:75], v[200:207], v252\n"
    "v_exp_f32 v210, v210\n"
    "v_exp_f32 v211, v211\n"
    "v_exp_f32 v212, v212\n"
    "v_exp_f32 v213, v213\n"
    "v_exp_f32 v214, v214\n"
    "v_exp_f32 v215, v215\n"
    "v_exp_f32 v216, v216\n"
    "v_exp_f32 v217, v217\n"
    "v_smfmac_f32_32x32x32_bf16 v[232:247], v[136:139], v[208:215], v252\n"
    "v_exp_f32 v218, v218\n"
    "v_exp_f32 v219, v219\n"
    "v_exp_f32 v220, v220\n"
    "s_waitcnt vmcnt(13)\n"
    "v_mfma_f32_32x32x16_bf16 v[200:215], v[16:19], v[248:251], 0\n"
    "buffer_load_dwordx4 v[44:47], v253, s[48:51], s91 offen offset:1024 sc1\n"
    "buffer_load_dwordx4 v[108:111], v254, s[48:51], s92 offen offset:1024 sc1\n"
    "v_permlane16_swap_b32 v76, v140\n"
    "v_permlane16_swap_b32 v77, v141\n"
    "v_permlane16_swap_b32 v78, v142\n"
    "v_permlane16_swap_b32 v79, v143\n"
    "v_exp_f32 v221, v221\n"
    "v_exp_f32 v222, v222\n"
    "v_exp_f32 v223, v223\n"
    "v_exp_f32 v224, v224\n"
    "v_exp_f32 v225, v225\n"
    "v_smfmac_f32_32x32x32_bf16 v[232:247], v[76:79], v[216:223], v252\n"
    "v_exp_f32 v226, v226\n"
    "v_exp_f32 v227, v227\n"
    "v_exp_f32 v228, v228\n"
    "v_exp_f32 v229, v229\n"
    "v_exp_f32 v230, v230\n"
    "v_exp_f32 v231, v231\n"
    "v_exp_f32 v200, v200\n"
    "v_exp_f32 v201, v201\n"
    "v_smfmac_f32_32x32x32_bf16 v[232:247], v[140:143], v[224:231], v252\n"
    "v_exp_f32 v202, v202\n"
    "v_exp_f32 v203, v203\n"
    "v_exp_f32 v204, v204\n"
    "s_waitcnt vmcnt(13)\n"
    "v_mfma_f32_32x32x16_bf16 v[216:231], v[20:23], v[248:251], 0\n"
    "buffer_load_dwordx4 v[48:51], v253, s[48:51], s91 offen offset:2048 sc1\n"
    "buffer_load_dwordx4 v[112:115], v254, s[48:51], s92 offen offset:2048 sc1\n"
    "v_permlane16_swap_b32 v80, v144\n"
    "v_permlane16_swap_b32 v81, v145\n"
    "v_permlane16_swap_b32 v82, v146\n"
    "v_permlane16_swap_b32 v83, v147\n"
    "v_exp_f32 v205, v205\n"
    "v_exp_f32 v206, v206\n"
    "v_exp_f32 v207, v207\n"
    "v_exp_f32 v208, v208\n"
    "v_exp_f32 v209, v209\n"
    "v_smfmac_f32_32x32x32_bf16 v[232:247], v[80:83], v[200:207], v252\n"
    "v_exp_f32 v210, v210\n"
    "v_exp_f32 v211, v211\n"
    "v_exp_f32 v212, v212\n"
    "v_exp_f32 v213, v213\n"
    "v_exp_f32 v214, v214\n"
    "v_exp_f32 v215, v215\n"
    "v_exp_f32 v216, v216\n"
    "v_exp_f32 v217, v217\n"
    "v_smfmac_f32_32x32x32_bf16 v[232:247], v[144:147], v[208:215], v252\n"
    "v_exp_f32 v218, v218\n"
    "v_exp_f32 v219, v219\n"
    "v_exp_f32 v220, v220\n"
    "s_waitcnt vmcnt(13)\n"
    "v_mfma_f32_32x32x16_bf16 v[200:215], v[24:27], v[248:251], 0\n"
    "buffer_load_dwordx4 v[52:55], v253, s[48:51], s91 offen offset:3072 sc1\n"
    "buffer_load_dwordx4 v[116:119], v254, s[48:51], s92 offen offset:3072 sc1\n"
    "s_add_u32 s93, s93, 4096\n"
    "s_and_b32 s93, s93, 0x3fff\n"
    "s_add_u32 s91, s85, s93\n"
    "s_add_u32 s92, s91, 65536\n"
    "v_permlane16_swap_b32 v84, v148\n"
    "v_permlane16_swap_b32 v85, v149\n"
    "v_permlane16_swap_b32 v86, v150\n"
    "v_permlane16_swap_b32 v87, v151\n"
    "v_exp_f32 v221, v221\n"
    "v_exp_f32 v222, v222\n"
    "v_exp_f32 v223, v223\n"
    "v_exp_f32 v224, v224\n"
    "v_exp_f32 v225, v225\n"
    "v_smfmac_f32_32x32x32_bf16 v[232:247], v[84:87], v[216:223], v252\n"
    "v_exp_f32 v226, v226\n"
    "v_exp_f32 v227, v227\n"
    "v_exp_f32 v228, v228\n"
    "v_exp_f32 v229, v229\n"
    "v_exp_f32 v230, v230\n"
    "v_exp_f32 v231, v231\n"
    "v_exp_f32 v200, v200\n"
    "v_exp_f32 v201, v201\n"
    "v_smfmac_f32_32x32x32_bf16 v[232:247], v[148:151], v[224:231], v252\n"
    "v_exp_f32 v202, v202\n"
    "v_exp_f32 v203, v203\n"
    "v_exp_f32 v204, v204\n"
    "s_waitcnt vmcnt(13)\n"
    "v_mfma_f32_32x32x16_bf16 v[216:231], v[28:31], v[248:251], 0\n"
    "buffer_load_dwordx4 v[56:59], v253, s[48:51], s91 offen offset:0 sc1\n"
    "buffer_load_dwordx4 v[120:123], v254, s[48:51], s92 offen offset:0 sc1\n"
    "v_permlane16_swap_b32 v88, v152\n"
    "v_permlane16_swap_b32 v89, v153\n"
    "v_permlane16_swap_b32 v90, v154\n"
    "v_permlane16_swap_b32 v91, v155\n"
    "v_exp_f32 v205, v205\n"
    "v_exp_f32 v206, v206\n"
    "v_exp_f32 v207, v207\n"
    "v_exp_f32 v208, v208\n"
    "v_exp_f32 v209, v209\n"
    "v_smfmac_f32_32x32x32_bf16 v[232:247], v[88:91], v[200:207], v252\n"
    "v_exp_f32 v210, v210\n"
    "v_exp_f32 v211, v211\n"
    "v_exp_f32 v212, v212\n"
    "v_exp_f32 v213, v213\n"
    "v_exp_f32 v214, v214\n"
    "v_exp_f32 v215, v215\n"
    "v_exp_f32 v216, v216\n"
    "v_exp_f32 v217, v217\n"
    "v_smfmac_f32_32x32x32_bf16 v[232:247], v[152:155], v[208:215], v252\n"
    "v_exp_f32 v218, v218\n"
    "v_exp_f32 v219, v219\n"
    "v_exp_f32 v220, v220\n"
    "s_waitcnt vmcnt(13)\n"
    "v_mfma_f32_32x32x16_bf16 v[200:215], v[32:35], v[248:251], 0\n"
    "buffer_load_dwordx4 v[60:63], v253, s[48:51], s91 offen offset:1024 sc1\n"
    "buffer_load_dwordx4 v[124:127], v254, s[48:51], s92 offen offset:1024 sc1\n"
    "v_permlane16_swap_b32 v92, v156\n"
    "v_permlane16_swap_b32 v93, v157\n"
    "v_permlane16_swap_b32 v94, v158\n"
    "v_permlane16_swap_b32 v95, v159\n"
    "v_exp_f32 v221, v221\n"
    "v_exp_f32 v222, v222\n"
    "v_exp_f32 v223, v223\n"
    "v_exp_f32 v224, v224\n"
    "v_exp_f32 v225, v225\n"
    "v_smfmac_f32_32x32x32_bf16 v[232:247], v[92:95], v[216:223], v252\n"
    "v_exp_f32 v226, v226\n"
    "v_exp_f32 v227, v227\n"
    "v_exp_f32 v228, v228\n"
    "v_exp_f32 v229, v229\n"
    "v_exp_f32 v230, v230\n"
    "v_exp_f32 v231, v231\n"
    "v_exp_f32 v200, v200\n"
    "v_exp_f32 v201, v201\n"
    "v_smfmac_f32_32x32x32_bf16 v[232:247], v[156:159], v[224:231], v252\n"
    "v_exp_f32 v202, v202\n"
    "v_exp_f32 v203, v203\n"
    "v_exp_f32 v204, v204\n"
    "s_waitcnt vmcnt(13)\n"
    "v_mfma_f32_32x32x16_bf16 v[216:231], v[36:39], v[248:251], 0\n"
    "buffer_load_dwordx4 v[64:67], v253, s[48:51], s91 offen offset:2048 sc1\n"
    "buffer_load_dwordx4 v[128:131], v254, s[48:51], s92 offen offset:2048 sc1\n"
    "v_permlane16_swap_b32 v96, v160\n"
    "v_permlane16_swap_b32 v97, v161\n"
    "v_permlane16_swap_b32 v98, v162\n"
    "v_permlane16_swap_b32 v99, v163\n"
    "v_exp_f32 v205, v205\n"
    "v_exp_f32 v206, v206\n"
    "v_exp_f32 v207, v207\n"
    "v_exp_f32 v208, v208\n"
    "v_exp_f32 v209, v209\n"
    "v_smfmac_f32_32x32x32_bf16 v[232:247], v[96:99], v[200:207], v252\n"
    "v_exp_f32 v210, v210\n"
    "v_exp_f32 v211, v211\n"
    "v_exp_f32 v212, v212\n"
    "v_exp_f32 v213, v213\n"
    "v_exp_f32 v214, v214\n"
    "v_exp_f32 v215, v215\n"
    "v_exp_f32 v216, v216\n"
    "v_exp_f32 v217, v217\n"
    "v_smfmac_f32_32x32x32_bf16 v[232:247], v[160:163], v[208:215], v252\n"
    "v_exp_f32 v218, v218\n"
    "v_exp_f32 v219, v219\n"
    "v_exp_f32 v220, v220\n"
    "s_waitcnt vmcnt(13)\n"
    "v_mfma_f32_32x32x16_bf16 v[200:215], v[40:43], v[248:251], 0\n"
    "buffer_load_dwordx4 v[68:71], v253, s[48:51], s91 offen offset:3072 sc1\n"
    "buffer_load_dwordx4 v[132:135], v254, s[48:51], s92 offen offset:3072 sc1\n"
    "s_add_u32 s93, s93, 4096\n"
    "s_and_b32 s93, s93, 0x3fff\n"
    "s_add_u32 s91, s85, s93\n"
    "s_add_u32 s92, s91, 65536\n"
    "v_permlane16_swap_b32 v100, v164\n"
    "v_permlane16_swap_b32 v101, v165\n"
    "v_permlane16_swap_b32 v102, v166\n"
    "v_permlane16_swap_b32 v103, v167\n"
    "v_exp_f32 v221, v221\n"
    "v_exp_f32 v222, v222\n"
    "v_exp_f32 v223, v223\n"
    "v_exp_f32 v224, v224\n"
    "v_exp_f32 v225, v225\n"
    "v_smfmac_f32_32x32x32_bf16 v[232:247], v[100:103], v[216:223], v252\n"
    "v_exp_f32 v226, v226\n"
    "v_exp_f32 v227, v227\n"
    "v_exp_f32 v228, v228\n"
    "v_exp_f32 v229, v229\n"
    "v_exp_f32 v230, v230\n"
    "v_exp_f32 v231, v231\n"
    "v_exp_f32 v200, v200\n"
    "v_exp_f32 v201, v201\n"
    "v_smfmac_f32_32x32x32_bf16 v[232:247], v[164:167], v[224:231], v252\n"
    "v_exp_f32 v202, v202\n"
    "v_exp_f32 v203, v203\n"
    "v_exp_f32 v204, v204\n"
    "s_waitcnt vmcnt(13)\n"
    "v_mfma_f32_32x32x16_bf16 v[216:231], v[44:47], v[248:251], 0\n"
    "v_permlane16_swap_b32 v104, v168\n"
    "v_permlane16_swap_b32 v105, v169\n"
    "v_permlane16_swap_b32 v106, v170\n"
    "v_permlane16_swap_b32 v107, v171\n"
    "v_exp_f32 v205, v205\n"
    "v_exp_f32 v206, v206\n"
    "v_exp_f32 v207, v207\n"
    "v_exp_f32 v208, v208\n"
    "v_exp_f32 v209, v209\n"
    "v_smfmac_f32_32x32x32_bf16 v[232:247], v[104:107], v[200:207], v252\n"
    "v_exp_f32 v210, v210\n"
    "v_exp_f32 v211, v211\n"
    "v_exp_f32 v212, v212\n"
    "v_exp_f32 v213, v213\n"
    "v_exp_f32 v214, v214\n"
    "v_exp_f32 v215, v215\n"
    "v_exp_f32 v216, v216\n"
    "v_exp_f32 v217, v217\n"
    "v_smfmac_f32_32x32x32_bf16 v[232:247], v[168:171], v[208:215], v252\n"
    "v_exp_f32 v218, v218\n"
    "v_exp_f32 v219, v219\n"
    "v_exp_f32 v220, v220\n"
    "s_waitcnt vmcnt(11)\n"
    "v_mfma_f32_32x32x16_bf16 v[200:215], v[48:51], v[248:251], 0\n"
    "v_permlane16_swap_b32 v108, v172\n"
    "v_permlane16_swap_b32 v109, v173\n"
    "v_permlane16_swap_b32 v110, v174\n"
    "v_permlane16_swap_b32 v111, v175\n"
    "v_exp_f32 v221, v221\n"
    "v_exp_f32 v222, v222\n"
    "v_exp_f32 v223, v223\n"
    "v_exp_f32 v224, v224\n"
    "v_exp_f32 v225, v225\n"
    "v_smfmac_f32_32x32x32_bf16 v[232:247], v[108:111], v[216:223], v252\n"
    "v_exp_f32 v226, v226\n"
    "v_exp_f32 v227, v227\n"
    "v_exp_f32 v228, v228\n"
    "v_exp_f32 v229, v229\n"
    "v_exp_f32 v230, v230\n"
    "v_exp_f32 v231, v231\n"
    "v_exp_f32 v200, v200\n"
    "v_exp_f32 v201, v201\n"
    "v_smfmac_f32_32x32x32_bf16 v[232:247], v[172:175], v[224:231], v252\n"
    "v_exp_f32 v202, v202\n"
    "v_exp_f32 v203, v203\n"
    "v_exp_f32 v204, v204\n"
    "s_waitcnt vmcnt(9)\n"
    "v_mfma_f32_32x32x16_bf16 v[216:231], v[52:55], v[248:251], 0\n"
    "v_permlane16_swap_b32 v112, v176\n"
    "v_permlane16_swap_b32 v113, v177\n"
    "v_permlane16_swap_b32 v114, v178\n"
    "v_permlane16_swap_b32 v115, v179\n"
    "v_exp_f32 v205, v205\n"
    "v_exp_f32 v206, v206\n"
    "v_exp_f32 v207, v207\n"
    "v_exp_f32 v208, v208\n"
    "v_exp_f32 v209, v209\n"
    "v_smfmac_f32_32x32x32_bf16 v[232:247], v[112:115], v[200:207], v252\n"
    "v_exp_f32 v210, v210\n"
    "v_exp_f32 v211, v211\n"
    "v_exp_f32 v212, v212\n"
    "v_exp_f32 v213, v213\n"
    "v_exp_f32 v214, v214\n"
    "v_exp_f32 v215, v215\n"
    "v_exp_f32 v216, v216\n"
    "v_exp_f32 v217, v217\n"
    "v_smfmac_f32_32x32x32_bf16 v[232:247], v[176:179], v[208:215], v252\n"
    "v_exp_f32 v218, v218\n"
    "v_exp_f32 v219, v219\n"
    "v_exp_f32 v220, v220\n"
    "s_waitcnt vmcnt(7)\n"
    "v_mfma_f32_32x32x16_bf16 v[200:215], v[56:59], v[248:251], 0\n"
    "v_permlane16_swap_b32 v116, v180\n"
    "v_permlane16_swap_b32 v117, v181\n"
    "v_permlane16_swap_b32 v118, v182\n"
    "v_permlane16_swap_b32 v119, v183\n"
    "v_exp_f32 v221, v221\n"
    "v_exp_f32 v222, v222\n"
    "v_exp_f32 v223, v223\n"
    "v_exp_f32 v224, v224\n"
    "v_exp_f32 v225, v225\n"
    "v_smfmac_f32_32x32x32_bf16 v[232:247], v[116:119], v[216:223], v252\n"
    "v_exp_f32 v226, v226\n"
    "v_exp_f32 v227, v227\n"
    "v_exp_f32 v228, v228\n"
    "v_exp_f32 v229, v229\n"
    "v_exp_f32 v230, v230\n"
    "v_exp_f32 v231, v231\n"
    "v_exp_f32 v200, v200\n"
    "v_exp_f32 v201, v201\n"
    "v_smfmac_f32_32x32x32_bf16 v[232:247], v[180:183], v[224:231], v252\n"
    "v_exp_f32 v202, v202\n"
    "v_exp_f32 v203, v203\n"
    "v_exp_f32 v204, v204\n"
    "s_waitcnt vmcnt(5)\n"
    "v_mfma_f32_32x32x16_bf16 v[216:231], v[60:63], v[248:251], 0\n"
    "v_permlane16_swap_b32 v120, v184\n"
    "v_permlane16_swap_b32 v121, v185\n"
    "v_permlane16_swap_b32 v122, v186\n"
    "v_permlane16_swap_b32 v123, v187\n"
    "v_exp_f32 v205, v205\n"
    "v_exp_f32 v206, v206\n"
    "v_exp_f32 v207, v207\n"
    "v_exp_f32 v208, v208\n"
    "v_exp_f32 v209, v209\n"
    "v_smfmac_f32_32x32x32_bf16 v[232:247], v[120:123], v[200:207], v252\n"
    "v_exp_f32 v210, v210\n"
    "v_exp_f32 v211, v211\n"
    "v_exp_f32 v212, v212\n"
    "v_exp_f32 v213, v213\n"
    "v_exp_f32 v214, v214\n"
    "v_exp_f32 v215, v215\n"
    "v_exp_f32 v216, v216\n"
    "v_exp_f32 v217, v217\n"
    "v_smfmac_f32_32x32x32_bf16 v[232:247], v[184:187], v[208:215], v252\n"
    "v_exp_f32 v218, v218\n"
    "v_exp_f32 v219, v219\n"
    "v_exp_f32 v220, v220\n"
    "s_waitcnt vmcnt(3)\n"
    "v_mfma_f32_32x32x16_bf16 v[200:215], v[64:67], v[248:251], 0\n"
    "v_permlane16_swap_b32 v124, v188\n"
    "v_permlane16_swap_b32 v125, v189\n"
    "v_permlane16_swap_b32 v126, v190\n"
    "v_permlane16_swap_b32 v127, v191\n"
    "v_exp_f32 v221, v221\n"
    "v_exp_f32 v222, v222\n"
    "v_exp_f32 v223, v223\n"
    "v_exp_f32 v224, v224\n"
    "v_exp_f32 v225, v225\n"
    "v_smfmac_f32_32x32x32_bf16 v[232:247], v[124:127], v[216:223], v252\n"
    "v_exp_f32 v226, v226\n"
    "v_exp_f32 v227, v227\n"
    "v_exp_f32 v228, v228\n"
    "v_exp_f32 v229, v229\n"
    "v_exp_f32 v230, v230\n"
    "v_exp_f32 v231, v231\n"
    "v_exp_f32 v200, v200\n"
    "v_exp_f32 v201, v201\n"
    "v_smfmac_f32_32x32x32_bf16 v[232:247], v[188:191], v[224:231], v252\n"
    "v_exp_f32 v202, v202\n"
    "v_exp_f32 v203, v203\n"
    "v_exp_f32 v204, v204\n"
    "s_waitcnt vmcnt(1)\n"
    "v_mfma_f32_32x32x16_bf16 v[216:231], v[68:71], v[248:251], 0\n"
    "ds_read_b128 v[248:251], v2 offset:1024\n"
    "ds_read_b32 v255, v7 offset:88\n"
    "ds_read_b32 v253, v7 offset:56\n"
    "ds_read_b32 v254, v7 offset:60\n"
    "v_permlane16_swap_b32 v128, v192\n"
    "v_permlane16_swap_b32 v129, v193\n"
    "v_permlane16_swap_b32 v130, v194\n"
    "v_permlane16_swap_b32 v131, v195\n"
    "v_exp_f32 v205, v205\n"
    "v_exp_f32 v206, v206\n"
    "v_exp_f32 v207, v207\n"
    "v_exp_f32 v208, v208\n"
    "v_exp_f32 v209, v209\n"
    "v_smfmac_f32_32x32x32_bf16 v[232:247], v[128:131], v[200:207], v252\n"
    "v_exp_f32 v210, v210\n"
    "v_exp_f32 v211, v211\n"
    "v_exp_f32 v212, v212\n"
    "v_exp_f32 v213, v213\n"
    "v_exp_f32 v214, v214\n"
    "v_exp_f32 v215, v215\n"
    "v_exp_f32 v216, v216\n"
    "v_exp_f32 v217, v217\n"
    "v_smfmac_f32_32x32x32_bf16 v[232:247], v[192:195], v[208:215], v252\n"
    "v_exp_f32 v218, v218\n"
    "v_exp_f32 v219, v219\n"
    "v_exp_f32 v220, v220\n"
    "s_waitcnt lgkmcnt(0)\n"
    "v_mfma_f32_32x32x16_bf16 v[200:215], v[8:11], v[248:251], 0\n"
    "s_waitcnt vmcnt(0)\n"
    "v_permlane16_swap_b32 v132, v196\n"
    "v_permlane16_swap_b32 v133, v197\n"
    "v_permlane16_swap_b32 v134, v198\n"
    "v_permlane16_swap_b32 v135, v199\n"
    "v_mul_f32 v253, v253, v254\n"
    "v_cmp_eq_u32 s[88:89], s86, v255\n"
    "v_cmp_ge_f32 vcc, s87, v253\n"
    "s_and_b64 vcc, vcc, s[88:89]\n"
    "s_cbranch_vccz .L_chk_k\n"
    ".L_chkr_k:\n"
    "v_exp_f32 v221, v221\n"
    "v_exp_f32 v222, v222\n"
    "v_exp_f32 v223, v223\n"
    "v_exp_f32 v224, v224\n"
    "v_exp_f32 v225, v225\n"
    "v_smfmac_f32_32x32x32_bf16 v[232:247], v[132:135], v[216:223], v252\n"
    "v_exp_f32 v226, v226\n"
    "v_exp_f32 v227, v227\n"
    "v_exp_f32 v228, v228\n"
    "v_exp_f32 v229, v229\n"
    "v_exp_f32 v230, v230\n"
    "v_exp_f32 v231, v231\n"
    "v_exp_f32 v200, v200\n"
    "v_exp_f32 v201, v201\n"
    "v_smfmac_f32_32x32x32_bf16 v[232:247], v[196:199], v[224:231], v252\n"
    "v_exp_f32 v202, v202\n"
    "v_exp_f32 v203, v203\n"
    "v_exp_f32 v204, v204\n"
    "v_mfma_f32_32x32x16_bf16 v[216:231], v[12:15], v[248:251], 0\n"
    "v_exp_f32 v205, v205\n"
    "v_exp_f32 v206, v206\n"
    "v_exp_f32 v207, v207\n"
    "v_exp_f32 v208, v208\n"
    "v_exp_f32 v209, v209\n"
    "v_exp_f32 v210, v210\n"
    "v_exp_f32 v211, v211\n"
    "v_exp_f32 v212, v212\n"
    "ds_write_b128 v3, v[232:235] offset:0\n"
    "ds_write_b128 v3, v[236:239] offset:1024\n"
    "ds_write_b32 v4, v240 offset:0\n"
    "v_mov_b32 v255, 1\n"
    "ds_write_b8 v5, v255 offset:36\n"
    "v_mov_b64 v[232:233], 0\n"
    "v_mov_b64 v[234:235], 0\n"
    "v_mov_b64 v[236:237], 0\n"
    "v_mov_b64 v[238:239], 0\n"
    "v_mov_b64 v[240:241], 0\n"
    "v_exp_f32 v213, v213\n"
    "v_exp_f32 v214, v214\n"
    "v_exp_f32 v215, v215\n"
    "v_smfmac_f32_32x32x32_bf16 v[232:247], v[72:75], v[200:207], v252\n"
    "v_exp_f32 v216, v216\n"
    "v_exp_f32 v217, v217\n"
    "v_smfmac_f32_32x32x32_bf16 v[232:247], v[136:139], v[208:215], v252\n"
    "v_exp_f32 v218, v218\n"
    "v_exp_f32 v219, v219\n"
    "v_exp_f32 v220, v220\n"
    "v_mfma_f32_32x32x16_bf16 v[200:215], v[16:19], v[248:251], 0\n"
    "v_exp_f32 v221, v221\n"
    "v_exp_f32 v222, v222\n"
    "v_exp_f32 v223, v223\n"
    "v_exp_f32 v224, v224\n"
    "v_exp_f32 v225, v225\n"
    "v_smfmac_f32_32x32x32_bf16 v[232:247], v[76:79], v[216:223], v252\n"
    "v_exp_f32 v226, v226\n"
    "v_exp_f32 v227, v227\n"
    "v_exp_f32 v228, v228\n"
    "v_exp_f32 v229, v229\n"
    "v_exp_f32 v230, v230\n"
    "v_exp_f32 v231, v231\n"
    "v_exp_f32 v200, v200\n"
    "v_exp_f32 v201, v201\n"
    "v_smfmac_f32_32x32x32_bf16 v[232:247], v[140:143], v[224:231], v252\n"
    "v_exp_f32 v202, v202\n"
    "v_exp_f32 v203, v203\n"
    "v_exp_f32 v204, v204\n"
    "v_mfma_f32_32x32x16_bf16 v[216:231], v[20:23], v[248:251], 0\n"
    "v_exp_f32 v205, v205\n"
    "v_exp_f32 v206, v206\n"
    "v_exp_f32 v207, v207\n"
    "v_exp_f32 v208, v208\n"
    "v_exp_f32 v209, v209\n"
    "v_smfmac_f32_32x32x32_bf16 v[232:247], v[80:83], v[200:207], v252\n"
    "v_exp_f32 v210, v210\n"
    "v_exp_f32 v211, v211\n"
    "v_exp_f32 v212, v212\n"
    "v_exp_f32 v213, v213\n"
    "v_exp_f32 v214, v214\n"
    "v_exp_f32 v215, v215\n"
    "v_exp_f32 v216, v216\n"
    "v_exp_f32 v217, v217\n"
    "v_smfmac_f32_32x32x32_bf16 v[232:247], v[144:147], v[208:215], v252\n"
    "v_exp_f32 v218, v218\n"
    "v_exp_f32 v219, v219\n"
    "v_exp_f32 v220, v220\n"
    "v_mfma_f32_32x32x16_bf16 v[200:215], v[24:27], v[248:251], 0\n"
    "v_exp_f32 v221, v221\n"
    "v_exp_f32 v222, v222\n"
    "v_exp_f32 v223, v223\n"
    "v_exp_f32 v224, v224\n"
    "v_exp_f32 v225, v225\n"
    "v_smfmac_f32_32x32x32_bf16 v[232:247], v[84:87], v[216:223], v252\n"
    "v_exp_f32 v226, v226\n"
    "v_exp_f32 v227, v227\n"
    "v_exp_f32 v228, v228\n"
    "v_exp_f32 v229, v229\n"
    "v_exp_f32 v230, v230\n"
    "v_exp_f32 v231, v231\n"
    "v_exp_f32 v200, v200\n"
    "v_exp_f32 v201, v201\n"
    "v_smfmac_f32_32x32x32_bf16 v[232:247], v[148:151], v[224:231], v252\n"
    "v_exp_f32 v202, v202\n"
    "v_exp_f32 v203, v203\n"
    "v_exp_f32 v204, v204\n"
    "v_mfma_f32_32x32x16_bf16 v[216:231], v[28:31], v[248:251], 0\n"
    "v_exp_f32 v205, v205\n"
    "v_exp_f32 v206, v206\n"
    "v_exp_f32 v207, v207\n"
    "v_exp_f32 v208, v208\n"
    "v_exp_f32 v209, v209\n"
    "v_smfmac_f32_32x32x32_bf16 v[232:247], v[88:91], v[200:207], v252\n"
    "v_exp_f32 v210, v210\n"
    "v_exp_f32 v211, v211\n"
    "v_exp_f32 v212, v212\n"
    "v_exp_f32 v213, v213\n"
    "v_exp_f32 v214, v214\n"
    "v_exp_f32 v215, v215\n"
    "v_exp_f32 v216, v216\n"
    "v_exp_f32 v217, v217\n"
    "v_smfmac_f32_32x32x32_bf16 v[232:247], v[152:155], v[208:215], v252\n"
    "v_exp_f32 v218, v218\n"
    "v_exp_f32 v219, v219\n"
    "v_exp_f32 v220, v220\n"
    "v_mfma_f32_32x32x16_bf16 v[200:215], v[32:35], v[248:251], 0\n"
    "v_exp_f32 v221, v221\n"
    "v_exp_f32 v222, v222\n"
    "v_exp_f32 v223, v223\n"
    "v_exp_f32 v224, v224\n"
    "v_exp_f32 v225, v225\n"
    "v_smfmac_f32_32x32x32_bf16 v[232:247], v[92:95], v[216:223], v252\n"
    "v_exp_f32 v226, v226\n"
    "v_exp_f32 v227, v227\n"
    "v_exp_f32 v228, v228\n"
    "v_exp_f32 v229, v229\n"
    "v_exp_f32 v230, v230\n"
    "v_exp_f32 v231, v231\n"
    "v_exp_f32 v200, v200\n"
    "v_exp_f32 v201, v201\n"
    "v_smfmac_f32_32x32x32_bf16 v[232:247], v[156:159], v[224:231], v252\n"
    "v_exp_f32 v202, v202\n"
    "v_exp_f32 v203, v203\n"
    "v_exp_f32 v204, v204\n"
    "v_mfma_f32_32x32x16_bf16 v[216:231], v[36:39], v[248:251], 0\n"
    "v_exp_f32 v205, v205\n"
    "v_exp_f32 v206, v206\n"
    "v_exp_f32 v207, v207\n"
    "v_exp_f32 v208, v208\n"
    "v_exp_f32 v209, v209\n"
    "v_smfmac_f32_32x32x32_bf16 v[232:247], v[96:99], v[200:207], v252\n"
    "v_exp_f32 v210, v210\n"
    "v_exp_f32 v211, v211\n"
    "v_exp_f32 v212, v212\n"
    "v_exp_f32 v213, v213\n"
    "v_exp_f32 v214, v214\n"
    "v_exp_f32 v215, v215\n"
    "v_exp_f32 v216, v216\n"
    "v_exp_f32 v217, v217\n"
    "v_smfmac_f32_32x32x32_bf16 v[232:247], v[160:163], v[208:215], v252\n"
    "v_exp_f32 v218, v218\n"
    "v_exp_f32 v219, v219\n"
    "v_exp_f32 v220, v220\n"
    "v_mfma_f32_32x32x16_bf16 v[200:215], v[40:43], v[248:251], 0\n"
    "v_exp_f32 v221, v221\n"
    "v_exp_f32 v222, v222\n"
    "v_exp_f32 v223, v223\n"
    "v_exp_f32 v224, v224\n"
    "v_exp_f32 v225, v225\n"
    "v_smfmac_f32_32x32x32_bf16 v[232:247], v[100:103], v[216:223], v252\n"
    "v_exp_f32 v226, v226\n"
    "v_exp_f32 v227, v227\n"
    "v_exp_f32 v228, v228\n"
    "v_exp_f32 v229, v229\n"
    "v_exp_f32 v230, v230\n"
    "v_exp_f32 v231, v231\n"
    "v_exp_f32 v200, v200\n"
    "v_exp_f32 v201, v201\n"
    "v_smfmac_f32_32x32x32_bf16 v[232:247], v[164:167], v[224:231], v252\n"
    "v_exp_f32 v202, v202\n"
    "v_exp_f32 v203, v203\n"
    "v_exp_f32 v204, v204\n"
    "v_mfma_f32_32x32x16_bf16 v[216:231], v[44:47], v[248:251], 0\n"
    "v_exp_f32 v205, v205\n"
    "v_exp_f32 v206, v206\n"
    "v_exp_f32 v207, v207\n"
    "v_exp_f32 v208, v208\n"
    "v_exp_f32 v209, v209\n"
    "v_smfmac_f32_32x32x32_bf16 v[232:247], v[104:107], v[200:207], v252\n"
    "v_exp_f32 v210, v210\n"
    "v_exp_f32 v211, v211\n"
    "v_exp_f32 v212, v212\n"
    "v_exp_f32 v213, v213\n"
    "v_exp_f32 v214, v214\n"
    "v_exp_f32 v215, v215\n"
    "v_exp_f32 v216, v216\n"
    "v_exp_f32 v217, v217\n"
    "v_smfmac_f32_32x32x32_bf16 v[232:247], v[168:171], v[208:215], v252\n"
    "v_exp_f32 v218, v218\n"
    "v_exp_f32 v219, v219\n"
    "v_exp_f32 v220, v220\n"
    "v_mfma_f32_32x32x16_bf16 v[200:215], v[48:51], v[248:251], 0\n"
    "v_exp_f32 v221, v221\n"
    "v_exp_f32 v222, v222\n"
    "v_exp_f32 v223, v223\n"
    "v_exp_f32 v224, v224\n"
    "v_exp_f32 v225, v225\n"
    "v_smfmac_f32_32x32x32_bf16 v[232:247], v[108:111], v[216:223], v252\n"
    "v_exp_f32 v226, v226\n"
    "v_exp_f32 v227, v227\n"
    "v_exp_f32 v228, v228\n"
    "v_exp_f32 v229, v229\n"
    "v_exp_f32 v230, v230\n"
    "v_exp_f32 v231, v231\n"
    "v_exp_f32 v200, v200\n"
    "v_exp_f32 v201, v201\n"
    "v_smfmac_f32_32x32x32_bf16 v[232:247], v[172:175], v[224:231], v252\n"
    "v_exp_f32 v202, v202\n"
    "v_exp_f32 v203, v203\n"
    "v_exp_f32 v204, v204\n"
    "v_mfma_f32_32x32x16_bf16 v[216:231], v[52:55], v[248:251], 0\n"
    "v_exp_f32 v205, v205\n"
    "v_exp_f32 v206, v206\n"
    "v_exp_f32 v207, v207\n"
    "v_exp_f32 v208, v208\n"
    "v_exp_f32 v209, v209\n"
    "v_smfmac_f32_32x32x32_bf16 v[232:247], v[112:115], v[200:207], v252\n"
    "v_exp_f32 v210, v210\n"
    "v_exp_f32 v211, v211\n"
    "v_exp_f32 v212, v212\n"
    "v_exp_f32 v213, v213\n"
    "v_exp_f32 v214, v214\n"
    "v_exp_f32 v215, v215\n"
    "v_exp_f32 v216, v216\n"
    "v_exp_f32 v217, v217\n"
    "v_smfmac_f32_32x32x32_bf16 v[232:247], v[176:179], v[208:215], v252\n"
    "v_exp_f32 v218, v218\n"
    "v_exp_f32 v219, v219\n"
    "v_exp_f32 v220, v220\n"
    "v_mfma_f32_32x32x16_bf16 v[200:215], v[56:59], v[248:251], 0\n"
    "v_exp_f32 v221, v221\n"
    "v_exp_f32 v222, v222\n"
    "v_exp_f32 v223, v223\n"
    "v_exp_f32 v224, v224\n"
    "v_exp_f32 v225, v225\n"
    "v_smfmac_f32_32x32x32_bf16 v[232:247], v[116:119], v[216:223], v252\n"
    "v_exp_f32 v226, v226\n"
    "v_exp_f32 v227, v227\n"
    "v_exp_f32 v228, v228\n"
    "v_exp_f32 v229, v229\n"
    "v_exp_f32 v230, v230\n"
    "v_exp_f32 v231, v231\n"
    "v_exp_f32 v200, v200\n"
    "v_exp_f32 v201, v201\n"
    "v_smfmac_f32_32x32x32_bf16 v[232:247], v[180:183], v[224:231], v252\n"
    "v_exp_f32 v202, v202\n"
    "v_exp_f32 v203, v203\n"
    "v_exp_f32 v204, v204\n"
    "v_mfma_f32_32x32x16_bf16 v[216:231], v[60:63], v[248:251], 0\n"
    "v_exp_f32 v205, v205\n"
    "v_exp_f32 v206, v206\n"
    "v_exp_f32 v207, v207\n"
    "v_exp_f32 v208, v208\n"
    "v_exp_f32 v209, v209\n"
    "v_smfmac_f32_32x32x32_bf16 v[232:247], v[120:123], v[200:207], v252\n"
    "v_exp_f32 v210, v210\n"
    "v_exp_f32 v211, v211\n"
    "v_exp_f32 v212, v212\n"
    "v_exp_f32 v213, v213\n"
    "v_exp_f32 v214, v214\n"
    "v_exp_f32 v215, v215\n"
    "v_exp_f32 v216, v216\n"
    "v_exp_f32 v217, v217\n"
    "v_smfmac_f32_32x32x32_bf16 v[232:247], v[184:187], v[208:215], v252\n"
    "v_exp_f32 v218, v218\n"
    "v_exp_f32 v219, v219\n"
    "v_exp_f32 v220, v220\n"
    "v_mfma_f32_32x32x16_bf16 v[200:215], v[64:67], v[248:251], 0\n"
    "v_exp_f32 v221, v221\n"
    "v_exp_f32 v222, v222\n"
    "v_exp_f32 v223, v223\n"
    "v_exp_f32 v224, v224\n"
    "v_exp_f32 v225, v225\n"
    "v_smfmac_f32_32x32x32_bf16 v[232:247], v[124:127], v[216:223], v252\n"
    "v_exp_f32 v226, v226\n"
    "v_exp_f32 v227, v227\n"
    "v_exp_f32 v228, v228\n"
    "v_exp_f32 v229, v229\n"
    "v_exp_f32 v230, v230\n"
    "v_exp_f32 v231, v231\n"
    "v_exp_f32 v200, v200\n"
    "v_exp_f32 v201, v201\n"
    "v_smfmac_f32_32x32x32_bf16 v[232:247], v[188:191], v[224:231], v252\n"
    "v_exp_f32 v202, v202\n"
    "v_exp_f32 v203, v203\n"
    "v_exp_f32 v204, v204\n"
    "v_mfma_f32_32x32x16_bf16 v[216:231], v[68:71], v[248:251], 0\n"
    "ds_read_b128 v[248:251], v2 offset:2048\n"
    "v_exp_f32 v205, v205\n"
    "v_exp_f32 v206, v206\n"
    "v_exp_f32 v207, v207\n"
    "v_exp_f32 v208, v208\n"
    "v_exp_f32 v209, v209\n"
    "v_smfmac_f32_32x32x32_bf16 v[232:247], v[128:131], v[200:207], v252\n"
    "v_exp_f32 v210, v210\n"
    "v_exp_f32 v211, v211\n"
    "v_exp_f32 v212, v212\n"
    "v_exp_f32 v213, v213\n"
    "v_exp_f32 v214, v214\n"
    "v_exp_f32 v215, v215\n"
    "v_exp_f32 v216, v216\n"
    "v_exp_f32 v217, v217\n"
    "v_smfmac_f32_32x32x32_bf16 v[232:247], v[192:195], v[208:215], v252\n"
    "v_exp_f32 v218, v218\n"
    "v_exp_f32 v219, v219\n"
    "v_exp_f32 v220, v220\n"
    "s_waitcnt lgkmcnt(0)\n"
    "v_mfma_f32_32x32x16_bf16 v[200:215], v[8:11], v[248:251], 0\n"
    "v_exp_f32 v221, v221\n"
    "v_exp_f32 v222, v222\n"
    "v_exp_f32 v223, v223\n"
    "v_exp_f32 v224, v224\n"
    "v_exp_f32 v225, v225\n"
    "v_smfmac_f32_32x32x32_bf16 v[232:247], v[132:135], v[216:223], v252\n"
    "v_exp_f32 v226, v226\n"
    "v_exp_f32 v227, v227\n"
    "v_exp_f32 v228, v228\n"
    "v_exp_f32 v229, v229\n"
    "v_exp_f32 v230, v230\n"
    "v_exp_f32 v231, v231\n"
    "v_exp_f32 v200, v200\n"
    "v_exp_f32 v201, v201\n"
    "v_smfmac_f32_32x32x32_bf16 v[232:247], v[196:199], v[224:231], v252\n"
    "v_exp_f32 v202, v202\n"
    "v_exp_f32 v203, v203\n"
    "v_exp_f32 v204, v204\n"
    "v_mfma_f32_32x32x16_bf16 v[216:231], v[12:15], v[248:251], 0\n"
    "v_exp_f32 v205, v205\n"
    "v_exp_f32 v206, v206\n"
    "v_exp_f32 v207, v207\n"
    "v_exp_f32 v208, v208\n"
    "v_exp_f32 v209, v209\n"
    "v_exp_f32 v210, v210\n"
    "v_exp_f32 v211, v211\n"
    "v_exp_f32 v212, v212\n"
    "ds_write_b128 v3, v[232:235] offset:9216\n"
    "ds_write_b128 v3, v[236:239] offset:10240\n"
    "ds_write_b32 v4, v240 offset:9216\n"
    "v_mov_b32 v255, 2\n"
    "ds_write_b8 v5, v255 offset:36\n"
    "v_mov_b64 v[232:233], 0\n"
    "v_mov_b64 v[234:235], 0\n"
    "v_mov_b64 v[236:237], 0\n"
    "v_mov_b64 v[238:239], 0\n"
    "v_mov_b64 v[240:241], 0\n"
    "v_exp_f32 v213, v213\n"
    "v_exp_f32 v214, v214\n"
    "v_exp_f32 v215, v215\n"
    "v_smfmac_f32_32x32x32_bf16 v[232:247], v[72:75], v[200:207], v252\n"
    "v_exp_f32 v216, v216\n"
    "v_exp_f32 v217, v217\n"
    "v_smfmac_f32_32x32x32_bf16 v[232:247], v[136:139], v[208:215], v252\n"
    "v_exp_f32 v218, v218\n"
    "v_exp_f32 v219, v219\n"
    "v_exp_f32 v220, v220\n"
    "v_mfma_f32_32x32x16_bf16 v[200:215], v[16:19], v[248:251], 0\n"
    "v_exp_f32 v221, v221\n"
    "v_exp_f32 v222, v222\n"
    "v_exp_f32 v223, v223\n"
    "v_exp_f32 v224, v224\n"
    "v_exp_f32 v225, v225\n"
    "v_smfmac_f32_32x32x32_bf16 v[232:247], v[76:79], v[216:223], v252\n"
    "v_exp_f32 v226, v226\n"
    "v_exp_f32 v227, v227\n"
    "v_exp_f32 v228, v228\n"
    "v_exp_f32 v229, v229\n"
    "v_exp_f32 v230, v230\n"
    "v_exp_f32 v231, v231\n"
    "v_exp_f32 v200, v200\n"
    "v_exp_f32 v201, v201\n"
    "v_smfmac_f32_32x32x32_bf16 v[232:247], v[140:143], v[224:231], v252\n"
    "v_exp_f32 v202, v202\n"
    "v_exp_f32 v203, v203\n"
    "v_exp_f32 v204, v204\n"
    "v_mfma_f32_32x32x16_bf16 v[216:231], v[20:23], v[248:251], 0\n"
    "v_exp_f32 v205, v205\n"
    "v_exp_f32 v206, v206\n"
    "v_exp_f32 v207, v207\n"
    "v_exp_f32 v208, v208\n"
    "v_exp_f32 v209, v209\n"
    "v_smfmac_f32_32x32x32_bf16 v[232:247], v[80:83], v[200:207], v252\n"
    "v_exp_f32 v210, v210\n"
    "v_exp_f32 v211, v211\n"
    "v_exp_f32 v212, v212\n"
    "v_exp_f32 v213, v213\n"
    "v_exp_f32 v214, v214\n"
    "v_exp_f32 v215, v215\n"
    "v_exp_f32 v216, v216\n"
    "v_exp_f32 v217, v217\n"
    "v_smfmac_f32_32x32x32_bf16 v[232:247], v[144:147], v[208:215], v252\n"
    "v_exp_f32 v218, v218\n"
    "v_exp_f32 v219, v219\n"
    "v_exp_f32 v220, v220\n"
    "v_mfma_f32_32x32x16_bf16 v[200:215], v[24:27], v[248:251], 0\n"
    "v_exp_f32 v221, v221\n"
    "v_exp_f32 v222, v222\n"
    "v_exp_f32 v223, v223\n"
    "v_exp_f32 v224, v224\n"
    "v_exp_f32 v225, v225\n"
    "v_smfmac_f32_32x32x32_bf16 v[232:247], v[84:87], v[216:223], v252\n"
    "v_exp_f32 v226, v226\n"
    "v_exp_f32 v227, v227\n"
    "v_exp_f32 v228, v228\n"
    "v_exp_f32 v229, v229\n"
    "v_exp_f32 v230, v230\n"
    "v_exp_f32 v231, v231\n"
    "v_exp_f32 v200, v200\n"
    "v_exp_f32 v201, v201\n"
    "v_smfmac_f32_32x32x32_bf16 v[232:247], v[148:151], v[224:231], v252\n"
    "v_exp_f32 v202, v202\n"
    "v_exp_f32 v203, v203\n"
    "v_exp_f32 v204, v204\n"
    "v_mfma_f32_32x32x16_bf16 v[216:231], v[28:31], v[248:251], 0\n"
    "v_exp_f32 v205, v205\n"
    "v_exp_f32 v206, v206\n"
    "v_exp_f32 v207, v207\n"
    "v_exp_f32 v208, v208\n"
    "v_exp_f32 v209, v209\n"
    "v_smfmac_f32_32x32x32_bf16 v[232:247], v[88:91], v[200:207], v252\n"
    "v_exp_f32 v210, v210\n"
    "v_exp_f32 v211, v211\n"
    "v_exp_f32 v212, v212\n"
    "v_exp_f32 v213, v213\n"
    "v_exp_f32 v214, v214\n"
    "v_exp_f32 v215, v215\n"
    "v_exp_f32 v216, v216\n"
    "v_exp_f32 v217, v217\n"
    "v_smfmac_f32_32x32x32_bf16 v[232:247], v[152:155], v[208:215], v252\n"
    "v_exp_f32 v218, v218\n"
    "v_exp_f32 v219, v219\n"
    "v_exp_f32 v220, v220\n"
    "v_mfma_f32_32x32x16_bf16 v[200:215], v[32:35], v[248:251], 0\n"
    "v_exp_f32 v221, v221\n"
    "v_exp_f32 v222, v222\n"
    "v_exp_f32 v223, v223\n"
    "v_exp_f32 v224, v224\n"
    "v_exp_f32 v225, v225\n"
    "v_smfmac_f32_32x32x32_bf16 v[232:247], v[92:95], v[216:223], v252\n"
    "v_exp_f32 v226, v226\n"
    "v_exp_f32 v227, v227\n"
    "v_exp_f32 v228, v228\n"
    "v_exp_f32 v229, v229\n"
    "v_exp_f32 v230, v230\n"
    "v_exp_f32 v231, v231\n"
    "v_exp_f32 v200, v200\n"
    "v_exp_f32 v201, v201\n"
    "v_smfmac_f32_32x32x32_bf16 v[232:247], v[156:159], v[224:231], v252\n"
    "v_exp_f32 v202, v202\n"
    "v_exp_f32 v203, v203\n"
    "v_exp_f32 v204, v204\n"
    "v_mfma_f32_32x32x16_bf16 v[216:231], v[36:39], v[248:251], 0\n"
    "v_exp_f32 v205, v205\n"
    "v_exp_f32 v206, v206\n"
    "v_exp_f32 v207, v207\n"
    "v_exp_f32 v208, v208\n"
    "v_exp_f32 v209, v209\n"
    "v_smfmac_f32_32x32x32_bf16 v[232:247], v[96:99], v[200:207], v252\n"
    "v_exp_f32 v210, v210\n"
    "v_exp_f32 v211, v211\n"
    "v_exp_f32 v212, v212\n"
    "v_exp_f32 v213, v213\n"
    "v_exp_f32 v214, v214\n"
    "v_exp_f32 v215, v215\n"
    "v_exp_f32 v216, v216\n"
    "v_exp_f32 v217, v217\n"
    "v_smfmac_f32_32x32x32_bf16 v[232:247], v[160:163], v[208:215], v252\n"
    "v_exp_f32 v218, v218\n"
    "v_exp_f32 v219, v219\n"
    "v_exp_f32 v220, v220\n"
    "v_mfma_f32_32x32x16_bf16 v[200:215], v[40:43], v[248:251], 0\n"
    "v_exp_f32 v221, v221\n"
    "v_exp_f32 v222, v222\n"
    "v_exp_f32 v223, v223\n"
    "v_exp_f32 v224, v224\n"
    "v_exp_f32 v225, v225\n"
    "v_smfmac_f32_32x32x32_bf16 v[232:247], v[100:103], v[216:223], v252\n"
    "v_exp_f32 v226, v226\n"
    "v_exp_f32 v227, v227\n"
    "v_exp_f32 v228, v228\n"
    "v_exp_f32 v229, v229\n"
    "v_exp_f32 v230, v230\n"
    "v_exp_f32 v231, v231\n"
    "v_exp_f32 v200, v200\n"
    "v_exp_f32 v201, v201\n"
    "v_smfmac_f32_32x32x32_bf16 v[232:247], v[164:167], v[224:231], v252\n"
    "v_exp_f32 v202, v202\n"
    "v_exp_f32 v203, v203\n"
    "v_exp_f32 v204, v204\n"
    "v_mfma_f32_32x32x16_bf16 v[216:231], v[44:47], v[248:251], 0\n"
    "v_exp_f32 v205, v205\n"
    "v_exp_f32 v206, v206\n"
    "v_exp_f32 v207, v207\n"
    "v_exp_f32 v208, v208\n"
    "v_exp_f32 v209, v209\n"
    "v_smfmac_f32_32x32x32_bf16 v[232:247], v[104:107], v[200:207], v252\n"
    "v_exp_f32 v210, v210\n"
    "v_exp_f32 v211, v211\n"
    "v_exp_f32 v212, v212\n"
    "v_exp_f32 v213, v213\n"
    "v_exp_f32 v214, v214\n"
    "v_exp_f32 v215, v215\n"
    "v_exp_f32 v216, v216\n"
    "v_exp_f32 v217, v217\n"
    "v_smfmac_f32_32x32x32_bf16 v[232:247], v[168:171], v[208:215], v252\n"
    "v_exp_f32 v218, v218\n"
    "v_exp_f32 v219, v219\n"
    "v_exp_f32 v220, v220\n"
    "v_mfma_f32_32x32x16_bf16 v[200:215], v[48:51], v[248:251], 0\n"
    "v_exp_f32 v221, v221\n"
    "v_exp_f32 v222, v222\n"
    "v_exp_f32 v223, v223\n"
    "v_exp_f32 v224, v224\n"
    "v_exp_f32 v225, v225\n"
    "v_smfmac_f32_32x32x32_bf16 v[232:247], v[108:111], v[216:223], v252\n"
    "v_exp_f32 v226, v226\n"
    "v_exp_f32 v227, v227\n"
    "v_exp_f32 v228, v228\n"
    "v_exp_f32 v229, v229\n"
    "v_exp_f32 v230, v230\n"
    "v_exp_f32 v231, v231\n"
    "v_exp_f32 v200, v200\n"
    "v_exp_f32 v201, v201\n"
    "v_smfmac_f32_32x32x32_bf16 v[232:247], v[172:175], v[224:231], v252\n"
    "v_exp_f32 v202, v202\n"
    "v_exp_f32 v203, v203\n"
    "v_exp_f32 v204, v204\n"
    "v_mfma_f32_32x32x16_bf16 v[216:231], v[52:55], v[248:251], 0\n"
    "v_exp_f32 v205, v205\n"
    "v_exp_f32 v206, v206\n"
    "v_exp_f32 v207, v207\n"
    "v_exp_f32 v208, v208\n"
    "v_exp_f32 v209, v209\n"
    "v_smfmac_f32_32x32x32_bf16 v[232:247], v[112:115], v[200:207], v252\n"
    "v_exp_f32 v210, v210\n"
    "v_exp_f32 v211, v211\n"
    "v_exp_f32 v212, v212\n"
    "v_exp_f32 v213, v213\n"
    "v_exp_f32 v214, v214\n"
    "v_exp_f32 v215, v215\n"
    "v_exp_f32 v216, v216\n"
    "v_exp_f32 v217, v217\n"
    "v_smfmac_f32_32x32x32_bf16 v[232:247], v[176:179], v[208:215], v252\n"
    "v_exp_f32 v218, v218\n"
    "v_exp_f32 v219, v219\n"
    "v_exp_f32 v220, v220\n"
    "v_mfma_f32_32x32x16_bf16 v[200:215], v[56:59], v[248:251], 0\n"
    "v_exp_f32 v221, v221\n"
    "v_exp_f32 v222, v222\n"
    "v_exp_f32 v223, v223\n"
    "v_exp_f32 v224, v224\n"
    "v_exp_f32 v225, v225\n"
    "v_smfmac_f32_32x32x32_bf16 v[232:247], v[116:119], v[216:223], v252\n"
    "v_exp_f32 v226, v226\n"
    "v_exp_f32 v227, v227\n"
    "v_exp_f32 v228, v228\n"
    "v_exp_f32 v229, v229\n"
    "v_exp_f32 v230, v230\n"
    "v_exp_f32 v231, v231\n"
    "v_exp_f32 v200, v200\n"
    "v_exp_f32 v201, v201\n"
    "v_smfmac_f32_32x32x32_bf16 v[232:247], v[180:183], v[224:231], v252\n"
    "v_exp_f32 v202, v202\n"
    "v_exp_f32 v203, v203\n"
    "v_exp_f32 v204, v204\n"
    "v_mfma_f32_32x32x16_bf16 v[216:231], v[60:63], v[248:251], 0\n"
    "v_exp_f32 v205, v205\n"
    "v_exp_f32 v206, v206\n"
    "v_exp_f32 v207, v207\n"
    "v_exp_f32 v208, v208\n"
    "v_exp_f32 v209, v209\n"
    "v_smfmac_f32_32x32x32_bf16 v[232:247], v[120:123], v[200:207], v252\n"
    "v_exp_f32 v210, v210\n"
    "v_exp_f32 v211, v211\n"
    "v_exp_f32 v212, v212\n"
    "v_exp_f32 v213, v213\n"
    "v_exp_f32 v214, v214\n"
    "v_exp_f32 v215, v215\n"
    "v_exp_f32 v216, v216\n"
    "v_exp_f32 v217, v217\n"
    "v_smfmac_f32_32x32x32_bf16 v[232:247], v[184:187], v[208:215], v252\n"
    "v_exp_f32 v218, v218\n"
    "v_exp_f32 v219, v219\n"
    "v_exp_f32 v220, v220\n"
    "v_mfma_f32_32x32x16_bf16 v[200:215], v[64:67], v[248:251], 0\n"
    "v_exp_f32 v221, v221\n"
    "v_exp_f32 v222, v222\n"
    "v_exp_f32 v223, v223\n"
    "v_exp_f32 v224, v224\n"
    "v_exp_f32 v225, v225\n"
    "v_smfmac_f32_32x32x32_bf16 v[232:247], v[124:127], v[216:223], v252\n"
    "v_exp_f32 v226, v226\n"
    "v_exp_f32 v227, v227\n"
    "v_exp_f32 v228, v228\n"
    "v_exp_f32 v229, v229\n"
    "v_exp_f32 v230, v230\n"
    "v_exp_f32 v231, v231\n"
    "v_exp_f32 v200, v200\n"
    "v_exp_f32 v201, v201\n"
    "v_smfmac_f32_32x32x32_bf16 v[232:247], v[188:191], v[224:231], v252\n"
    "v_exp_f32 v202, v202\n"
    "v_exp_f32 v203, v203\n"
    "v_exp_f32 v204, v204\n"
    "v_mfma_f32_32x32x16_bf16 v[216:231], v[68:71], v[248:251], 0\n"
    "ds_read_b128 v[248:251], v2 offset:3072\n"
    "v_exp_f32 v205, v205\n"
    "v_exp_f32 v206, v206\n"
    "v_exp_f32 v207, v207\n"
    "v_exp_f32 v208, v208\n"
    "v_exp_f32 v209, v209\n"
    "v_smfmac_f32_32x32x32_bf16 v[232:247], v[128:131], v[200:207], v252\n"
    "v_exp_f32 v210, v210\n"
    "v_exp_f32 v211, v211\n"
    "v_exp_f32 v212, v212\n"
    "v_exp_f32 v213, v213\n"
    "v_exp_f32 v214, v214\n"
    "v_exp_f32 v215, v215\n"
    "v_exp_f32 v216, v216\n"
    "v_exp_f32 v217, v217\n"
    "v_smfmac_f32_32x32x32_bf16 v[232:247], v[192:195], v[208:215], v252\n"
    "v_exp_f32 v218, v218\n"
    "v_exp_f32 v219, v219\n"
    "v_exp_f32 v220, v220\n"
    "s_waitcnt lgkmcnt(0)\n"
    "v_mfma_f32_32x32x16_bf16 v[200:215], v[8:11], v[248:251], 0\n"
    "v_exp_f32 v221, v221\n"
    "v_exp_f32 v222, v222\n"
    "v_exp_f32 v223, v223\n"
    "v_exp_f32 v224, v224\n"
    "v_exp_f32 v225, v225\n"
    "v_smfmac_f32_32x32x32_bf16 v[232:247], v[132:135], v[216:223], v252\n"
    "v_exp_f32 v226, v226\n"
    "v_exp_f32 v227, v227\n"
    "v_exp_f32 v228, v228\n"
    "v_exp_f32 v229, v229\n"
    "v_exp_f32 v230, v230\n"
    "v_exp_f32 v231, v231\n"
    "v_exp_f32 v200, v200\n"
    "v_exp_f32 v201, v201\n"
    "v_smfmac_f32_32x32x32_bf16 v[232:247], v[196:199], v[224:231], v252\n"
    "v_exp_f32 v202, v202\n"
    "v_exp_f32 v203, v203\n"
    "v_exp_f32 v204, v204\n"
    "v_mfma_f32_32x32x16_bf16 v[216:231], v[12:15], v[248:251], 0\n"
    "v_exp_f32 v205, v205\n"
    "v_exp_f32 v206, v206\n"
    "v_exp_f32 v207, v207\n"
    "v_exp_f32 v208, v208\n"
    "v_exp_f32 v209, v209\n"
    "v_exp_f32 v210, v210\n"
    "v_exp_f32 v211, v211\n"
    "v_exp_f32 v212, v212\n"
    "ds_write_b128 v3, v[232:235] offset:18432\n"
    "ds_write_b128 v3, v[236:239] offset:19456\n"
    "ds_write_b32 v4, v240 offset:18432\n"
    "v_mov_b32 v255, 3\n"
    "ds_write_b8 v5, v255 offset:36\n"
    "v_mov_b64 v[232:233], 0\n"
    "v_mov_b64 v[234:235], 0\n"
    "v_mov_b64 v[236:237], 0\n"
    "v_mov_b64 v[238:239], 0\n"
    "v_mov_b64 v[240:241], 0\n"
    "v_exp_f32 v213, v213\n"
    "v_exp_f32 v214, v214\n"
    "v_exp_f32 v215, v215\n"
    "v_smfmac_f32_32x32x32_bf16 v[232:247], v[72:75], v[200:207], v252\n"
    "v_exp_f32 v216, v216\n"
    "v_exp_f32 v217, v217\n"
    "v_smfmac_f32_32x32x32_bf16 v[232:247], v[136:139], v[208:215], v252\n"
    "v_exp_f32 v218, v218\n"
    "v_exp_f32 v219, v219\n"
    "v_exp_f32 v220, v220\n"
    "v_mfma_f32_32x32x16_bf16 v[200:215], v[16:19], v[248:251], 0\n"
    "v_exp_f32 v221, v221\n"
    "v_exp_f32 v222, v222\n"
    "v_exp_f32 v223, v223\n"
    "v_exp_f32 v224, v224\n"
    "v_exp_f32 v225, v225\n"
    "v_smfmac_f32_32x32x32_bf16 v[232:247], v[76:79], v[216:223], v252\n"
    "v_exp_f32 v226, v226\n"
    "v_exp_f32 v227, v227\n"
    "v_exp_f32 v228, v228\n"
    "v_exp_f32 v229, v229\n"
    "v_exp_f32 v230, v230\n"
    "v_exp_f32 v231, v231\n"
    "v_exp_f32 v200, v200\n"
    "v_exp_f32 v201, v201\n"
    "v_smfmac_f32_32x32x32_bf16 v[232:247], v[140:143], v[224:231], v252\n"
    "v_exp_f32 v202, v202\n"
    "v_exp_f32 v203, v203\n"
    "v_exp_f32 v204, v204\n"
    "v_mfma_f32_32x32x16_bf16 v[216:231], v[20:23], v[248:251], 0\n"
    "v_exp_f32 v205, v205\n"
    "v_exp_f32 v206, v206\n"
    "v_exp_f32 v207, v207\n"
    "v_exp_f32 v208, v208\n"
    "v_exp_f32 v209, v209\n"
    "v_smfmac_f32_32x32x32_bf16 v[232:247], v[80:83], v[200:207], v252\n"
    "v_exp_f32 v210, v210\n"
    "v_exp_f32 v211, v211\n"
    "v_exp_f32 v212, v212\n"
    "v_exp_f32 v213, v213\n"
    "v_exp_f32 v214, v214\n"
    "v_exp_f32 v215, v215\n"
    "v_exp_f32 v216, v216\n"
    "v_exp_f32 v217, v217\n"
    "v_smfmac_f32_32x32x32_bf16 v[232:247], v[144:147], v[208:215], v252\n"
    "v_exp_f32 v218, v218\n"
    "v_exp_f32 v219, v219\n"
    "v_exp_f32 v220, v220\n"
    "v_mfma_f32_32x32x16_bf16 v[200:215], v[24:27], v[248:251], 0\n"
    "v_exp_f32 v221, v221\n"
    "v_exp_f32 v222, v222\n"
    "v_exp_f32 v223, v223\n"
    "v_exp_f32 v224, v224\n"
    "v_exp_f32 v225, v225\n"
    "v_smfmac_f32_32x32x32_bf16 v[232:247], v[84:87], v[216:223], v252\n"
    "v_exp_f32 v226, v226\n"
    "v_exp_f32 v227, v227\n"
    "v_exp_f32 v228, v228\n"
    "v_exp_f32 v229, v229\n"
    "v_exp_f32 v230, v230\n"
    "v_exp_f32 v231, v231\n"
    "v_exp_f32 v200, v200\n"
    "v_exp_f32 v201, v201\n"
    "v_smfmac_f32_32x32x32_bf16 v[232:247], v[148:151], v[224:231], v252\n"
    "v_exp_f32 v202, v202\n"
    "v_exp_f32 v203, v203\n"
    "v_exp_f32 v204, v204\n"
    "v_mfma_f32_32x32x16_bf16 v[216:231], v[28:31], v[248:251], 0\n"
    "v_exp_f32 v205, v205\n"
    "v_exp_f32 v206, v206\n"
    "v_exp_f32 v207, v207\n"
    "v_exp_f32 v208, v208\n"
    "v_exp_f32 v209, v209\n"
    "v_smfmac_f32_32x32x32_bf16 v[232:247], v[88:91], v[200:207], v252\n"
    "v_exp_f32 v210, v210\n"
    "v_exp_f32 v211, v211\n"
    "v_exp_f32 v212, v212\n"
    "v_exp_f32 v213, v213\n"
    "v_exp_f32 v214, v214\n"
    "v_exp_f32 v215, v215\n"
    "v_exp_f32 v216, v216\n"
    "v_exp_f32 v217, v217\n"
    "v_smfmac_f32_32x32x32_bf16 v[232:247], v[152:155], v[208:215], v252\n"
    "v_exp_f32 v218, v218\n"
    "v_exp_f32 v219, v219\n"
    "v_exp_f32 v220, v220\n"
    "v_mfma_f32_32x32x16_bf16 v[200:215], v[32:35], v[248:251], 0\n"
    "v_exp_f32 v221, v221\n"
    "v_exp_f32 v222, v222\n"
    "v_exp_f32 v223, v223\n"
    "v_exp_f32 v224, v224\n"
    "v_exp_f32 v225, v225\n"
    "v_smfmac_f32_32x32x32_bf16 v[232:247], v[92:95], v[216:223], v252\n"
    "v_exp_f32 v226, v226\n"
    "v_exp_f32 v227, v227\n"
    "v_exp_f32 v228, v228\n"
    "v_exp_f32 v229, v229\n"
    "v_exp_f32 v230, v230\n"
    "v_exp_f32 v231, v231\n"
    "v_exp_f32 v200, v200\n"
    "v_exp_f32 v201, v201\n"
    "v_smfmac_f32_32x32x32_bf16 v[232:247], v[156:159], v[224:231], v252\n"
    "v_exp_f32 v202, v202\n"
    "v_exp_f32 v203, v203\n"
    "v_exp_f32 v204, v204\n"
    "v_mfma_f32_32x32x16_bf16 v[216:231], v[36:39], v[248:251], 0\n"
    "v_exp_f32 v205, v205\n"
    "v_exp_f32 v206, v206\n"
    "v_exp_f32 v207, v207\n"
    "v_exp_f32 v208, v208\n"
    "v_exp_f32 v209, v209\n"
    "v_smfmac_f32_32x32x32_bf16 v[232:247], v[96:99], v[200:207], v252\n"
    "v_exp_f32 v210, v210\n"
    "v_exp_f32 v211, v211\n"
    "v_exp_f32 v212, v212\n"
    "v_exp_f32 v213, v213\n"
    "v_exp_f32 v214, v214\n"
    "v_exp_f32 v215, v215\n"
    "v_exp_f32 v216, v216\n"
    "v_exp_f32 v217, v217\n"
    "v_smfmac_f32_32x32x32_bf16 v[232:247], v[160:163], v[208:215], v252\n"
    "v_exp_f32 v218, v218\n"
    "v_exp_f32 v219, v219\n"
    "v_exp_f32 v220, v220\n"
    "v_mfma_f32_32x32x16_bf16 v[200:215], v[40:43], v[248:251], 0\n"
    "v_exp_f32 v221, v221\n"
    "v_exp_f32 v222, v222\n"
    "v_exp_f32 v223, v223\n"
    "v_exp_f32 v224, v224\n"
    "v_exp_f32 v225, v225\n"
    "v_smfmac_f32_32x32x32_bf16 v[232:247], v[100:103], v[216:223], v252\n"
    "v_exp_f32 v226, v226\n"
    "v_exp_f32 v227, v227\n"
    "v_exp_f32 v228, v228\n"
    "v_exp_f32 v229, v229\n"
    "v_exp_f32 v230, v230\n"
    "v_exp_f32 v231, v231\n"
    "v_exp_f32 v200, v200\n"
    "v_exp_f32 v201, v201\n"
    "v_smfmac_f32_32x32x32_bf16 v[232:247], v[164:167], v[224:231], v252\n"
    "v_exp_f32 v202, v202\n"
    "v_exp_f32 v203, v203\n"
    "v_exp_f32 v204, v204\n"
    "v_mfma_f32_32x32x16_bf16 v[216:231], v[44:47], v[248:251], 0\n"
    "v_exp_f32 v205, v205\n"
    "v_exp_f32 v206, v206\n"
    "v_exp_f32 v207, v207\n"
    "v_exp_f32 v208, v208\n"
    "v_exp_f32 v209, v209\n"
    "v_smfmac_f32_32x32x32_bf16 v[232:247], v[104:107], v[200:207], v252\n"
    "v_exp_f32 v210, v210\n"
    "v_exp_f32 v211, v211\n"
    "v_exp_f32 v212, v212\n"
    "v_exp_f32 v213, v213\n"
    "v_exp_f32 v214, v214\n"
    "v_exp_f32 v215, v215\n"
    "v_exp_f32 v216, v216\n"
    "v_exp_f32 v217, v217\n"
    "v_smfmac_f32_32x32x32_bf16 v[232:247], v[168:171], v[208:215], v252\n"
    "v_exp_f32 v218, v218\n"
    "v_exp_f32 v219, v219\n"
    "v_exp_f32 v220, v220\n"
    "v_mfma_f32_32x32x16_bf16 v[200:215], v[48:51], v[248:251], 0\n"
    "v_exp_f32 v221, v221\n"
    "v_exp_f32 v222, v222\n"
    "v_exp_f32 v223, v223\n"
    "v_exp_f32 v224, v224\n"
    "v_exp_f32 v225, v225\n"
    "v_smfmac_f32_32x32x32_bf16 v[232:247], v[108:111], v[216:223], v252\n"
    "v_exp_f32 v226, v226\n"
    "v_exp_f32 v227, v227\n"
    "v_exp_f32 v228, v228\n"
    "v_exp_f32 v229, v229\n"
    "v_exp_f32 v230, v230\n"
    "v_exp_f32 v231, v231\n"
    "v_exp_f32 v200, v200\n"
    "v_exp_f32 v201, v201\n"
    "v_smfmac_f32_32x32x32_bf16 v[232:247], v[172:175], v[224:231], v252\n"
    "v_exp_f32 v202, v202\n"
    "v_exp_f32 v203, v203\n"
    "v_exp_f32 v204, v204\n"
    "v_mfma_f32_32x32x16_bf16 v[216:231], v[52:55], v[248:251], 0\n"
    "v_exp_f32 v205, v205\n"
    "v_exp_f32 v206, v206\n"
    "v_exp_f32 v207, v207\n"
    "v_exp_f32 v208, v208\n"
    "v_exp_f32 v209, v209\n"
    "v_smfmac_f32_32x32x32_bf16 v[232:247], v[112:115], v[200:207], v252\n"
    "v_exp_f32 v210, v210\n"
    "v_exp_f32 v211, v211\n"
    "v_exp_f32 v212, v212\n"
    "v_exp_f32 v213, v213\n"
    "v_exp_f32 v214, v214\n"
    "v_exp_f32 v215, v215\n"
    "v_exp_f32 v216, v216\n"
    "v_exp_f32 v217, v217\n"
    "v_smfmac_f32_32x32x32_bf16 v[232:247], v[176:179], v[208:215], v252\n"
    "v_exp_f32 v218, v218\n"
    "v_exp_f32 v219, v219\n"
    "v_exp_f32 v220, v220\n"
    "v_mfma_f32_32x32x16_bf16 v[200:215], v[56:59], v[248:251], 0\n"
    "v_exp_f32 v221, v221\n"
    "v_exp_f32 v222, v222\n"
    "v_exp_f32 v223, v223\n"
    "v_exp_f32 v224, v224\n"
    "v_exp_f32 v225, v225\n"
    "v_smfmac_f32_32x32x32_bf16 v[232:247], v[116:119], v[216:223], v252\n"
    "v_exp_f32 v226, v226\n"
    "v_exp_f32 v227, v227\n"
    "v_exp_f32 v228, v228\n"
    "v_exp_f32 v229, v229\n"
    "v_exp_f32 v230, v230\n"
    "v_exp_f32 v231, v231\n"
    "v_exp_f32 v200, v200\n"
    "v_exp_f32 v201, v201\n"
    "v_smfmac_f32_32x32x32_bf16 v[232:247], v[180:183], v[224:231], v252\n"
    "v_exp_f32 v202, v202\n"
    "v_exp_f32 v203, v203\n"
    "v_exp_f32 v204, v204\n"
    "v_mfma_f32_32x32x16_bf16 v[216:231], v[60:63], v[248:251], 0\n"
    "v_exp_f32 v205, v205\n"
    "v_exp_f32 v206, v206\n"
    "v_exp_f32 v207, v207\n"
    "v_exp_f32 v208, v208\n"
    "v_exp_f32 v209, v209\n"
    "v_smfmac_f32_32x32x32_bf16 v[232:247], v[120:123], v[200:207], v252\n"
    "v_exp_f32 v210, v210\n"
    "v_exp_f32 v211, v211\n"
    "v_exp_f32 v212, v212\n"
    "v_exp_f32 v213, v213\n"
    "v_exp_f32 v214, v214\n"
    "v_exp_f32 v215, v215\n"
    "v_exp_f32 v216, v216\n"
    "v_exp_f32 v217, v217\n"
    "v_smfmac_f32_32x32x32_bf16 v[232:247], v[184:187], v[208:215], v252\n"
    "v_exp_f32 v218, v218\n"
    "v_exp_f32 v219, v219\n"
    "v_exp_f32 v220, v220\n"
    "v_mfma_f32_32x32x16_bf16 v[200:215], v[64:67], v[248:251], 0\n"
    "v_exp_f32 v221, v221\n"
    "v_exp_f32 v222, v222\n"
    "v_exp_f32 v223, v223\n"
    "v_exp_f32 v224, v224\n"
    "v_exp_f32 v225, v225\n"
    "v_smfmac_f32_32x32x32_bf16 v[232:247], v[124:127], v[216:223], v252\n"
    "v_exp_f32 v226, v226\n"
    "v_exp_f32 v227, v227\n"
    "v_exp_f32 v228, v228\n"
    "v_exp_f32 v229, v229\n"
    "v_exp_f32 v230, v230\n"
    "v_exp_f32 v231, v231\n"
    "v_exp_f32 v200, v200\n"
    "v_exp_f32 v201, v201\n"
    "v_smfmac_f32_32x32x32_bf16 v[232:247], v[188:191], v[224:231], v252\n"
    "v_exp_f32 v202, v202\n"
    "v_exp_f32 v203, v203\n"
    "v_exp_f32 v204, v204\n"
    "v_mfma_f32_32x32x16_bf16 v[216:231], v[68:71], v[248:251], 0\n"
    "ds_read_b128 v[248:251], v2 offset:4096\n"
    "v_exp_f32 v205, v205\n"
    "v_exp_f32 v206, v206\n"
    "v_exp_f32 v207, v207\n"
    "v_exp_f32 v208, v208\n"
    "v_exp_f32 v209, v209\n"
    "v_smfmac_f32_32x32x32_bf16 v[232:247], v[128:131], v[200:207], v252\n"
    "v_exp_f32 v210, v210\n"
    "v_exp_f32 v211, v211\n"
    "v_exp_f32 v212, v212\n"
    "v_exp_f32 v213, v213\n"
    "v_exp_f32 v214, v214\n"
    "v_exp_f32 v215, v215\n"
    "v_exp_f32 v216, v216\n"
    "v_exp_f32 v217, v217\n"
    "v_smfmac_f32_32x32x32_bf16 v[232:247], v[192:195], v[208:215], v252\n"
    "v_exp_f32 v218, v218\n"
    "v_exp_f32 v219, v219\n"
    "v_exp_f32 v220, v220\n"
    "s_waitcnt lgkmcnt(0)\n"
    "v_mfma_f32_32x32x16_bf16 v[200:215], v[8:11], v[248:251], 0\n"
    "v_exp_f32 v221, v221\n"
    "v_exp_f32 v222, v222\n"
    "v_exp_f32 v223, v223\n"
    "v_exp_f32 v224, v224\n"
    "v_exp_f32 v225, v225\n"
    "v_smfmac_f32_32x32x32_bf16 v[232:247], v[132:135], v[216:223], v252\n"
    "v_exp_f32 v226, v226\n"
    "v_exp_f32 v227, v227\n"
    "v_exp_f32 v228, v228\n"
    "v_exp_f32 v229, v229\n"
    "v_exp_f32 v230, v230\n"
    "v_exp_f32 v231, v231\n"
    "v_exp_f32 v200, v200\n"
    "v_exp_f32 v201, v201\n"
    "v_smfmac_f32_32x32x32_bf16 v[232:247], v[196:199], v[224:231], v252\n"
    "v_exp_f32 v202, v202\n"
    "v_exp_f32 v203, v203\n"
    "v_exp_f32 v204, v204\n"
    "v_mfma_f32_32x32x16_bf16 v[216:231], v[12:15], v[248:251], 0\n"
    "v_exp_f32 v205, v205\n"
    "v_exp_f32 v206, v206\n"
    "v_exp_f32 v207, v207\n"
    "v_exp_f32 v208, v208\n"
    "v_exp_f32 v209, v209\n"
    "v_exp_f32 v210, v210\n"
    "v_exp_f32 v211, v211\n"
    "v_exp_f32 v212, v212\n"
    "ds_write_b128 v3, v[232:235] offset:27648\n"
    "ds_write_b128 v3, v[236:239] offset:28672\n"
    "ds_write_b32 v4, v240 offset:27648\n"
    "v_mov_b32 v255, 4\n"
    "ds_write_b8 v5, v255 offset:36\n"
    "v_mov_b64 v[232:233], 0\n"
    "v_mov_b64 v[234:235], 0\n"
    "v_mov_b64 v[236:237], 0\n"
    "v_mov_b64 v[238:239], 0\n"
    "v_mov_b64 v[240:241], 0\n"
    "v_exp_f32 v213, v213\n"
    "v_exp_f32 v214, v214\n"
    "v_exp_f32 v215, v215\n"
    "v_smfmac_f32_32x32x32_bf16 v[232:247], v[72:75], v[200:207], v252\n"
    "v_exp_f32 v216, v216\n"
    "v_exp_f32 v217, v217\n"
    "v_smfmac_f32_32x32x32_bf16 v[232:247], v[136:139], v[208:215], v252\n"
    "v_exp_f32 v218, v218\n"
    "v_exp_f32 v219, v219\n"
    "v_exp_f32 v220, v220\n"
    "v_mfma_f32_32x32x16_bf16 v[200:215], v[16:19], v[248:251], 0\n"
    "v_exp_f32 v221, v221\n"
    "v_exp_f32 v222, v222\n"
    "v_exp_f32 v223, v223\n"
    "v_exp_f32 v224, v224\n"
    "v_exp_f32 v225, v225\n"
    "v_smfmac_f32_32x32x32_bf16 v[232:247], v[76:79], v[216:223], v252\n"
    "v_exp_f32 v226, v226\n"
    "v_exp_f32 v227, v227\n"
    "v_exp_f32 v228, v228\n"
    "v_exp_f32 v229, v229\n"
    "v_exp_f32 v230, v230\n"
    "v_exp_f32 v231, v231\n"
    "v_exp_f32 v200, v200\n"
    "v_exp_f32 v201, v201\n"
    "v_smfmac_f32_32x32x32_bf16 v[232:247], v[140:143], v[224:231], v252\n"
    "v_exp_f32 v202, v202\n"
    "v_exp_f32 v203, v203\n"
    "v_exp_f32 v204, v204\n"
    "v_mfma_f32_32x32x16_bf16 v[216:231], v[20:23], v[248:251], 0\n"
    "v_exp_f32 v205, v205\n"
    "v_exp_f32 v206, v206\n"
    "v_exp_f32 v207, v207\n"
    "v_exp_f32 v208, v208\n"
    "v_exp_f32 v209, v209\n"
    "v_smfmac_f32_32x32x32_bf16 v[232:247], v[80:83], v[200:207], v252\n"
    "v_exp_f32 v210, v210\n"
    "v_exp_f32 v211, v211\n"
    "v_exp_f32 v212, v212\n"
    "v_exp_f32 v213, v213\n"
    "v_exp_f32 v214, v214\n"
    "v_exp_f32 v215, v215\n"
    "v_exp_f32 v216, v216\n"
    "v_exp_f32 v217, v217\n"
    "v_smfmac_f32_32x32x32_bf16 v[232:247], v[144:147], v[208:215], v252\n"
    "v_exp_f32 v218, v218\n"
    "v_exp_f32 v219, v219\n"
    "v_exp_f32 v220, v220\n"
    "v_mfma_f32_32x32x16_bf16 v[200:215], v[24:27], v[248:251], 0\n"
    "v_exp_f32 v221, v221\n"
    "v_exp_f32 v222, v222\n"
    "v_exp_f32 v223, v223\n"
    "v_exp_f32 v224, v224\n"
    "v_exp_f32 v225, v225\n"
    "v_smfmac_f32_32x32x32_bf16 v[232:247], v[84:87], v[216:223], v252\n"
    "v_exp_f32 v226, v226\n"
    "v_exp_f32 v227, v227\n"
    "v_exp_f32 v228, v228\n"
    "v_exp_f32 v229, v229\n"
    "v_exp_f32 v230, v230\n"
    "v_exp_f32 v231, v231\n"
    "v_exp_f32 v200, v200\n"
    "v_exp_f32 v201, v201\n"
    "v_smfmac_f32_32x32x32_bf16 v[232:247], v[148:151], v[224:231], v252\n"
    "v_exp_f32 v202, v202\n"
    "v_exp_f32 v203, v203\n"
    "v_exp_f32 v204, v204\n"
    "v_mfma_f32_32x32x16_bf16 v[216:231], v[28:31], v[248:251], 0\n"
    "v_exp_f32 v205, v205\n"
    "v_exp_f32 v206, v206\n"
    "v_exp_f32 v207, v207\n"
    "v_exp_f32 v208, v208\n"
    "v_exp_f32 v209, v209\n"
    "v_smfmac_f32_32x32x32_bf16 v[232:247], v[88:91], v[200:207], v252\n"
    "v_exp_f32 v210, v210\n"
    "v_exp_f32 v211, v211\n"
    "v_exp_f32 v212, v212\n"
    "v_exp_f32 v213, v213\n"
    "v_exp_f32 v214, v214\n"
    "v_exp_f32 v215, v215\n"
    "v_exp_f32 v216, v216\n"
    "v_exp_f32 v217, v217\n"
    "v_smfmac_f32_32x32x32_bf16 v[232:247], v[152:155], v[208:215], v252\n"
    "v_exp_f32 v218, v218\n"
    "v_exp_f32 v219, v219\n"
    "v_exp_f32 v220, v220\n"
    "v_mfma_f32_32x32x16_bf16 v[200:215], v[32:35], v[248:251], 0\n"
    "v_exp_f32 v221, v221\n"
    "v_exp_f32 v222, v222\n"
    "v_exp_f32 v223, v223\n"
    "v_exp_f32 v224, v224\n"
    "v_exp_f32 v225, v225\n"
    "v_smfmac_f32_32x32x32_bf16 v[232:247], v[92:95], v[216:223], v252\n"
    "v_exp_f32 v226, v226\n"
    "v_exp_f32 v227, v227\n"
    "v_exp_f32 v228, v228\n"
    "v_exp_f32 v229, v229\n"
    "v_exp_f32 v230, v230\n"
    "v_exp_f32 v231, v231\n"
    "v_exp_f32 v200, v200\n"
    "v_exp_f32 v201, v201\n"
    "v_smfmac_f32_32x32x32_bf16 v[232:247], v[156:159], v[224:231], v252\n"
    "v_exp_f32 v202, v202\n"
    "v_exp_f32 v203, v203\n"
    "v_exp_f32 v204, v204\n"
    "v_mfma_f32_32x32x16_bf16 v[216:231], v[36:39], v[248:251], 0\n"
    "v_exp_f32 v205, v205\n"
    "v_exp_f32 v206, v206\n"
    "v_exp_f32 v207, v207\n"
    "v_exp_f32 v208, v208\n"
    "v_exp_f32 v209, v209\n"
    "v_smfmac_f32_32x32x32_bf16 v[232:247], v[96:99], v[200:207], v252\n"
    "v_exp_f32 v210, v210\n"
    "v_exp_f32 v211, v211\n"
    "v_exp_f32 v212, v212\n"
    "v_exp_f32 v213, v213\n"
    "v_exp_f32 v214, v214\n"
    "v_exp_f32 v215, v215\n"
    "v_exp_f32 v216, v216\n"
    "v_exp_f32 v217, v217\n"
    "v_smfmac_f32_32x32x32_bf16 v[232:247], v[160:163], v[208:215], v252\n"
    "v_exp_f32 v218, v218\n"
    "v_exp_f32 v219, v219\n"
    "v_exp_f32 v220, v220\n"
    "v_mfma_f32_32x32x16_bf16 v[200:215], v[40:43], v[248:251], 0\n"
    "v_exp_f32 v221, v221\n"
    "v_exp_f32 v222, v222\n"
    "v_exp_f32 v223, v223\n"
    "v_exp_f32 v224, v224\n"
    "v_exp_f32 v225, v225\n"
    "v_smfmac_f32_32x32x32_bf16 v[232:247], v[100:103], v[216:223], v252\n"
    "v_exp_f32 v226, v226\n"
    "v_exp_f32 v227, v227\n"
    "v_exp_f32 v228, v228\n"
    "v_exp_f32 v229, v229\n"
    "v_exp_f32 v230, v230\n"
    "v_exp_f32 v231, v231\n"
    "v_exp_f32 v200, v200\n"
    "v_exp_f32 v201, v201\n"
    "v_smfmac_f32_32x32x32_bf16 v[232:247], v[164:167], v[224:231], v252\n"
    "v_exp_f32 v202, v202\n"
    "v_exp_f32 v203, v203\n"
    "v_exp_f32 v204, v204\n"
    "v_mfma_f32_32x32x16_bf16 v[216:231], v[44:47], v[248:251], 0\n"
    "v_exp_f32 v205, v205\n"
    "v_exp_f32 v206, v206\n"
    "v_exp_f32 v207, v207\n"
    "v_exp_f32 v208, v208\n"
    "v_exp_f32 v209, v209\n"
    "v_smfmac_f32_32x32x32_bf16 v[232:247], v[104:107], v[200:207], v252\n"
    "v_exp_f32 v210, v210\n"
    "v_exp_f32 v211, v211\n"
    "v_exp_f32 v212, v212\n"
    "v_exp_f32 v213, v213\n"
    "v_exp_f32 v214, v214\n"
    "v_exp_f32 v215, v215\n"
    "v_exp_f32 v216, v216\n"
    "v_exp_f32 v217, v217\n"
    "v_smfmac_f32_32x32x32_bf16 v[232:247], v[168:171], v[208:215], v252\n"
    "v_exp_f32 v218, v218\n"
    "v_exp_f32 v219, v219\n"
    "v_exp_f32 v220, v220\n"
    "v_mfma_f32_32x32x16_bf16 v[200:215], v[48:51], v[248:251], 0\n"
    "v_exp_f32 v221, v221\n"
    "v_exp_f32 v222, v222\n"
    "v_exp_f32 v223, v223\n"
    "v_exp_f32 v224, v224\n"
    "v_exp_f32 v225, v225\n"
    "v_smfmac_f32_32x32x32_bf16 v[232:247], v[108:111], v[216:223], v252\n"
    "v_exp_f32 v226, v226\n"
    "v_exp_f32 v227, v227\n"
    "v_exp_f32 v228, v228\n"
    "v_exp_f32 v229, v229\n"
    "v_exp_f32 v230, v230\n"
    "v_exp_f32 v231, v231\n"
    "v_exp_f32 v200, v200\n"
    "v_exp_f32 v201, v201\n"
    "v_smfmac_f32_32x32x32_bf16 v[232:247], v[172:175], v[224:231], v252\n"
    "v_exp_f32 v202, v202\n"
    "v_exp_f32 v203, v203\n"
    "v_exp_f32 v204, v204\n"
    "v_mfma_f32_32x32x16_bf16 v[216:231], v[52:55], v[248:251], 0\n"
    "v_exp_f32 v205, v205\n"
    "v_exp_f32 v206, v206\n"
    "v_exp_f32 v207, v207\n"
    "v_exp_f32 v208, v208\n"
    "v_exp_f32 v209, v209\n"
    "v_smfmac_f32_32x32x32_bf16 v[232:247], v[112:115], v[200:207], v252\n"
    "v_exp_f32 v210, v210\n"
    "v_exp_f32 v211, v211\n"
    "v_exp_f32 v212, v212\n"
    "v_exp_f32 v213, v213\n"
    "v_exp_f32 v214, v214\n"
    "v_exp_f32 v215, v215\n"
    "v_exp_f32 v216, v216\n"
    "v_exp_f32 v217, v217\n"
    "v_smfmac_f32_32x32x32_bf16 v[232:247], v[176:179], v[208:215], v252\n"
    "v_exp_f32 v218, v218\n"
    "v_exp_f32 v219, v219\n"
    "v_exp_f32 v220, v220\n"
    "v_mfma_f32_32x32x16_bf16 v[200:215], v[56:59], v[248:251], 0\n"
    "v_exp_f32 v221, v221\n"
    "v_exp_f32 v222, v222\n"
    "v_exp_f32 v223, v223\n"
    "v_exp_f32 v224, v224\n"
    "v_exp_f32 v225, v225\n"
    "v_smfmac_f32_32x32x32_bf16 v[232:247], v[116:119], v[216:223], v252\n"
    "v_exp_f32 v226, v226\n"
    "v_exp_f32 v227, v227\n"
    "v_exp_f32 v228, v228\n"
    "v_exp_f32 v229, v229\n"
    "v_exp_f32 v230, v230\n"
    "v_exp_f32 v231, v231\n"
    "v_exp_f32 v200, v200\n"
    "v_exp_f32 v201, v201\n"
    "v_smfmac_f32_32x32x32_bf16 v[232:247], v[180:183], v[224:231], v252\n"
    "v_exp_f32 v202, v202\n"
    "v_exp_f32 v203, v203\n"
    "v_exp_f32 v204, v204\n"
    "v_mfma_f32_32x32x16_bf16 v[216:231], v[60:63], v[248:251], 0\n"
    "v_exp_f32 v205, v205\n"
    "v_exp_f32 v206, v206\n"
    "v_exp_f32 v207, v207\n"
    "v_exp_f32 v208, v208\n"
    "v_exp_f32 v209, v209\n"
    "v_smfmac_f32_32x32x32_bf16 v[232:247], v[120:123], v[200:207], v252\n"
    "v_exp_f32 v210, v210\n"
    "v_exp_f32 v211, v211\n"
    "v_exp_f32 v212, v212\n"
    "v_exp_f32 v213, v213\n"
    "v_exp_f32 v214, v214\n"
    "v_exp_f32 v215, v215\n"
    "v_exp_f32 v216, v216\n"
    "v_exp_f32 v217, v217\n"
    "v_smfmac_f32_32x32x32_bf16 v[232:247], v[184:187], v[208:215], v252\n"
    "v_exp_f32 v218, v218\n"
    "v_exp_f32 v219, v219\n"
    "v_exp_f32 v220, v220\n"
    "v_mfma_f32_32x32x16_bf16 v[200:215], v[64:67], v[248:251], 0\n"
    "v_exp_f32 v221, v221\n"
    "v_exp_f32 v222, v222\n"
    "v_exp_f32 v223, v223\n"
    "v_exp_f32 v224, v224\n"
    "v_exp_f32 v225, v225\n"
    "v_smfmac_f32_32x32x32_bf16 v[232:247], v[124:127], v[216:223], v252\n"
    "v_exp_f32 v226, v226\n"
    "v_exp_f32 v227, v227\n"
    "v_exp_f32 v228, v228\n"
    "v_exp_f32 v229, v229\n"
    "v_exp_f32 v230, v230\n"
    "v_exp_f32 v231, v231\n"
    "v_exp_f32 v200, v200\n"
    "v_exp_f32 v201, v201\n"
    "v_smfmac_f32_32x32x32_bf16 v[232:247], v[188:191], v[224:231], v252\n"
    "v_exp_f32 v202, v202\n"
    "v_exp_f32 v203, v203\n"
    "v_exp_f32 v204, v204\n"
    "v_mfma_f32_32x32x16_bf16 v[216:231], v[68:71], v[248:251], 0\n"
    "ds_read_b128 v[248:251], v2 offset:5120\n"
    "ds_read_b32 v255, v7 offset:64\n"
    "v_exp_f32 v205, v205\n"
    "v_exp_f32 v206, v206\n"
    "v_exp_f32 v207, v207\n"
    "v_exp_f32 v208, v208\n"
    "v_exp_f32 v209, v209\n"
    "v_smfmac_f32_32x32x32_bf16 v[232:247], v[128:131], v[200:207], v252\n"
    "v_exp_f32 v210, v210\n"
    "v_exp_f32 v211, v211\n"
    "v_exp_f32 v212, v212\n"
    "v_exp_f32 v213, v213\n"
    "v_exp_f32 v214, v214\n"
    "v_exp_f32 v215, v215\n"
    "v_exp_f32 v216, v216\n"
    "v_exp_f32 v217, v217\n"
    "v_smfmac_f32_32x32x32_bf16 v[232:247], v[192:195], v[208:215], v252\n"
    "v_exp_f32 v218, v218\n"
    "v_exp_f32 v219, v219\n"
    "v_exp_f32 v220, v220\n"
    "s_waitcnt lgkmcnt(0)\n"
    "v_cmp_ne_u32 vcc, 0, v255\n"
    "s_cbranch_vccz .L_ofw4_k\n"
    ".L_ofw4r_k:\n"
    "v_mfma_f32_32x32x16_bf16 v[200:215], v[8:11], v[248:251], 0\n"
    "v_exp_f32 v221, v221\n"
    "v_exp_f32 v222, v222\n"
    "v_exp_f32 v223, v223\n"
    "v_exp_f32 v224, v224\n"
    "v_exp_f32 v225, v225\n"
    "v_smfmac_f32_32x32x32_bf16 v[232:247], v[132:135], v[216:223], v252\n"
    "v_exp_f32 v226, v226\n"
    "v_exp_f32 v227, v227\n"
    "v_exp_f32 v228, v228\n"
    "v_exp_f32 v229, v229\n"
    "v_exp_f32 v230, v230\n"
    "v_exp_f32 v231, v231\n"
    "v_exp_f32 v200, v200\n"
    "v_exp_f32 v201, v201\n"
    "v_smfmac_f32_32x32x32_bf16 v[232:247], v[196:199], v[224:231], v252\n"
    "v_exp_f32 v202, v202\n"
    "v_exp_f32 v203, v203\n"
    "v_exp_f32 v204, v204\n"
    "v_mfma_f32_32x32x16_bf16 v[216:231], v[12:15], v[248:251], 0\n"
    "v_exp_f32 v205, v205\n"
    "v_exp_f32 v206, v206\n"
    "v_exp_f32 v207, v207\n"
    "v_exp_f32 v208, v208\n"
    "v_exp_f32 v209, v209\n"
    "v_exp_f32 v210, v210\n"
    "v_exp_f32 v211, v211\n"
    "v_exp_f32 v212, v212\n"
    "ds_write_b128 v3, v[232:235] offset:0\n"
    "ds_write_b128 v3, v[236:239] offset:1024\n"
    "ds_write_b32 v4, v240 offset:0\n"
    "v_mov_b32 v255, 5\n"
    "ds_write_b8 v5, v255 offset:36\n"
    "v_mov_b64 v[232:233], 0\n"
    "v_mov_b64 v[234:235], 0\n"
    "v_mov_b64 v[236:237], 0\n"
    "v_mov_b64 v[238:239], 0\n"
    "v_mov_b64 v[240:241], 0\n"
    "v_exp_f32 v213, v213\n"
    "v_exp_f32 v214, v214\n"
    "v_exp_f32 v215, v215\n"
    "v_smfmac_f32_32x32x32_bf16 v[232:247], v[72:75], v[200:207], v252\n"
    "v_exp_f32 v216, v216\n"
    "v_exp_f32 v217, v217\n"
    "v_smfmac_f32_32x32x32_bf16 v[232:247], v[136:139], v[208:215], v252\n"
    "v_exp_f32 v218, v218\n"
    "v_exp_f32 v219, v219\n"
    "v_exp_f32 v220, v220\n"
    "v_mfma_f32_32x32x16_bf16 v[200:215], v[16:19], v[248:251], 0\n"
    "v_exp_f32 v221, v221\n"
    "v_exp_f32 v222, v222\n"
    "v_exp_f32 v223, v223\n"
    "v_exp_f32 v224, v224\n"
    "v_exp_f32 v225, v225\n"
    "v_smfmac_f32_32x32x32_bf16 v[232:247], v[76:79], v[216:223], v252\n"
    "v_exp_f32 v226, v226\n"
    "v_exp_f32 v227, v227\n"
    "v_exp_f32 v228, v228\n"
    "v_exp_f32 v229, v229\n"
    "v_exp_f32 v230, v230\n"
    "v_exp_f32 v231, v231\n"
    "v_exp_f32 v200, v200\n"
    "v_exp_f32 v201, v201\n"
    "v_smfmac_f32_32x32x32_bf16 v[232:247], v[140:143], v[224:231], v252\n"
    "v_exp_f32 v202, v202\n"
    "v_exp_f32 v203, v203\n"
    "v_exp_f32 v204, v204\n"
    "v_mfma_f32_32x32x16_bf16 v[216:231], v[20:23], v[248:251], 0\n"
    "v_exp_f32 v205, v205\n"
    "v_exp_f32 v206, v206\n"
    "v_exp_f32 v207, v207\n"
    "v_exp_f32 v208, v208\n"
    "v_exp_f32 v209, v209\n"
    "v_smfmac_f32_32x32x32_bf16 v[232:247], v[80:83], v[200:207], v252\n"
    "v_exp_f32 v210, v210\n"
    "v_exp_f32 v211, v211\n"
    "v_exp_f32 v212, v212\n"
    "v_exp_f32 v213, v213\n"
    "v_exp_f32 v214, v214\n"
    "v_exp_f32 v215, v215\n"
    "v_exp_f32 v216, v216\n"
    "v_exp_f32 v217, v217\n"
    "v_smfmac_f32_32x32x32_bf16 v[232:247], v[144:147], v[208:215], v252\n"
    "v_exp_f32 v218, v218\n"
    "v_exp_f32 v219, v219\n"
    "v_exp_f32 v220, v220\n"
    "v_mfma_f32_32x32x16_bf16 v[200:215], v[24:27], v[248:251], 0\n"
    "v_exp_f32 v221, v221\n"
    "v_exp_f32 v222, v222\n"
    "v_exp_f32 v223, v223\n"
    "v_exp_f32 v224, v224\n"
    "v_exp_f32 v225, v225\n"
    "v_smfmac_f32_32x32x32_bf16 v[232:247], v[84:87], v[216:223], v252\n"
    "v_exp_f32 v226, v226\n"
    "v_exp_f32 v227, v227\n"
    "v_exp_f32 v228, v228\n"
    "v_exp_f32 v229, v229\n"
    "v_exp_f32 v230, v230\n"
    "v_exp_f32 v231, v231\n"
    "v_exp_f32 v200, v200\n"
    "v_exp_f32 v201, v201\n"
    "v_smfmac_f32_32x32x32_bf16 v[232:247], v[148:151], v[224:231], v252\n"
    "v_exp_f32 v202, v202\n"
    "v_exp_f32 v203, v203\n"
    "v_exp_f32 v204, v204\n"
    "v_mfma_f32_32x32x16_bf16 v[216:231], v[28:31], v[248:251], 0\n"
    "v_exp_f32 v205, v205\n"
    "v_exp_f32 v206, v206\n"
    "v_exp_f32 v207, v207\n"
    "v_exp_f32 v208, v208\n"
    "v_exp_f32 v209, v209\n"
    "v_smfmac_f32_32x32x32_bf16 v[232:247], v[88:91], v[200:207], v252\n"
    "v_exp_f32 v210, v210\n"
    "v_exp_f32 v211, v211\n"
    "v_exp_f32 v212, v212\n"
    "v_exp_f32 v213, v213\n"
    "v_exp_f32 v214, v214\n"
    "v_exp_f32 v215, v215\n"
    "v_exp_f32 v216, v216\n"
    "v_exp_f32 v217, v217\n"
    "v_smfmac_f32_32x32x32_bf16 v[232:247], v[152:155], v[208:215], v252\n"
    "v_exp_f32 v218, v218\n"
    "v_exp_f32 v219, v219\n"
    "v_exp_f32 v220, v220\n"
    "v_mfma_f32_32x32x16_bf16 v[200:215], v[32:35], v[248:251], 0\n"
    "v_exp_f32 v221, v221\n"
    "v_exp_f32 v222, v222\n"
    "v_exp_f32 v223, v223\n"
    "v_exp_f32 v224, v224\n"
    "v_exp_f32 v225, v225\n"
    "v_smfmac_f32_32x32x32_bf16 v[232:247], v[92:95], v[216:223], v252\n"
    "v_exp_f32 v226, v226\n"
    "v_exp_f32 v227, v227\n"
    "v_exp_f32 v228, v228\n"
    "v_exp_f32 v229, v229\n"
    "v_exp_f32 v230, v230\n"
    "v_exp_f32 v231, v231\n"
    "v_exp_f32 v200, v200\n"
    "v_exp_f32 v201, v201\n"
    "v_smfmac_f32_32x32x32_bf16 v[232:247], v[156:159], v[224:231], v252\n"
    "v_exp_f32 v202, v202\n"
    "v_exp_f32 v203, v203\n"
    "v_exp_f32 v204, v204\n"
    "v_mfma_f32_32x32x16_bf16 v[216:231], v[36:39], v[248:251], 0\n"
    "v_exp_f32 v205, v205\n"
    "v_exp_f32 v206, v206\n"
    "v_exp_f32 v207, v207\n"
    "v_exp_f32 v208, v208\n"
    "v_exp_f32 v209, v209\n"
    "v_smfmac_f32_32x32x32_bf16 v[232:247], v[96:99], v[200:207], v252\n"
    "v_exp_f32 v210, v210\n"
    "v_exp_f32 v211, v211\n"
    "v_exp_f32 v212, v212\n"
    "v_exp_f32 v213, v213\n"
    "v_exp_f32 v214, v214\n"
    "v_exp_f32 v215, v215\n"
    "v_exp_f32 v216, v216\n"
    "v_exp_f32 v217, v217\n"
    "v_smfmac_f32_32x32x32_bf16 v[232:247], v[160:163], v[208:215], v252\n"
    "v_exp_f32 v218, v218\n"
    "v_exp_f32 v219, v219\n"
    "v_exp_f32 v220, v220\n"
    "v_mfma_f32_32x32x16_bf16 v[200:215], v[40:43], v[248:251], 0\n"
    "v_exp_f32 v221, v221\n"
    "v_exp_f32 v222, v222\n"
    "v_exp_f32 v223, v223\n"
    "v_exp_f32 v224, v224\n"
    "v_exp_f32 v225, v225\n"
    "v_smfmac_f32_32x32x32_bf16 v[232:247], v[100:103], v[216:223], v252\n"
    "v_exp_f32 v226, v226\n"
    "v_exp_f32 v227, v227\n"
    "v_exp_f32 v228, v228\n"
    "v_exp_f32 v229, v229\n"
    "v_exp_f32 v230, v230\n"
    "v_exp_f32 v231, v231\n"
    "v_exp_f32 v200, v200\n"
    "v_exp_f32 v201, v201\n"
    "v_smfmac_f32_32x32x32_bf16 v[232:247], v[164:167], v[224:231], v252\n"
    "v_exp_f32 v202, v202\n"
    "v_exp_f32 v203, v203\n"
    "v_exp_f32 v204, v204\n"
    "v_mfma_f32_32x32x16_bf16 v[216:231], v[44:47], v[248:251], 0\n"
    "v_exp_f32 v205, v205\n"
    "v_exp_f32 v206, v206\n"
    "v_exp_f32 v207, v207\n"
    "v_exp_f32 v208, v208\n"
    "v_exp_f32 v209, v209\n"
    "v_smfmac_f32_32x32x32_bf16 v[232:247], v[104:107], v[200:207], v252\n"
    "v_exp_f32 v210, v210\n"
    "v_exp_f32 v211, v211\n"
    "v_exp_f32 v212, v212\n"
    "v_exp_f32 v213, v213\n"
    "v_exp_f32 v214, v214\n"
    "v_exp_f32 v215, v215\n"
    "v_exp_f32 v216, v216\n"
    "v_exp_f32 v217, v217\n"
    "v_smfmac_f32_32x32x32_bf16 v[232:247], v[168:171], v[208:215], v252\n"
    "v_exp_f32 v218, v218\n"
    "v_exp_f32 v219, v219\n"
    "v_exp_f32 v220, v220\n"
    "v_mfma_f32_32x32x16_bf16 v[200:215], v[48:51], v[248:251], 0\n"
    "v_exp_f32 v221, v221\n"
    "v_exp_f32 v222, v222\n"
    "v_exp_f32 v223, v223\n"
    "v_exp_f32 v224, v224\n"
    "v_exp_f32 v225, v225\n"
    "v_smfmac_f32_32x32x32_bf16 v[232:247], v[108:111], v[216:223], v252\n"
    "v_exp_f32 v226, v226\n"
    "v_exp_f32 v227, v227\n"
    "v_exp_f32 v228, v228\n"
    "v_exp_f32 v229, v229\n"
    "v_exp_f32 v230, v230\n"
    "v_exp_f32 v231, v231\n"
    "v_exp_f32 v200, v200\n"
    "v_exp_f32 v201, v201\n"
    "v_smfmac_f32_32x32x32_bf16 v[232:247], v[172:175], v[224:231], v252\n"
    "v_exp_f32 v202, v202\n"
    "v_exp_f32 v203, v203\n"
    "v_exp_f32 v204, v204\n"
    "v_mfma_f32_32x32x16_bf16 v[216:231], v[52:55], v[248:251], 0\n"
    "v_exp_f32 v205, v205\n"
    "v_exp_f32 v206, v206\n"
    "v_exp_f32 v207, v207\n"
    "v_exp_f32 v208, v208\n"
    "v_exp_f32 v209, v209\n"
    "v_smfmac_f32_32x32x32_bf16 v[232:247], v[112:115], v[200:207], v252\n"
    "v_exp_f32 v210, v210\n"
    "v_exp_f32 v211, v211\n"
    "v_exp_f32 v212, v212\n"
    "v_exp_f32 v213, v213\n"
    "v_exp_f32 v214, v214\n"
    "v_exp_f32 v215, v215\n"
    "v_exp_f32 v216, v216\n"
    "v_exp_f32 v217, v217\n"
    "v_smfmac_f32_32x32x32_bf16 v[232:247], v[176:179], v[208:215], v252\n"
    "v_exp_f32 v218, v218\n"
    "v_exp_f32 v219, v219\n"
    "v_exp_f32 v220, v220\n"
    "v_mfma_f32_32x32x16_bf16 v[200:215], v[56:59], v[248:251], 0\n"
    "v_exp_f32 v221, v221\n"
    "v_exp_f32 v222, v222\n"
    "v_exp_f32 v223, v223\n"
    "v_exp_f32 v224, v224\n"
    "v_exp_f32 v225, v225\n"
    "v_smfmac_f32_32x32x32_bf16 v[232:247], v[116:119], v[216:223], v252\n"
    "v_exp_f32 v226, v226\n"
    "v_exp_f32 v227, v227\n"
    "v_exp_f32 v228, v228\n"
    "v_exp_f32 v229, v229\n"
    "v_exp_f32 v230, v230\n"
    "v_exp_f32 v231, v231\n"
    "v_exp_f32 v200, v200\n"
    "v_exp_f32 v201, v201\n"
    "v_smfmac_f32_32x32x32_bf16 v[232:247], v[180:183], v[224:231], v252\n"
    "v_exp_f32 v202, v202\n"
    "v_exp_f32 v203, v203\n"
    "v_exp_f32 v204, v204\n"
    "v_mfma_f32_32x32x16_bf16 v[216:231], v[60:63], v[248:251], 0\n"
    "v_exp_f32 v205, v205\n"
    "v_exp_f32 v206, v206\n"
    "v_exp_f32 v207, v207\n"
    "v_exp_f32 v208, v208\n"
    "v_exp_f32 v209, v209\n"
    "v_smfmac_f32_32x32x32_bf16 v[232:247], v[120:123], v[200:207], v252\n"
    "v_exp_f32 v210, v210\n"
    "v_exp_f32 v211, v211\n"
    "v_exp_f32 v212, v212\n"
    "v_exp_f32 v213, v213\n"
    "v_exp_f32 v214, v214\n"
    "v_exp_f32 v215, v215\n"
    "v_exp_f32 v216, v216\n"
    "v_exp_f32 v217, v217\n"
    "v_smfmac_f32_32x32x32_bf16 v[232:247], v[184:187], v[208:215], v252\n"
    "v_exp_f32 v218, v218\n"
    "v_exp_f32 v219, v219\n"
    "v_exp_f32 v220, v220\n"
    "v_mfma_f32_32x32x16_bf16 v[200:215], v[64:67], v[248:251], 0\n"
    "v_exp_f32 v221, v221\n"
    "v_exp_f32 v222, v222\n"
    "v_exp_f32 v223, v223\n"
    "v_exp_f32 v224, v224\n"
    "v_exp_f32 v225, v225\n"
    "v_smfmac_f32_32x32x32_bf16 v[232:247], v[124:127], v[216:223], v252\n"
    "v_exp_f32 v226, v226\n"
    "v_exp_f32 v227, v227\n"
    "v_exp_f32 v228, v228\n"
    "v_exp_f32 v229, v229\n"
    "v_exp_f32 v230, v230\n"
    "v_exp_f32 v231, v231\n"
    "v_exp_f32 v200, v200\n"
    "v_exp_f32 v201, v201\n"
    "v_smfmac_f32_32x32x32_bf16 v[232:247], v[188:191], v[224:231], v252\n"
    "v_exp_f32 v202, v202\n"
    "v_exp_f32 v203, v203\n"
    "v_exp_f32 v204, v204\n"
    "v_mfma_f32_32x32x16_bf16 v[216:231], v[68:71], v[248:251], 0\n"
    "ds_read_b128 v[248:251], v2 offset:6144\n"
    "ds_read_b32 v255, v7 offset:68\n"
    "v_exp_f32 v205, v205\n"
    "v_exp_f32 v206, v206\n"
    "v_exp_f32 v207, v207\n"
    "v_exp_f32 v208, v208\n"
    "v_exp_f32 v209, v209\n"
    "v_smfmac_f32_32x32x32_bf16 v[232:247], v[128:131], v[200:207], v252\n"
    "v_exp_f32 v210, v210\n"
    "v_exp_f32 v211, v211\n"
    "v_exp_f32 v212, v212\n"
    "v_exp_f32 v213, v213\n"
    "v_exp_f32 v214, v214\n"
    "v_exp_f32 v215, v215\n"
    "v_exp_f32 v216, v216\n"
    "v_exp_f32 v217, v217\n"
    "v_smfmac_f32_32x32x32_bf16 v[232:247], v[192:195], v[208:215], v252\n"
    "v_exp_f32 v218, v218\n"
    "v_exp_f32 v219, v219\n"
    "v_exp_f32 v220, v220\n"
    "s_waitcnt lgkmcnt(0)\n"
    "v_cmp_ne_u32 vcc, 0, v255\n"
    "s_cbranch_vccz .L_ofw5_k\n"
    ".L_ofw5r_k:\n"
    "v_mfma_f32_32x32x16_bf16 v[200:215], v[8:11], v[248:251], 0\n"
    "v_exp_f32 v221, v221\n"
    "v_exp_f32 v222, v222\n"
    "v_exp_f32 v223, v223\n"
    "v_exp_f32 v224, v224\n"
    "v_exp_f32 v225, v225\n"
    "v_smfmac_f32_32x32x32_bf16 v[232:247], v[132:135], v[216:223], v252\n"
    "v_exp_f32 v226, v226\n"
    "v_exp_f32 v227, v227\n"
    "v_exp_f32 v228, v228\n"
    "v_exp_f32 v229, v229\n"
    "v_exp_f32 v230, v230\n"
    "v_exp_f32 v231, v231\n"
    "v_exp_f32 v200, v200\n"
    "v_exp_f32 v201, v201\n"
    "v_smfmac_f32_32x32x32_bf16 v[232:247], v[196:199], v[224:231], v252\n"
    "v_exp_f32 v202, v202\n"
    "v_exp_f32 v203, v203\n"
    "v_exp_f32 v204, v204\n"
    "v_mfma_f32_32x32x16_bf16 v[216:231], v[12:15], v[248:251], 0\n"
    "v_exp_f32 v205, v205\n"
    "v_exp_f32 v206, v206\n"
    "v_exp_f32 v207, v207\n"
    "v_exp_f32 v208, v208\n"
    "v_exp_f32 v209, v209\n"
    "v_exp_f32 v210, v210\n"
    "v_exp_f32 v211, v211\n"
    "v_exp_f32 v212, v212\n"
    "ds_write_b128 v3, v[232:235] offset:9216\n"
    "ds_write_b128 v3, v[236:239] offset:10240\n"
    "ds_write_b32 v4, v240 offset:9216\n"
    "v_mov_b32 v255, 6\n"
    "ds_write_b8 v5, v255 offset:36\n"
    "v_mov_b64 v[232:233], 0\n"
    "v_mov_b64 v[234:235], 0\n"
    "v_mov_b64 v[236:237], 0\n"
    "v_mov_b64 v[238:239], 0\n"
    "v_mov_b64 v[240:241], 0\n"
    "v_exp_f32 v213, v213\n"
    "v_exp_f32 v214, v214\n"
    "v_exp_f32 v215, v215\n"
    "v_smfmac_f32_32x32x32_bf16 v[232:247], v[72:75], v[200:207], v252\n"
    "v_exp_f32 v216, v216\n"
    "v_exp_f32 v217, v217\n"
    "v_smfmac_f32_32x32x32_bf16 v[232:247], v[136:139], v[208:215], v252\n"
    "v_exp_f32 v218, v218\n"
    "v_exp_f32 v219, v219\n"
    "v_exp_f32 v220, v220\n"
    "v_mfma_f32_32x32x16_bf16 v[200:215], v[16:19], v[248:251], 0\n"
    "v_exp_f32 v221, v221\n"
    "v_exp_f32 v222, v222\n"
    "v_exp_f32 v223, v223\n"
    "v_exp_f32 v224, v224\n"
    "v_exp_f32 v225, v225\n"
    "v_smfmac_f32_32x32x32_bf16 v[232:247], v[76:79], v[216:223], v252\n"
    "v_exp_f32 v226, v226\n"
    "v_exp_f32 v227, v227\n"
    "v_exp_f32 v228, v228\n"
    "v_exp_f32 v229, v229\n"
    "v_exp_f32 v230, v230\n"
    "v_exp_f32 v231, v231\n"
    "v_exp_f32 v200, v200\n"
    "v_exp_f32 v201, v201\n"
    "v_smfmac_f32_32x32x32_bf16 v[232:247], v[140:143], v[224:231], v252\n"
    "v_exp_f32 v202, v202\n"
    "v_exp_f32 v203, v203\n"
    "v_exp_f32 v204, v204\n"
    "v_mfma_f32_32x32x16_bf16 v[216:231], v[20:23], v[248:251], 0\n"
    "v_exp_f32 v205, v205\n"
    "v_exp_f32 v206, v206\n"
    "v_exp_f32 v207, v207\n"
    "v_exp_f32 v208, v208\n"
    "v_exp_f32 v209, v209\n"
    "v_smfmac_f32_32x32x32_bf16 v[232:247], v[80:83], v[200:207], v252\n"
    "v_exp_f32 v210, v210\n"
    "v_exp_f32 v211, v211\n"
    "v_exp_f32 v212, v212\n"
    "v_exp_f32 v213, v213\n"
    "v_exp_f32 v214, v214\n"
    "v_exp_f32 v215, v215\n"
    "v_exp_f32 v216, v216\n"
    "v_exp_f32 v217, v217\n"
    "v_smfmac_f32_32x32x32_bf16 v[232:247], v[144:147], v[208:215], v252\n"
    "v_exp_f32 v218, v218\n"
    "v_exp_f32 v219, v219\n"
    "v_exp_f32 v220, v220\n"
    "v_mfma_f32_32x32x16_bf16 v[200:215], v[24:27], v[248:251], 0\n"
    "v_exp_f32 v221, v221\n"
    "v_exp_f32 v222, v222\n"
    "v_exp_f32 v223, v223\n"
    "v_exp_f32 v224, v224\n"
    "v_exp_f32 v225, v225\n"
    "v_smfmac_f32_32x32x32_bf16 v[232:247], v[84:87], v[216:223], v252\n"
    "v_exp_f32 v226, v226\n"
    "v_exp_f32 v227, v227\n"
    "v_exp_f32 v228, v228\n"
    "v_exp_f32 v229, v229\n"
    "v_exp_f32 v230, v230\n"
    "v_exp_f32 v231, v231\n"
    "v_exp_f32 v200, v200\n"
    "v_exp_f32 v201, v201\n"
    "v_smfmac_f32_32x32x32_bf16 v[232:247], v[148:151], v[224:231], v252\n"
    "v_exp_f32 v202, v202\n"
    "v_exp_f32 v203, v203\n"
    "v_exp_f32 v204, v204\n"
    "v_mfma_f32_32x32x16_bf16 v[216:231], v[28:31], v[248:251], 0\n"
    "v_exp_f32 v205, v205\n"
    "v_exp_f32 v206, v206\n"
    "v_exp_f32 v207, v207\n"
    "v_exp_f32 v208, v208\n"
    "v_exp_f32 v209, v209\n"
    "v_smfmac_f32_32x32x32_bf16 v[232:247], v[88:91], v[200:207], v252\n"
    "v_exp_f32 v210, v210\n"
    "v_exp_f32 v211, v211\n"
    "v_exp_f32 v212, v212\n"
    "v_exp_f32 v213, v213\n"
    "v_exp_f32 v214, v214\n"
    "v_exp_f32 v215, v215\n"
    "v_exp_f32 v216, v216\n"
    "v_exp_f32 v217, v217\n"
    "v_smfmac_f32_32x32x32_bf16 v[232:247], v[152:155], v[208:215], v252\n"
    "v_exp_f32 v218, v218\n"
    "v_exp_f32 v219, v219\n"
    "v_exp_f32 v220, v220\n"
    "v_mfma_f32_32x32x16_bf16 v[200:215], v[32:35], v[248:251], 0\n"
    "v_exp_f32 v221, v221\n"
    "v_exp_f32 v222, v222\n"
    "v_exp_f32 v223, v223\n"
    "v_exp_f32 v224, v224\n"
    "v_exp_f32 v225, v225\n"
    "v_smfmac_f32_32x32x32_bf16 v[232:247], v[92:95], v[216:223], v252\n"
    "v_exp_f32 v226, v226\n"
    "v_exp_f32 v227, v227\n"
    "v_exp_f32 v228, v228\n"
    "v_exp_f32 v229, v229\n"
    "v_exp_f32 v230, v230\n"
    "v_exp_f32 v231, v231\n"
    "v_exp_f32 v200, v200\n"
    "v_exp_f32 v201, v201\n"
    "v_smfmac_f32_32x32x32_bf16 v[232:247], v[156:159], v[224:231], v252\n"
    "v_exp_f32 v202, v202\n"
    "v_exp_f32 v203, v203\n"
    "v_exp_f32 v204, v204\n"
    "v_mfma_f32_32x32x16_bf16 v[216:231], v[36:39], v[248:251], 0\n"
    "v_exp_f32 v205, v205\n"
    "v_exp_f32 v206, v206\n"
    "v_exp_f32 v207, v207\n"
    "v_exp_f32 v208, v208\n"
    "v_exp_f32 v209, v209\n"
    "v_smfmac_f32_32x32x32_bf16 v[232:247], v[96:99], v[200:207], v252\n"
    "v_exp_f32 v210, v210\n"
    "v_exp_f32 v211, v211\n"
    "v_exp_f32 v212, v212\n"
    "v_exp_f32 v213, v213\n"
    "v_exp_f32 v214, v214\n"
    "v_exp_f32 v215, v215\n"
    "v_exp_f32 v216, v216\n"
    "v_exp_f32 v217, v217\n"
    "v_smfmac_f32_32x32x32_bf16 v[232:247], v[160:163], v[208:215], v252\n"
    "v_exp_f32 v218, v218\n"
    "v_exp_f32 v219, v219\n"
    "v_exp_f32 v220, v220\n"
    "v_mfma_f32_32x32x16_bf16 v[200:215], v[40:43], v[248:251], 0\n"
    "v_exp_f32 v221, v221\n"
    "v_exp_f32 v222, v222\n"
    "v_exp_f32 v223, v223\n"
    "v_exp_f32 v224, v224\n"
    "v_exp_f32 v225, v225\n"
    "v_smfmac_f32_32x32x32_bf16 v[232:247], v[100:103], v[216:223], v252\n"
    "v_exp_f32 v226, v226\n"
    "v_exp_f32 v227, v227\n"
    "v_exp_f32 v228, v228\n"
    "v_exp_f32 v229, v229\n"
    "v_exp_f32 v230, v230\n"
    "v_exp_f32 v231, v231\n"
    "v_exp_f32 v200, v200\n"
    "v_exp_f32 v201, v201\n"
    "v_smfmac_f32_32x32x32_bf16 v[232:247], v[164:167], v[224:231], v252\n"
    "v_exp_f32 v202, v202\n"
    "v_exp_f32 v203, v203\n"
    "v_exp_f32 v204, v204\n"
    "v_mfma_f32_32x32x16_bf16 v[216:231], v[44:47], v[248:251], 0\n"
    "v_exp_f32 v205, v205\n"
    "v_exp_f32 v206, v206\n"
    "v_exp_f32 v207, v207\n"
    "v_exp_f32 v208, v208\n"
    "v_exp_f32 v209, v209\n"
    "v_smfmac_f32_32x32x32_bf16 v[232:247], v[104:107], v[200:207], v252\n"
    "v_exp_f32 v210, v210\n"
    "v_exp_f32 v211, v211\n"
    "v_exp_f32 v212, v212\n"
    "v_exp_f32 v213, v213\n"
    "v_exp_f32 v214, v214\n"
    "v_exp_f32 v215, v215\n"
    "v_exp_f32 v216, v216\n"
    "v_exp_f32 v217, v217\n"
    "v_smfmac_f32_32x32x32_bf16 v[232:247], v[168:171], v[208:215], v252\n"
    "v_exp_f32 v218, v218\n"
    "v_exp_f32 v219, v219\n"
    "v_exp_f32 v220, v220\n"
    "v_mfma_f32_32x32x16_bf16 v[200:215], v[48:51], v[248:251], 0\n"
    "v_exp_f32 v221, v221\n"
    "v_exp_f32 v222, v222\n"
    "v_exp_f32 v223, v223\n"
    "v_exp_f32 v224, v224\n"
    "v_exp_f32 v225, v225\n"
    "v_smfmac_f32_32x32x32_bf16 v[232:247], v[108:111], v[216:223], v252\n"
    "v_exp_f32 v226, v226\n"
    "v_exp_f32 v227, v227\n"
    "v_exp_f32 v228, v228\n"
    "v_exp_f32 v229, v229\n"
    "v_exp_f32 v230, v230\n"
    "v_exp_f32 v231, v231\n"
    "v_exp_f32 v200, v200\n"
    "v_exp_f32 v201, v201\n"
    "v_smfmac_f32_32x32x32_bf16 v[232:247], v[172:175], v[224:231], v252\n"
    "v_exp_f32 v202, v202\n"
    "v_exp_f32 v203, v203\n"
    "v_exp_f32 v204, v204\n"
    "v_mfma_f32_32x32x16_bf16 v[216:231], v[52:55], v[248:251], 0\n"
    "v_exp_f32 v205, v205\n"
    "v_exp_f32 v206, v206\n"
    "v_exp_f32 v207, v207\n"
    "v_exp_f32 v208, v208\n"
    "v_exp_f32 v209, v209\n"
    "v_smfmac_f32_32x32x32_bf16 v[232:247], v[112:115], v[200:207], v252\n"
    "v_exp_f32 v210, v210\n"
    "v_exp_f32 v211, v211\n"
    "v_exp_f32 v212, v212\n"
    "v_exp_f32 v213, v213\n"
    "v_exp_f32 v214, v214\n"
    "v_exp_f32 v215, v215\n"
    "v_exp_f32 v216, v216\n"
    "v_exp_f32 v217, v217\n"
    "v_smfmac_f32_32x32x32_bf16 v[232:247], v[176:179], v[208:215], v252\n"
    "v_exp_f32 v218, v218\n"
    "v_exp_f32 v219, v219\n"
    "v_exp_f32 v220, v220\n"
    "v_mfma_f32_32x32x16_bf16 v[200:215], v[56:59], v[248:251], 0\n"
    "v_exp_f32 v221, v221\n"
    "v_exp_f32 v222, v222\n"
    "v_exp_f32 v223, v223\n"
    "v_exp_f32 v224, v224\n"
    "v_exp_f32 v225, v225\n"
    "v_smfmac_f32_32x32x32_bf16 v[232:247], v[116:119], v[216:223], v252\n"
    "v_exp_f32 v226, v226\n"
    "v_exp_f32 v227, v227\n"
    "v_exp_f32 v228, v228\n"
    "v_exp_f32 v229, v229\n"
    "v_exp_f32 v230, v230\n"
    "v_exp_f32 v231, v231\n"
    "v_exp_f32 v200, v200\n"
    "v_exp_f32 v201, v201\n"
    "v_smfmac_f32_32x32x32_bf16 v[232:247], v[180:183], v[224:231], v252\n"
    "v_exp_f32 v202, v202\n"
    "v_exp_f32 v203, v203\n"
    "v_exp_f32 v204, v204\n"
    "v_mfma_f32_32x32x16_bf16 v[216:231], v[60:63], v[248:251], 0\n"
    "v_exp_f32 v205, v205\n"
    "v_exp_f32 v206, v206\n"
    "v_exp_f32 v207, v207\n"
    "v_exp_f32 v208, v208\n"
    "v_exp_f32 v209, v209\n"
    "v_smfmac_f32_32x32x32_bf16 v[232:247], v[120:123], v[200:207], v252\n"
    "v_exp_f32 v210, v210\n"
    "v_exp_f32 v211, v211\n"
    "v_exp_f32 v212, v212\n"
    "v_exp_f32 v213, v213\n"
    "v_exp_f32 v214, v214\n"
    "v_exp_f32 v215, v215\n"
    "v_exp_f32 v216, v216\n"
    "v_exp_f32 v217, v217\n"
    "v_smfmac_f32_32x32x32_bf16 v[232:247], v[184:187], v[208:215], v252\n"
    "v_exp_f32 v218, v218\n"
    "v_exp_f32 v219, v219\n"
    "v_exp_f32 v220, v220\n"
    "v_mfma_f32_32x32x16_bf16 v[200:215], v[64:67], v[248:251], 0\n"
    "v_exp_f32 v221, v221\n"
    "v_exp_f32 v222, v222\n"
    "v_exp_f32 v223, v223\n"
    "v_exp_f32 v224, v224\n"
    "v_exp_f32 v225, v225\n"
    "v_smfmac_f32_32x32x32_bf16 v[232:247], v[124:127], v[216:223], v252\n"
    "v_exp_f32 v226, v226\n"
    "v_exp_f32 v227, v227\n"
    "v_exp_f32 v228, v228\n"
    "v_exp_f32 v229, v229\n"
    "v_exp_f32 v230, v230\n"
    "v_exp_f32 v231, v231\n"
    "v_exp_f32 v200, v200\n"
    "v_exp_f32 v201, v201\n"
    "v_smfmac_f32_32x32x32_bf16 v[232:247], v[188:191], v[224:231], v252\n"
    "v_exp_f32 v202, v202\n"
    "v_exp_f32 v203, v203\n"
    "v_exp_f32 v204, v204\n"
    "v_mfma_f32_32x32x16_bf16 v[216:231], v[68:71], v[248:251], 0\n"
    "ds_read_b128 v[248:251], v2 offset:7168\n"
    "ds_read_b32 v255, v7 offset:72\n"
    "v_exp_f32 v205, v205\n"
    "v_exp_f32 v206, v206\n"
    "v_exp_f32 v207, v207\n"
    "v_exp_f32 v208, v208\n"
    "v_exp_f32 v209, v209\n"
    "v_smfmac_f32_32x32x32_bf16 v[232:247], v[128:131], v[200:207], v252\n"
    "v_exp_f32 v210, v210\n"
    "v_exp_f32 v211, v211\n"
    "v_exp_f32 v212, v212\n"
    "v_exp_f32 v213, v213\n"
    "v_exp_f32 v214, v214\n"
    "v_exp_f32 v215, v215\n"
    "v_exp_f32 v216, v216\n"
    "v_exp_f32 v217, v217\n"
    "v_smfmac_f32_32x32x32_bf16 v[232:247], v[192:195], v[208:215], v252\n"
    "v_exp_f32 v218, v218\n"
    "v_exp_f32 v219, v219\n"
    "v_exp_f32 v220, v220\n"
    "s_waitcnt lgkmcnt(0)\n"
    "v_cmp_ne_u32 vcc, 0, v255\n"
    "s_cbranch_vccz .L_ofw6_k\n"
    ".L_ofw6r_k:\n"
    "v_mfma_f32_32x32x16_bf16 v[200:215], v[8:11], v[248:251], 0\n"
    "v_exp_f32 v221, v221\n"
    "v_exp_f32 v222, v222\n"
    "v_exp_f32 v223, v223\n"
    "v_exp_f32 v224, v224\n"
    "v_exp_f32 v225, v225\n"
    "v_smfmac_f32_32x32x32_bf16 v[232:247], v[132:135], v[216:223], v252\n"
    "v_exp_f32 v226, v226\n"
    "v_exp_f32 v227, v227\n"
    "v_exp_f32 v228, v228\n"
    "v_exp_f32 v229, v229\n"
    "v_exp_f32 v230, v230\n"
    "v_exp_f32 v231, v231\n"
    "v_exp_f32 v200, v200\n"
    "v_exp_f32 v201, v201\n"
    "v_smfmac_f32_32x32x32_bf16 v[232:247], v[196:199], v[224:231], v252\n"
    "v_exp_f32 v202, v202\n"
    "v_exp_f32 v203, v203\n"
    "v_exp_f32 v204, v204\n"
    "v_mfma_f32_32x32x16_bf16 v[216:231], v[12:15], v[248:251], 0\n"
    "v_exp_f32 v205, v205\n"
    "v_exp_f32 v206, v206\n"
    "v_exp_f32 v207, v207\n"
    "v_exp_f32 v208, v208\n"
    "v_exp_f32 v209, v209\n"
    "v_exp_f32 v210, v210\n"
    "v_exp_f32 v211, v211\n"
    "v_exp_f32 v212, v212\n"
    "ds_write_b128 v3, v[232:235] offset:18432\n"
    "ds_write_b128 v3, v[236:239] offset:19456\n"
    "ds_write_b32 v4, v240 offset:18432\n"
    "v_mov_b32 v255, 7\n"
    "ds_write_b8 v5, v255 offset:36\n"
    "v_mov_b64 v[232:233], 0\n"
    "v_mov_b64 v[234:235], 0\n"
    "v_mov_b64 v[236:237], 0\n"
    "v_mov_b64 v[238:239], 0\n"
    "v_mov_b64 v[240:241], 0\n"
    "v_exp_f32 v213, v213\n"
    "v_exp_f32 v214, v214\n"
    "v_exp_f32 v215, v215\n"
    "v_smfmac_f32_32x32x32_bf16 v[232:247], v[72:75], v[200:207], v252\n"
    "v_exp_f32 v216, v216\n"
    "v_exp_f32 v217, v217\n"
    "v_smfmac_f32_32x32x32_bf16 v[232:247], v[136:139], v[208:215], v252\n"
    "v_exp_f32 v218, v218\n"
    "v_exp_f32 v219, v219\n"
    "v_exp_f32 v220, v220\n"
    "v_mfma_f32_32x32x16_bf16 v[200:215], v[16:19], v[248:251], 0\n"
    "v_exp_f32 v221, v221\n"
    "v_exp_f32 v222, v222\n"
    "v_exp_f32 v223, v223\n"
    "v_exp_f32 v224, v224\n"
    "v_exp_f32 v225, v225\n"
    "v_smfmac_f32_32x32x32_bf16 v[232:247], v[76:79], v[216:223], v252\n"
    "v_exp_f32 v226, v226\n"
    "v_exp_f32 v227, v227\n"
    "v_exp_f32 v228, v228\n"
    "v_exp_f32 v229, v229\n"
    "v_exp_f32 v230, v230\n"
    "v_exp_f32 v231, v231\n"
    "v_exp_f32 v200, v200\n"
    "v_exp_f32 v201, v201\n"
    "v_smfmac_f32_32x32x32_bf16 v[232:247], v[140:143], v[224:231], v252\n"
    "v_exp_f32 v202, v202\n"
    "v_exp_f32 v203, v203\n"
    "v_exp_f32 v204, v204\n"
    "v_mfma_f32_32x32x16_bf16 v[216:231], v[20:23], v[248:251], 0\n"
    "v_exp_f32 v205, v205\n"
    "v_exp_f32 v206, v206\n"
    "v_exp_f32 v207, v207\n"
    "v_exp_f32 v208, v208\n"
    "v_exp_f32 v209, v209\n"
    "v_smfmac_f32_32x32x32_bf16 v[232:247], v[80:83], v[200:207], v252\n"
    "v_exp_f32 v210, v210\n"
    "v_exp_f32 v211, v211\n"
    "v_exp_f32 v212, v212\n"
    "v_exp_f32 v213, v213\n"
    "v_exp_f32 v214, v214\n"
    "v_exp_f32 v215, v215\n"
    "v_exp_f32 v216, v216\n"
    "v_exp_f32 v217, v217\n"
    "v_smfmac_f32_32x32x32_bf16 v[232:247], v[144:147], v[208:215], v252\n"
    "v_exp_f32 v218, v218\n"
    "v_exp_f32 v219, v219\n"
    "v_exp_f32 v220, v220\n"
    "v_mfma_f32_32x32x16_bf16 v[200:215], v[24:27], v[248:251], 0\n"
    "v_exp_f32 v221, v221\n"
    "v_exp_f32 v222, v222\n"
    "v_exp_f32 v223, v223\n"
    "v_exp_f32 v224, v224\n"
    "v_exp_f32 v225, v225\n"
    "v_smfmac_f32_32x32x32_bf16 v[232:247], v[84:87], v[216:223], v252\n"
    "v_exp_f32 v226, v226\n"
    "v_exp_f32 v227, v227\n"
    "v_exp_f32 v228, v228\n"
    "v_exp_f32 v229, v229\n"
    "v_exp_f32 v230, v230\n"
    "v_exp_f32 v231, v231\n"
    "v_exp_f32 v200, v200\n"
    "v_exp_f32 v201, v201\n"
    "v_smfmac_f32_32x32x32_bf16 v[232:247], v[148:151], v[224:231], v252\n"
    "v_exp_f32 v202, v202\n"
    "v_exp_f32 v203, v203\n"
    "v_exp_f32 v204, v204\n"
    "v_mfma_f32_32x32x16_bf16 v[216:231], v[28:31], v[248:251], 0\n"
    "v_exp_f32 v205, v205\n"
    "v_exp_f32 v206, v206\n"
    "v_exp_f32 v207, v207\n"
    "v_exp_f32 v208, v208\n"
    "v_exp_f32 v209, v209\n"
    "v_smfmac_f32_32x32x32_bf16 v[232:247], v[88:91], v[200:207], v252\n"
    "v_exp_f32 v210, v210\n"
    "v_exp_f32 v211, v211\n"
    "v_exp_f32 v212, v212\n"
    "v_exp_f32 v213, v213\n"
    "v_exp_f32 v214, v214\n"
    "v_exp_f32 v215, v215\n"
    "v_exp_f32 v216, v216\n"
    "v_exp_f32 v217, v217\n"
    "v_smfmac_f32_32x32x32_bf16 v[232:247], v[152:155], v[208:215], v252\n"
    "v_exp_f32 v218, v218\n"
    "v_exp_f32 v219, v219\n"
    "v_exp_f32 v220, v220\n"
    "v_mfma_f32_32x32x16_bf16 v[200:215], v[32:35], v[248:251], 0\n"
    "v_exp_f32 v221, v221\n"
    "v_exp_f32 v222, v222\n"
    "v_exp_f32 v223, v223\n"
    "v_exp_f32 v224, v224\n"
    "v_exp_f32 v225, v225\n"
    "v_smfmac_f32_32x32x32_bf16 v[232:247], v[92:95], v[216:223], v252\n"
    "v_exp_f32 v226, v226\n"
    "v_exp_f32 v227, v227\n"
    "v_exp_f32 v228, v228\n"
    "v_exp_f32 v229, v229\n"
    "v_exp_f32 v230, v230\n"
    "v_exp_f32 v231, v231\n"
    "v_exp_f32 v200, v200\n"
    "v_exp_f32 v201, v201\n"
    "v_smfmac_f32_32x32x32_bf16 v[232:247], v[156:159], v[224:231], v252\n"
    "v_exp_f32 v202, v202\n"
    "v_exp_f32 v203, v203\n"
    "v_exp_f32 v204, v204\n"
    "v_mfma_f32_32x32x16_bf16 v[216:231], v[36:39], v[248:251], 0\n"
    "v_exp_f32 v205, v205\n"
    "v_exp_f32 v206, v206\n"
    "v_exp_f32 v207, v207\n"
    "v_exp_f32 v208, v208\n"
    "v_exp_f32 v209, v209\n"
    "v_smfmac_f32_32x32x32_bf16 v[232:247], v[96:99], v[200:207], v252\n"
    "v_exp_f32 v210, v210\n"
    "v_exp_f32 v211, v211\n"
    "v_exp_f32 v212, v212\n"
    "v_exp_f32 v213, v213\n"
    "v_exp_f32 v214, v214\n"
    "v_exp_f32 v215, v215\n"
    "v_exp_f32 v216, v216\n"
    "v_exp_f32 v217, v217\n"
    "v_smfmac_f32_32x32x32_bf16 v[232:247], v[160:163], v[208:215], v252\n"
    "v_exp_f32 v218, v218\n"
    "v_exp_f32 v219, v219\n"
    "v_exp_f32 v220, v220\n"
    "v_mfma_f32_32x32x16_bf16 v[200:215], v[40:43], v[248:251], 0\n"
    "v_exp_f32 v221, v221\n"
    "v_exp_f32 v222, v222\n"
    "v_exp_f32 v223, v223\n"
    "v_exp_f32 v224, v224\n"
    "v_exp_f32 v225, v225\n"
    "v_smfmac_f32_32x32x32_bf16 v[232:247], v[100:103], v[216:223], v252\n"
    "v_exp_f32 v226, v226\n"
    "v_exp_f32 v227, v227\n"
    "v_exp_f32 v228, v228\n"
    "v_exp_f32 v229, v229\n"
    "v_exp_f32 v230, v230\n"
    "v_exp_f32 v231, v231\n"
    "v_exp_f32 v200, v200\n"
    "v_exp_f32 v201, v201\n"
    "v_smfmac_f32_32x32x32_bf16 v[232:247], v[164:167], v[224:231], v252\n"
    "v_exp_f32 v202, v202\n"
    "v_exp_f32 v203, v203\n"
    "v_exp_f32 v204, v204\n"
    "v_mfma_f32_32x32x16_bf16 v[216:231], v[44:47], v[248:251], 0\n"
    "v_exp_f32 v205, v205\n"
    "v_exp_f32 v206, v206\n"
    "v_exp_f32 v207, v207\n"
    "v_exp_f32 v208, v208\n"
    "v_exp_f32 v209, v209\n"
    "v_smfmac_f32_32x32x32_bf16 v[232:247], v[104:107], v[200:207], v252\n"
    "v_exp_f32 v210, v210\n"
    "v_exp_f32 v211, v211\n"
    "v_exp_f32 v212, v212\n"
    "v_exp_f32 v213, v213\n"
    "v_exp_f32 v214, v214\n"
    "v_exp_f32 v215, v215\n"
    "v_exp_f32 v216, v216\n"
    "v_exp_f32 v217, v217\n"
    "v_smfmac_f32_32x32x32_bf16 v[232:247], v[168:171], v[208:215], v252\n"
    "v_exp_f32 v218, v218\n"
    "v_exp_f32 v219, v219\n"
    "v_exp_f32 v220, v220\n"
    "v_mfma_f32_32x32x16_bf16 v[200:215], v[48:51], v[248:251], 0\n"
    "v_exp_f32 v221, v221\n"
    "v_exp_f32 v222, v222\n"
    "v_exp_f32 v223, v223\n"
    "v_exp_f32 v224, v224\n"
    "v_exp_f32 v225, v225\n"
    "v_smfmac_f32_32x32x32_bf16 v[232:247], v[108:111], v[216:223], v252\n"
    "v_exp_f32 v226, v226\n"
    "v_exp_f32 v227, v227\n"
    "v_exp_f32 v228, v228\n"
    "v_exp_f32 v229, v229\n"
    "v_exp_f32 v230, v230\n"
    "v_exp_f32 v231, v231\n"
    "v_exp_f32 v200, v200\n"
    "v_exp_f32 v201, v201\n"
    "v_smfmac_f32_32x32x32_bf16 v[232:247], v[172:175], v[224:231], v252\n"
    "v_exp_f32 v202, v202\n"
    "v_exp_f32 v203, v203\n"
    "v_exp_f32 v204, v204\n"
    "v_mfma_f32_32x32x16_bf16 v[216:231], v[52:55], v[248:251], 0\n"
    "v_exp_f32 v205, v205\n"
    "v_exp_f32 v206, v206\n"
    "v_exp_f32 v207, v207\n"
    "v_exp_f32 v208, v208\n"
    "v_exp_f32 v209, v209\n"
    "v_smfmac_f32_32x32x32_bf16 v[232:247], v[112:115], v[200:207], v252\n"
    "v_exp_f32 v210, v210\n"
    "v_exp_f32 v211, v211\n"
    "v_exp_f32 v212, v212\n"
    "v_exp_f32 v213, v213\n"
    "v_exp_f32 v214, v214\n"
    "v_exp_f32 v215, v215\n"
    "v_exp_f32 v216, v216\n"
    "v_exp_f32 v217, v217\n"
    "v_smfmac_f32_32x32x32_bf16 v[232:247], v[176:179], v[208:215], v252\n"
    "v_exp_f32 v218, v218\n"
    "v_exp_f32 v219, v219\n"
    "v_exp_f32 v220, v220\n"
    "v_mfma_f32_32x32x16_bf16 v[200:215], v[56:59], v[248:251], 0\n"
    "v_exp_f32 v221, v221\n"
    "v_exp_f32 v222, v222\n"
    "v_exp_f32 v223, v223\n"
    "v_exp_f32 v224, v224\n"
    "v_exp_f32 v225, v225\n"
    "v_smfmac_f32_32x32x32_bf16 v[232:247], v[116:119], v[216:223], v252\n"
    "v_exp_f32 v226, v226\n"
    "v_exp_f32 v227, v227\n"
    "v_exp_f32 v228, v228\n"
    "v_exp_f32 v229, v229\n"
    "v_exp_f32 v230, v230\n"
    "v_exp_f32 v231, v231\n"
    "v_exp_f32 v200, v200\n"
    "v_exp_f32 v201, v201\n"
    "v_smfmac_f32_32x32x32_bf16 v[232:247], v[180:183], v[224:231], v252\n"
    "v_exp_f32 v202, v202\n"
    "v_exp_f32 v203, v203\n"
    "v_exp_f32 v204, v204\n"
    "v_mfma_f32_32x32x16_bf16 v[216:231], v[60:63], v[248:251], 0\n"
    "v_exp_f32 v205, v205\n"
    "v_exp_f32 v206, v206\n"
    "v_exp_f32 v207, v207\n"
    "v_exp_f32 v208, v208\n"
    "v_exp_f32 v209, v209\n"
    "v_smfmac_f32_32x32x32_bf16 v[232:247], v[120:123], v[200:207], v252\n"
    "v_exp_f32 v210, v210\n"
    "v_exp_f32 v211, v211\n"
    "v_exp_f32 v212, v212\n"
    "v_exp_f32 v213, v213\n"
    "v_exp_f32 v214, v214\n"
    "v_exp_f32 v215, v215\n"
    "v_exp_f32 v216, v216\n"
    "v_exp_f32 v217, v217\n"
    "v_smfmac_f32_32x32x32_bf16 v[232:247], v[184:187], v[208:215], v252\n"
    "v_exp_f32 v218, v218\n"
    "v_exp_f32 v219, v219\n"
    "v_exp_f32 v220, v220\n"
    "v_mfma_f32_32x32x16_bf16 v[200:215], v[64:67], v[248:251], 0\n"
    "v_exp_f32 v221, v221\n"
    "v_exp_f32 v222, v222\n"
    "v_exp_f32 v223, v223\n"
    "v_exp_f32 v224, v224\n"
    "v_exp_f32 v225, v225\n"
    "v_smfmac_f32_32x32x32_bf16 v[232:247], v[124:127], v[216:223], v252\n"
    "v_exp_f32 v226, v226\n"
    "v_exp_f32 v227, v227\n"
    "v_exp_f32 v228, v228\n"
    "v_exp_f32 v229, v229\n"
    "v_exp_f32 v230, v230\n"
    "v_exp_f32 v231, v231\n"
    "v_exp_f32 v200, v200\n"
    "v_exp_f32 v201, v201\n"
    "v_smfmac_f32_32x32x32_bf16 v[232:247], v[188:191], v[224:231], v252\n"
    "v_exp_f32 v202, v202\n"
    "v_exp_f32 v203, v203\n"
    "v_exp_f32 v204, v204\n"
    "v_mfma_f32_32x32x16_bf16 v[216:231], v[68:71], v[248:251], 0\n"
    "ds_read_b32 v255, v7 offset:76\n"
    "v_exp_f32 v205, v205\n"
    "v_exp_f32 v206, v206\n"
    "v_exp_f32 v207, v207\n"
    "v_exp_f32 v208, v208\n"
    "v_exp_f32 v209, v209\n"
    "v_smfmac_f32_32x32x32_bf16 v[232:247], v[128:131], v[200:207], v252\n"
    "v_exp_f32 v210, v210\n"
    "v_exp_f32 v211, v211\n"
    "v_exp_f32 v212, v212\n"
    "v_exp_f32 v213, v213\n"
    "v_exp_f32 v214, v214\n"
    "v_exp_f32 v215, v215\n"
    "v_exp_f32 v216, v216\n"
    "v_exp_f32 v217, v217\n"
    "v_smfmac_f32_32x32x32_bf16 v[232:247], v[192:195], v[208:215], v252\n"
    "v_exp_f32 v218, v218\n"
    "v_exp_f32 v219, v219\n"
    "v_exp_f32 v220, v220\n"
    "s_waitcnt lgkmcnt(0)\n"
    "v_cmp_ne_u32 vcc, 0, v255\n"
    "s_cbranch_vccz .L_ofw7_k\n"
    ".L_ofw7r_k:\n"
    "v_exp_f32 v221, v221\n"
    "v_exp_f32 v222, v222\n"
    "v_exp_f32 v223, v223\n"
    "v_exp_f32 v224, v224\n"
    "v_exp_f32 v225, v225\n"
    "v_smfmac_f32_32x32x32_bf16 v[232:247], v[132:135], v[216:223], v252\n"
    "v_exp_f32 v226, v226\n"
    "v_exp_f32 v227, v227\n"
    "v_exp_f32 v228, v228\n"
    "v_exp_f32 v229, v229\n"
    "v_exp_f32 v230, v230\n"
    "v_exp_f32 v231, v231\n"
    "s_nop 1\n"
    "v_smfmac_f32_32x32x32_bf16 v[232:247], v[196:199], v[224:231], v252\n"
    "s_nop 11\n"
    "ds_write_b128 v3, v[232:235] offset:27648\n"
    "ds_write_b128 v3, v[236:239] offset:28672\n"
    "ds_write_b32 v4, v240 offset:27648\n"
    "v_mov_b32 v255, 8\n"
    "ds_write_b8 v5, v255 offset:36\n"
    "s_branch .L_end_k\n"
    ".L_ofw4_k:\n"
    "s_mov_b32 s96, 131072\n"
    ".Lsp_9_k:\n"
    "ds_read_b32 v255, v7 offset:64\n"
    "s_waitcnt lgkmcnt(0)\n"
    "v_readfirstlane_b32 s94, v255\n"
    "s_cmp_eq_u32 s94, 0x1\n"
    "s_cbranch_scc1 .Lspd_10_k\n"
    "s_sleep 1\n"
    "s_sub_u32 s96, s96, 1\n"
    "s_cbranch_scc0 .Lsp_9_k\n"
    ".Lspd_10_k:\n"
    "s_branch .L_ofw4r_k\n"
    ".L_ofw5_k:\n"
    "s_mov_b32 s96, 131072\n"
    ".Lsp_11_k:\n"
    "ds_read_b32 v255, v7 offset:68\n"
    "s_waitcnt lgkmcnt(0)\n"
    "v_readfirstlane_b32 s94, v255\n"
    "s_cmp_eq_u32 s94, 0x1\n"
    "s_cbranch_scc1 .Lspd_12_k\n"
    "s_sleep 1\n"
    "s_sub_u32 s96, s96, 1\n"
    "s_cbranch_scc0 .Lsp_11_k\n"
    ".Lspd_12_k:\n"
    "s_branch .L_ofw5r_k\n"
    ".L_ofw6_k:\n"
    "s_mov_b32 s96, 131072\n"
    ".Lsp_13_k:\n"
    "ds_read_b32 v255, v7 offset:72\n"
    "s_waitcnt lgkmcnt(0)\n"
    "v_readfirstlane_b32 s94, v255\n"
    "s_cmp_eq_u32 s94, 0x1\n"
    "s_cbranch_scc1 .Lspd_14_k\n"
    "s_sleep 1\n"
    "s_sub_u32 s96, s96, 1\n"
    "s_cbranch_scc0 .Lsp_13_k\n"
    ".Lspd_14_k:\n"
    "s_branch .L_ofw6r_k\n"
    ".L_ofw7_k:\n"
    "s_mov_b32 s96, 131072\n"
    ".Lsp_15_k:\n"
    "ds_read_b32 v255, v7 offset:76\n"
    "s_waitcnt lgkmcnt(0)\n"
    "v_readfirstlane_b32 s94, v255\n"
    "s_cmp_eq_u32 s94, 0x1\n"
    "s_cbranch_scc1 .Lspd_16_k\n"
    "s_sleep 1\n"
    "s_sub_u32 s96, s96, 1\n"
    "s_cbranch_scc0 .Lsp_15_k\n"
    ".Lspd_16_k:\n"
    "s_branch .L_ofw7r_k\n"
    ".L_chk_k:\n"
    "s_mov_b32 s96, 131072\n"
    ".Lsp_17_k:\n"
    "ds_read_b32 v255, v7 offset:88\n"
    "s_waitcnt lgkmcnt(0)\n"
    "v_readfirstlane_b32 s94, v255\n"
    "s_cmp_eq_u32 s94, 0x1010101\n"
    "s_cbranch_scc1 .Lspd_18_k\n"
    "s_sleep 1\n"
    "s_sub_u32 s96, s96, 1\n"
    "s_cbranch_scc0 .Lsp_17_k\n"
    ".Lspd_18_k:\n"
    "s_cmp_lg_u32 s86, 2\n"
    "s_cbranch_scc1 .L_noe_k\n"
    "v_mov_b32 v253, 0\n"
    "v_mov_b32 v254, s70\n"
    "v_mov_b32 v255, s71\n"
    "s_mov_b64 exec, 1\n"
    "buffer_store_dwordx2 v[254:255], v253, s[48:51], 0 offen sc1\n"
    "s_mov_b64 exec, -1\n"
    "s_waitcnt vmcnt(0)\n"
    ".L_noe_k:\n"
    "ds_read_b32 v253, v7 offset:56\n"
    "ds_read_b32 v254, v7 offset:60\n"
    "s_waitcnt lgkmcnt(0)\n"
    "v_mul_f32 v253, v253, v254\n"
    "v_cmp_ge_f32 vcc, s87, v253\n"
    "s_cbranch_vccnz .L_chkr_k\n"
    "s_setprio 0\n"
    "s_waitcnt vmcnt(0)\n"
    "s_nop 7\n"
    "s_nop 7\n"
    "v_and_b32 v254, 31, v1\n"
    "v_lshlrev_b32 v254, 4, v254\n"
    "v_add_u32 v6, 102144, v254\n"
    "s_lshl_b32 s76, s75, 2\n"
    "v_add_u32 v254, s76, v6\n"
    "s_mov_b32 s88, 0\n"
    ".L_sl1_k:\n"
    "ds_read_b128 v[248:251], v2\n"
    "v_mov_b32 v253, 0xff800000\n"
    "s_waitcnt lgkmcnt(0)\n"
    "v_mfma_f32_32x32x16_bf16 v[200:215], v[8:11], v[248:251], 0\n"
    "s_nop 11\n"
    "v_max3_f32 v253, v253, v200, v201\n"
    "v_max3_f32 v253, v253, v202, v203\n"
    "v_max3_f32 v253, v253, v204, v205\n"
    "v_max3_f32 v253, v253, v206, v207\n"
    "v_max3_f32 v253, v253, v208, v209\n"
    "v_max3_f32 v253, v253, v210, v211\n"
    "v_max3_f32 v253, v253, v212, v213\n"
    "v_max3_f32 v253, v253, v214, v215\n"
    "v_mfma_f32_32x32x16_bf16 v[200:215], v[12:15], v[248:251], 0\n"
    "s_nop 11\n"
    "v_max3_f32 v253, v253, v200, v201\n"
    "v_max3_f32 v253, v253, v202, v203\n"
    "v_max3_f32 v253, v253, v204, v205\n"
    "v_max3_f32 v253, v253, v206, v207\n"
    "v_max3_f32 v253, v253, v208, v209\n"
    "v_max3_f32 v253, v253, v210, v211\n"
    "v_max3_f32 v253, v253, v212, v213\n"
    "v_max3_f32 v253, v253, v214, v215\n"
    "v_mfma_f32_32x32x16_bf16 v[200:215], v[16:19], v[248:251], 0\n"
    "s_nop 11\n"
    "v_max3_f32 v253, v253, v200, v201\n"
    "v_max3_f32 v253, v253, v202, v203\n"
    "v_max3_f32 v253, v253, v204, v205\n"
    "v_max3_f32 v253, v253, v206, v207\n"
    "v_max3_f32 v253, v253, v208, v209\n"
    "v_max3_f32 v253, v253, v210, v211\n"
    "v_max3_f32 v253, v253, v212, v213\n"
    "v_max3_f32 v253, v253, v214, v215\n"
    "v_mfma_f32_32x32x16_bf16 v[200:215], v[20:23], v[248:251], 0\n"
    "s_nop 11\n"
    "v_max3_f32 v253, v253, v200, v201\n"
    "v_max3_f32 v253, v253, v202, v203\n"
    "v_max3_f32 v253, v253, v204, v205\n"
    "v_max3_f32 v253, v253, v206, v207\n"
    "v_max3_f32 v253, v253, v208, v209\n"
    "v_max3_f32 v253, v253, v210, v211\n"
    "v_max3_f32 v253, v253, v212, v213\n"
    "v_max3_f32 v253, v253, v214, v215\n"
    "v_mfma_f32_32x32x16_bf16 v[200:215], v[24:27], v[248:251], 0\n"
    "s_nop 11\n"
    "v_max3_f32 v253, v253, v200, v201\n"
    "v_max3_f32 v253, v253, v202, v203\n"
    "v_max3_f32 v253, v253, v204, v205\n"
    "v_max3_f32 v253, v253, v206, v207\n"
    "v_max3_f32 v253, v253, v208, v209\n"
    "v_max3_f32 v253, v253, v210, v211\n"
    "v_max3_f32 v253, v253, v212, v213\n"
    "v_max3_f32 v253, v253, v214, v215\n"
    "v_mfma_f32_32x32x16_bf16 v[200:215], v[28:31], v[248:251], 0\n"
    "s_nop 11\n"
    "v_max3_f32 v253, v253, v200, v201\n"
    "v_max3_f32 v253, v253, v202, v203\n"
    "v_max3_f32 v253, v253, v204, v205\n"
    "v_max3_f32 v253, v253, v206, v207\n"
    "v_max3_f32 v253, v253, v208, v209\n"
    "v_max3_f32 v253, v253, v210, v211\n"
    "v_max3_f32 v253, v253, v212, v213\n"
    "v_max3_f32 v253, v253, v214, v215\n"
    "v_mfma_f32_32x32x16_bf16 v[200:215], v[32:35], v[248:251], 0\n"
    "s_nop 11\n"
    "v_max3_f32 v253, v253, v200, v201\n"
    "v_max3_f32 v253, v253, v202, v203\n"
    "v_max3_f32 v253, v253, v204, v205\n"
    "v_max3_f32 v253, v253, v206, v207\n"
    "v_max3_f32 v253, v253, v208, v209\n"
    "v_max3_f32 v253, v253, v210, v211\n"
    "v_max3_f32 v253, v253, v212, v213\n"
    "v_max3_f32 v253, v253, v214, v215\n"
    "v_mfma_f32_32x32x16_bf16 v[200:215], v[36:39], v[248:251], 0\n"
    "s_nop 11\n"
    "v_max3_f32 v253, v253, v200, v201\n"
    "v_max3_f32 v253, v253, v202, v203\n"
    "v_max3_f32 v253, v253, v204, v205\n"
    "v_max3_f32 v253, v253, v206, v207\n"
    "v_max3_f32 v253, v253, v208, v209\n"
    "v_max3_f32 v253, v253, v210, v211\n"
    "v_max3_f32 v253, v253, v212, v213\n"
    "v_max3_f32 v253, v253, v214, v215\n"
    "v_mfma_f32_32x32x16_bf16 v[200:215], v[40:43], v[248:251], 0\n"
    "s_nop 11\n"
    "v_max3_f32 v253, v253, v200, v201\n"
    "v_max3_f32 v253, v253, v202, v203\n"
    "v_max3_f32 v253, v253, v204, v205\n"
    "v_max3_f32 v253, v253, v206, v207\n"
    "v_max3_f32 v253, v253, v208, v209\n"
    "v_max3_f32 v253, v253, v210, v211\n"
    "v_max3_f32 v253, v253, v212, v213\n"
    "v_max3_f32 v253, v253, v214, v215\n"
    "v_mfma_f32_32x32x16_bf16 v[200:215], v[44:47], v[248:251], 0\n"
    "s_nop 11\n"
    "v_max3_f32 v253, v253, v200, v201\n"
    "v_max3_f32 v253, v253, v202, v203\n"
    "v_max3_f32 v253, v253, v204, v205\n"
    "v_max3_f32 v253, v253, v206, v207\n"
    "v_max3_f32 v253, v253, v208, v209\n"
    "v_max3_f32 v253, v253, v210, v211\n"
    "v_max3_f32 v253, v253, v212, v213\n"
    "v_max3_f32 v253, v253, v214, v215\n"
    "v_mfma_f32_32x32x16_bf16 v[200:215], v[48:51], v[248:251], 0\n"
    "s_nop 11\n"
    "v_max3_f32 v253, v253, v200, v201\n"
    "v_max3_f32 v253, v253, v202, v203\n"
    "v_max3_f32 v253, v253, v204, v205\n"
    "v_max3_f32 v253, v253, v206, v207\n"
    "v_max3_f32 v253, v253, v208, v209\n"
    "v_max3_f32 v253, v253, v210, v211\n"
    "v_max3_f32 v253, v253, v212, v213\n"
    "v_max3_f32 v253, v253, v214, v215\n"
    "v_mfma_f32_32x32x16_bf16 v[200:215], v[52:55], v[248:251], 0\n"
    "s_nop 11\n"
    "v_max3_f32 v253, v253, v200, v201\n"
    "v_max3_f32 v253, v253, v202, v203\n"
    "v_max3_f32 v253, v253, v204, v205\n"
    "v_max3_f32 v253, v253, v206, v207\n"
    "v_max3_f32 v253, v253, v208, v209\n"
    "v_max3_f32 v253, v253, v210, v211\n"
    "v_max3_f32 v253, v253, v212, v213\n"
    "v_max3_f32 v253, v253, v214, v215\n"
    "v_mfma_f32_32x32x16_bf16 v[200:215], v[56:59], v[248:251], 0\n"
    "s_nop 11\n"
    "v_max3_f32 v253, v253, v200, v201\n"
    "v_max3_f32 v253, v253, v202, v203\n"
    "v_max3_f32 v253, v253, v204, v205\n"
    "v_max3_f32 v253, v253, v206, v207\n"
    "v_max3_f32 v253, v253, v208, v209\n"
    "v_max3_f32 v253, v253, v210, v211\n"
    "v_max3_f32 v253, v253, v212, v213\n"
    "v_max3_f32 v253, v253, v214, v215\n"
    "v_mfma_f32_32x32x16_bf16 v[200:215], v[60:63], v[248:251], 0\n"
    "s_nop 11\n"
    "v_max3_f32 v253, v253, v200, v201\n"
    "v_max3_f32 v253, v253, v202, v203\n"
    "v_max3_f32 v253, v253, v204, v205\n"
    "v_max3_f32 v253, v253, v206, v207\n"
    "v_max3_f32 v253, v253, v208, v209\n"
    "v_max3_f32 v253, v253, v210, v211\n"
    "v_max3_f32 v253, v253, v212, v213\n"
    "v_max3_f32 v253, v253, v214, v215\n"
    "v_mfma_f32_32x32x16_bf16 v[200:215], v[64:67], v[248:251], 0\n"
    "s_nop 11\n"
    "v_max3_f32 v253, v253, v200, v201\n"
    "v_max3_f32 v253, v253, v202, v203\n"
    "v_max3_f32 v253, v253, v204, v205\n"
    "v_max3_f32 v253, v253, v206, v207\n"
    "v_max3_f32 v253, v253, v208, v209\n"
    "v_max3_f32 v253, v253, v210, v211\n"
    "v_max3_f32 v253, v253, v212, v213\n"
    "v_max3_f32 v253, v253, v214, v215\n"
    "v_mfma_f32_32x32x16_bf16 v[200:215], v[68:71], v[248:251], 0\n"
    "s_nop 11\n"
    "v_max3_f32 v253, v253, v200, v201\n"
    "v_max3_f32 v253, v253, v202, v203\n"
    "v_max3_f32 v253, v253, v204, v205\n"
    "v_max3_f32 v253, v253, v206, v207\n"
    "v_max3_f32 v253, v253, v208, v209\n"
    "v_max3_f32 v253, v253, v210, v211\n"
    "v_max3_f32 v253, v253, v212, v213\n"
    "v_max3_f32 v253, v253, v214, v215\n"
    "v_mov_b32 v255, v253\n"
    "s_nop 1\n"
    "v_permlane32_swap_b32 v253, v255\n"
    "v_max_f32 v253, v253, v255\n"
    "ds_write_b32 v254, v253\n"
    "v_add_u32 v254, 512, v254\n"
    "v_add_u32 v2, 1024, v2\n"
    "s_add_u32 s88, s88, 1\n"
    "s_cmp_lt_u32 s88, 8\n"
    "s_cbranch_scc1 .L_sl1_k\n"
    "v_mov_b32 v255, 1\n"
    "ds_write_b8 v5, v255 offset:52\n"
    "s_mov_b32 s96, 131072\n"
    ".Lsp_19_k:\n"
    "ds_read_b32 v255, v7 offset:52\n"
    "s_waitcnt lgkmcnt(0)\n"
    "v_readfirstlane_b32 s94, v255\n"
    "s_cmp_eq_u32 s94, 0x1010101\n"
    "s_cbranch_scc1 .Lspd_20_k\n"
    "s_sleep 1\n"
    "s_sub_u32 s96, s96, 1\n"
    "s_cbranch_scc0 .Lsp_19_k\n"
    ".Lspd_20_k:\n"
    "v_subrev_u32 v2, 8192, v2\n"
    "s_mov_b32 s88, 0\n"
    ".L_sl2_k:\n"
    "ds_read_b128 v[248:251], v2\n"
    "ds_read_b128 v[200:203], v6\n"
    "s_waitcnt lgkmcnt(0)\n"
    "v_max3_f32 v253, v200, v201, v202\n"
    "v_max_f32 v253, v253, v203\n"
    "v_mov_b64 v[232:233], 0\n"
    "v_mov_b64 v[234:235], 0\n"
    "v_mov_b64 v[236:237], 0\n"
    "v_mov_b64 v[238:239], 0\n"
    "v_mov_b64 v[240:241], 0\n"
    "v_mov_b64 v[242:243], 0\n"
    "v_mov_b64 v[244:245], 0\n"
    "v_mov_b64 v[246:247], 0\n"
    "v_mfma_f32_32x32x16_bf16 v[200:215], v[8:11], v[248:251], 0\n"
    "s_nop 11\n"
    "v_sub_f32 v200, v200, v253\n"
    "v_sub_f32 v201, v201, v253\n"
    "v_sub_f32 v202, v202, v253\n"
    "v_sub_f32 v203, v203, v253\n"
    "v_sub_f32 v204, v204, v253\n"
    "v_sub_f32 v205, v205, v253\n"
    "v_sub_f32 v206, v206, v253\n"
    "v_sub_f32 v207, v207, v253\n"
    "v_sub_f32 v208, v208, v253\n"
    "v_sub_f32 v209, v209, v253\n"
    "v_sub_f32 v210, v210, v253\n"
    "v_sub_f32 v211, v211, v253\n"
    "v_sub_f32 v212, v212, v253\n"
    "v_sub_f32 v213, v213, v253\n"
    "v_sub_f32 v214, v214, v253\n"
    "v_sub_f32 v215, v215, v253\n"
    "v_exp_f32 v200, v200\n"
    "v_exp_f32 v201, v201\n"
    "v_exp_f32 v202, v202\n"
    "v_exp_f32 v203, v203\n"
    "v_exp_f32 v204, v204\n"
    "v_exp_f32 v205, v205\n"
    "v_exp_f32 v206, v206\n"
    "v_exp_f32 v207, v207\n"
    "v_exp_f32 v208, v208\n"
    "v_exp_f32 v209, v209\n"
    "v_exp_f32 v210, v210\n"
    "v_exp_f32 v211, v211\n"
    "v_exp_f32 v212, v212\n"
    "v_exp_f32 v213, v213\n"
    "v_exp_f32 v214, v214\n"
    "v_exp_f32 v215, v215\n"
    "v_smfmac_f32_32x32x32_bf16 v[232:247], v[72:75], v[200:207], v252\n"
    "s_nop 0\n"
    "v_smfmac_f32_32x32x32_bf16 v[232:247], v[136:139], v[208:215], v252\n"
    "v_mfma_f32_32x32x16_bf16 v[200:215], v[12:15], v[248:251], 0\n"
    "s_nop 11\n"
    "v_sub_f32 v200, v200, v253\n"
    "v_sub_f32 v201, v201, v253\n"
    "v_sub_f32 v202, v202, v253\n"
    "v_sub_f32 v203, v203, v253\n"
    "v_sub_f32 v204, v204, v253\n"
    "v_sub_f32 v205, v205, v253\n"
    "v_sub_f32 v206, v206, v253\n"
    "v_sub_f32 v207, v207, v253\n"
    "v_sub_f32 v208, v208, v253\n"
    "v_sub_f32 v209, v209, v253\n"
    "v_sub_f32 v210, v210, v253\n"
    "v_sub_f32 v211, v211, v253\n"
    "v_sub_f32 v212, v212, v253\n"
    "v_sub_f32 v213, v213, v253\n"
    "v_sub_f32 v214, v214, v253\n"
    "v_sub_f32 v215, v215, v253\n"
    "v_exp_f32 v200, v200\n"
    "v_exp_f32 v201, v201\n"
    "v_exp_f32 v202, v202\n"
    "v_exp_f32 v203, v203\n"
    "v_exp_f32 v204, v204\n"
    "v_exp_f32 v205, v205\n"
    "v_exp_f32 v206, v206\n"
    "v_exp_f32 v207, v207\n"
    "v_exp_f32 v208, v208\n"
    "v_exp_f32 v209, v209\n"
    "v_exp_f32 v210, v210\n"
    "v_exp_f32 v211, v211\n"
    "v_exp_f32 v212, v212\n"
    "v_exp_f32 v213, v213\n"
    "v_exp_f32 v214, v214\n"
    "v_exp_f32 v215, v215\n"
    "v_smfmac_f32_32x32x32_bf16 v[232:247], v[76:79], v[200:207], v252\n"
    "s_nop 0\n"
    "v_smfmac_f32_32x32x32_bf16 v[232:247], v[140:143], v[208:215], v252\n"
    "v_mfma_f32_32x32x16_bf16 v[200:215], v[16:19], v[248:251], 0\n"
    "s_nop 11\n"
    "v_sub_f32 v200, v200, v253\n"
    "v_sub_f32 v201, v201, v253\n"
    "v_sub_f32 v202, v202, v253\n"
    "v_sub_f32 v203, v203, v253\n"
    "v_sub_f32 v204, v204, v253\n"
    "v_sub_f32 v205, v205, v253\n"
    "v_sub_f32 v206, v206, v253\n"
    "v_sub_f32 v207, v207, v253\n"
    "v_sub_f32 v208, v208, v253\n"
    "v_sub_f32 v209, v209, v253\n"
    "v_sub_f32 v210, v210, v253\n"
    "v_sub_f32 v211, v211, v253\n"
    "v_sub_f32 v212, v212, v253\n"
    "v_sub_f32 v213, v213, v253\n"
    "v_sub_f32 v214, v214, v253\n"
    "v_sub_f32 v215, v215, v253\n"
    "v_exp_f32 v200, v200\n"
    "v_exp_f32 v201, v201\n"
    "v_exp_f32 v202, v202\n"
    "v_exp_f32 v203, v203\n"
    "v_exp_f32 v204, v204\n"
    "v_exp_f32 v205, v205\n"
    "v_exp_f32 v206, v206\n"
    "v_exp_f32 v207, v207\n"
    "v_exp_f32 v208, v208\n"
    "v_exp_f32 v209, v209\n"
    "v_exp_f32 v210, v210\n"
    "v_exp_f32 v211, v211\n"
    "v_exp_f32 v212, v212\n"
    "v_exp_f32 v213, v213\n"
    "v_exp_f32 v214, v214\n"
    "v_exp_f32 v215, v215\n"
    "v_smfmac_f32_32x32x32_bf16 v[232:247], v[80:83], v[200:207], v252\n"
    "s_nop 0\n"
    "v_smfmac_f32_32x32x32_bf16 v[232:247], v[144:147], v[208:215], v252\n"
    "v_mfma_f32_32x32x16_bf16 v[200:215], v[20:23], v[248:251], 0\n"
    "s_nop 11\n"
    "v_sub_f32 v200, v200, v253\n"
    "v_sub_f32 v201, v201, v253\n"
    "v_sub_f32 v202, v202, v253\n"
    "v_sub_f32 v203, v203, v253\n"
    "v_sub_f32 v204, v204, v253\n"
    "v_sub_f32 v205, v205, v253\n"
    "v_sub_f32 v206, v206, v253\n"
    "v_sub_f32 v207, v207, v253\n"
    "v_sub_f32 v208, v208, v253\n"
    "v_sub_f32 v209, v209, v253\n"
    "v_sub_f32 v210, v210, v253\n"
    "v_sub_f32 v211, v211, v253\n"
    "v_sub_f32 v212, v212, v253\n"
    "v_sub_f32 v213, v213, v253\n"
    "v_sub_f32 v214, v214, v253\n"
    "v_sub_f32 v215, v215, v253\n"
    "v_exp_f32 v200, v200\n"
    "v_exp_f32 v201, v201\n"
    "v_exp_f32 v202, v202\n"
    "v_exp_f32 v203, v203\n"
    "v_exp_f32 v204, v204\n"
    "v_exp_f32 v205, v205\n"
    "v_exp_f32 v206, v206\n"
    "v_exp_f32 v207, v207\n"
    "v_exp_f32 v208, v208\n"
    "v_exp_f32 v209, v209\n"
    "v_exp_f32 v210, v210\n"
    "v_exp_f32 v211, v211\n"
    "v_exp_f32 v212, v212\n"
    "v_exp_f32 v213, v213\n"
    "v_exp_f32 v214, v214\n"
    "v_exp_f32 v215, v215\n"
    "v_smfmac_f32_32x32x32_bf16 v[232:247], v[84:87], v[200:207], v252\n"
    "s_nop 0\n"
    "v_smfmac_f32_32x32x32_bf16 v[232:247], v[148:151], v[208:215], v252\n"
    "v_mfma_f32_32x32x16_bf16 v[200:215], v[24:27], v[248:251], 0\n"
    "s_nop 11\n"
    "v_sub_f32 v200, v200, v253\n"
    "v_sub_f32 v201, v201, v253\n"
    "v_sub_f32 v202, v202, v253\n"
    "v_sub_f32 v203, v203, v253\n"
    "v_sub_f32 v204, v204, v253\n"
    "v_sub_f32 v205, v205, v253\n"
    "v_sub_f32 v206, v206, v253\n"
    "v_sub_f32 v207, v207, v253\n"
    "v_sub_f32 v208, v208, v253\n"
    "v_sub_f32 v209, v209, v253\n"
    "v_sub_f32 v210, v210, v253\n"
    "v_sub_f32 v211, v211, v253\n"
    "v_sub_f32 v212, v212, v253\n"
    "v_sub_f32 v213, v213, v253\n"
    "v_sub_f32 v214, v214, v253\n"
    "v_sub_f32 v215, v215, v253\n"
    "v_exp_f32 v200, v200\n"
    "v_exp_f32 v201, v201\n"
    "v_exp_f32 v202, v202\n"
    "v_exp_f32 v203, v203\n"
    "v_exp_f32 v204, v204\n"
    "v_exp_f32 v205, v205\n"
    "v_exp_f32 v206, v206\n"
    "v_exp_f32 v207, v207\n"
    "v_exp_f32 v208, v208\n"
    "v_exp_f32 v209, v209\n"
    "v_exp_f32 v210, v210\n"
    "v_exp_f32 v211, v211\n"
    "v_exp_f32 v212, v212\n"
    "v_exp_f32 v213, v213\n"
    "v_exp_f32 v214, v214\n"
    "v_exp_f32 v215, v215\n"
    "v_smfmac_f32_32x32x32_bf16 v[232:247], v[88:91], v[200:207], v252\n"
    "s_nop 0\n"
    "v_smfmac_f32_32x32x32_bf16 v[232:247], v[152:155], v[208:215], v252\n"
    "v_mfma_f32_32x32x16_bf16 v[200:215], v[28:31], v[248:251], 0\n"
    "s_nop 11\n"
    "v_sub_f32 v200, v200, v253\n"
    "v_sub_f32 v201, v201, v253\n"
    "v_sub_f32 v202, v202, v253\n"
    "v_sub_f32 v203, v203, v253\n"
    "v_sub_f32 v204, v204, v253\n"
    "v_sub_f32 v205, v205, v253\n"
    "v_sub_f32 v206, v206, v253\n"
    "v_sub_f32 v207, v207, v253\n"
    "v_sub_f32 v208, v208, v253\n"
    "v_sub_f32 v209, v209, v253\n"
    "v_sub_f32 v210, v210, v253\n"
    "v_sub_f32 v211, v211, v253\n"
    "v_sub_f32 v212, v212, v253\n"
    "v_sub_f32 v213, v213, v253\n"
    "v_sub_f32 v214, v214, v253\n"
    "v_sub_f32 v215, v215, v253\n"
    "v_exp_f32 v200, v200\n"
    "v_exp_f32 v201, v201\n"
    "v_exp_f32 v202, v202\n"
    "v_exp_f32 v203, v203\n"
    "v_exp_f32 v204, v204\n"
    "v_exp_f32 v205, v205\n"
    "v_exp_f32 v206, v206\n"
    "v_exp_f32 v207, v207\n"
    "v_exp_f32 v208, v208\n"
    "v_exp_f32 v209, v209\n"
    "v_exp_f32 v210, v210\n"
    "v_exp_f32 v211, v211\n"
    "v_exp_f32 v212, v212\n"
    "v_exp_f32 v213, v213\n"
    "v_exp_f32 v214, v214\n"
    "v_exp_f32 v215, v215\n"
    "v_smfmac_f32_32x32x32_bf16 v[232:247], v[92:95], v[200:207], v252\n"
    "s_nop 0\n"
    "v_smfmac_f32_32x32x32_bf16 v[232:247], v[156:159], v[208:215], v252\n"
    "v_mfma_f32_32x32x16_bf16 v[200:215], v[32:35], v[248:251], 0\n"
    "s_nop 11\n"
    "v_sub_f32 v200, v200, v253\n"
    "v_sub_f32 v201, v201, v253\n"
    "v_sub_f32 v202, v202, v253\n"
    "v_sub_f32 v203, v203, v253\n"
    "v_sub_f32 v204, v204, v253\n"
    "v_sub_f32 v205, v205, v253\n"
    "v_sub_f32 v206, v206, v253\n"
    "v_sub_f32 v207, v207, v253\n"
    "v_sub_f32 v208, v208, v253\n"
    "v_sub_f32 v209, v209, v253\n"
    "v_sub_f32 v210, v210, v253\n"
    "v_sub_f32 v211, v211, v253\n"
    "v_sub_f32 v212, v212, v253\n"
    "v_sub_f32 v213, v213, v253\n"
    "v_sub_f32 v214, v214, v253\n"
    "v_sub_f32 v215, v215, v253\n"
    "v_exp_f32 v200, v200\n"
    "v_exp_f32 v201, v201\n"
    "v_exp_f32 v202, v202\n"
    "v_exp_f32 v203, v203\n"
    "v_exp_f32 v204, v204\n"
    "v_exp_f32 v205, v205\n"
    "v_exp_f32 v206, v206\n"
    "v_exp_f32 v207, v207\n"
    "v_exp_f32 v208, v208\n"
    "v_exp_f32 v209, v209\n"
    "v_exp_f32 v210, v210\n"
    "v_exp_f32 v211, v211\n"
    "v_exp_f32 v212, v212\n"
    "v_exp_f32 v213, v213\n"
    "v_exp_f32 v214, v214\n"
    "v_exp_f32 v215, v215\n"
    "v_smfmac_f32_32x32x32_bf16 v[232:247], v[96:99], v[200:207], v252\n"
    "s_nop 0\n"
    "v_smfmac_f32_32x32x32_bf16 v[232:247], v[160:163], v[208:215], v252\n"
    "v_mfma_f32_32x32x16_bf16 v[200:215], v[36:39], v[248:251], 0\n"
    "s_nop 11\n"
    "v_sub_f32 v200, v200, v253\n"
    "v_sub_f32 v201, v201, v253\n"
    "v_sub_f32 v202, v202, v253\n"
    "v_sub_f32 v203, v203, v253\n"
    "v_sub_f32 v204, v204, v253\n"
    "v_sub_f32 v205, v205, v253\n"
    "v_sub_f32 v206, v206, v253\n"
    "v_sub_f32 v207, v207, v253\n"
    "v_sub_f32 v208, v208, v253\n"
    "v_sub_f32 v209, v209, v253\n"
    "v_sub_f32 v210, v210, v253\n"
    "v_sub_f32 v211, v211, v253\n"
    "v_sub_f32 v212, v212, v253\n"
    "v_sub_f32 v213, v213, v253\n"
    "v_sub_f32 v214, v214, v253\n"
    "v_sub_f32 v215, v215, v253\n"
    "v_exp_f32 v200, v200\n"
    "v_exp_f32 v201, v201\n"
    "v_exp_f32 v202, v202\n"
    "v_exp_f32 v203, v203\n"
    "v_exp_f32 v204, v204\n"
    "v_exp_f32 v205, v205\n"
    "v_exp_f32 v206, v206\n"
    "v_exp_f32 v207, v207\n"
    "v_exp_f32 v208, v208\n"
    "v_exp_f32 v209, v209\n"
    "v_exp_f32 v210, v210\n"
    "v_exp_f32 v211, v211\n"
    "v_exp_f32 v212, v212\n"
    "v_exp_f32 v213, v213\n"
    "v_exp_f32 v214, v214\n"
    "v_exp_f32 v215, v215\n"
    "v_smfmac_f32_32x32x32_bf16 v[232:247], v[100:103], v[200:207], v252\n"
    "s_nop 0\n"
    "v_smfmac_f32_32x32x32_bf16 v[232:247], v[164:167], v[208:215], v252\n"
    "v_mfma_f32_32x32x16_bf16 v[200:215], v[40:43], v[248:251], 0\n"
    "s_nop 11\n"
    "v_sub_f32 v200, v200, v253\n"
    "v_sub_f32 v201, v201, v253\n"
    "v_sub_f32 v202, v202, v253\n"
    "v_sub_f32 v203, v203, v253\n"
    "v_sub_f32 v204, v204, v253\n"
    "v_sub_f32 v205, v205, v253\n"
    "v_sub_f32 v206, v206, v253\n"
    "v_sub_f32 v207, v207, v253\n"
    "v_sub_f32 v208, v208, v253\n"
    "v_sub_f32 v209, v209, v253\n"
    "v_sub_f32 v210, v210, v253\n"
    "v_sub_f32 v211, v211, v253\n"
    "v_sub_f32 v212, v212, v253\n"
    "v_sub_f32 v213, v213, v253\n"
    "v_sub_f32 v214, v214, v253\n"
    "v_sub_f32 v215, v215, v253\n"
    "v_exp_f32 v200, v200\n"
    "v_exp_f32 v201, v201\n"
    "v_exp_f32 v202, v202\n"
    "v_exp_f32 v203, v203\n"
    "v_exp_f32 v204, v204\n"
    "v_exp_f32 v205, v205\n"
    "v_exp_f32 v206, v206\n"
    "v_exp_f32 v207, v207\n"
    "v_exp_f32 v208, v208\n"
    "v_exp_f32 v209, v209\n"
    "v_exp_f32 v210, v210\n"
    "v_exp_f32 v211, v211\n"
    "v_exp_f32 v212, v212\n"
    "v_exp_f32 v213, v213\n"
    "v_exp_f32 v214, v214\n"
    "v_exp_f32 v215, v215\n"
    "v_smfmac_f32_32x32x32_bf16 v[232:247], v[104:107], v[200:207], v252\n"
    "s_nop 0\n"
    "v_smfmac_f32_32x32x32_bf16 v[232:247], v[168:171], v[208:215], v252\n"
    "v_mfma_f32_32x32x16_bf16 v[200:215], v[44:47], v[248:251], 0\n"
    "s_nop 11\n"
    "v_sub_f32 v200, v200, v253\n"
    "v_sub_f32 v201, v201, v253\n"
    "v_sub_f32 v202, v202, v253\n"
    "v_sub_f32 v203, v203, v253\n"
    "v_sub_f32 v204, v204, v253\n"
    "v_sub_f32 v205, v205, v253\n"
    "v_sub_f32 v206, v206, v253\n"
    "v_sub_f32 v207, v207, v253\n"
    "v_sub_f32 v208, v208, v253\n"
    "v_sub_f32 v209, v209, v253\n"
    "v_sub_f32 v210, v210, v253\n"
    "v_sub_f32 v211, v211, v253\n"
    "v_sub_f32 v212, v212, v253\n"
    "v_sub_f32 v213, v213, v253\n"
    "v_sub_f32 v214, v214, v253\n"
    "v_sub_f32 v215, v215, v253\n"
    "v_exp_f32 v200, v200\n"
    "v_exp_f32 v201, v201\n"
    "v_exp_f32 v202, v202\n"
    "v_exp_f32 v203, v203\n"
    "v_exp_f32 v204, v204\n"
    "v_exp_f32 v205, v205\n"
    "v_exp_f32 v206, v206\n"
    "v_exp_f32 v207, v207\n"
    "v_exp_f32 v208, v208\n"
    "v_exp_f32 v209, v209\n"
    "v_exp_f32 v210, v210\n"
    "v_exp_f32 v211, v211\n"
    "v_exp_f32 v212, v212\n"
    "v_exp_f32 v213, v213\n"
    "v_exp_f32 v214, v214\n"
    "v_exp_f32 v215, v215\n"
    "v_smfmac_f32_32x32x32_bf16 v[232:247], v[108:111], v[200:207], v252\n"
    "s_nop 0\n"
    "v_smfmac_f32_32x32x32_bf16 v[232:247], v[172:175], v[208:215], v252\n"
    "v_mfma_f32_32x32x16_bf16 v[200:215], v[48:51], v[248:251], 0\n"
    "s_nop 11\n"
    "v_sub_f32 v200, v200, v253\n"
    "v_sub_f32 v201, v201, v253\n"
    "v_sub_f32 v202, v202, v253\n"
    "v_sub_f32 v203, v203, v253\n"
    "v_sub_f32 v204, v204, v253\n"
    "v_sub_f32 v205, v205, v253\n"
    "v_sub_f32 v206, v206, v253\n"
    "v_sub_f32 v207, v207, v253\n"
    "v_sub_f32 v208, v208, v253\n"
    "v_sub_f32 v209, v209, v253\n"
    "v_sub_f32 v210, v210, v253\n"
    "v_sub_f32 v211, v211, v253\n"
    "v_sub_f32 v212, v212, v253\n"
    "v_sub_f32 v213, v213, v253\n"
    "v_sub_f32 v214, v214, v253\n"
    "v_sub_f32 v215, v215, v253\n"
    "v_exp_f32 v200, v200\n"
    "v_exp_f32 v201, v201\n"
    "v_exp_f32 v202, v202\n"
    "v_exp_f32 v203, v203\n"
    "v_exp_f32 v204, v204\n"
    "v_exp_f32 v205, v205\n"
    "v_exp_f32 v206, v206\n"
    "v_exp_f32 v207, v207\n"
    "v_exp_f32 v208, v208\n"
    "v_exp_f32 v209, v209\n"
    "v_exp_f32 v210, v210\n"
    "v_exp_f32 v211, v211\n"
    "v_exp_f32 v212, v212\n"
    "v_exp_f32 v213, v213\n"
    "v_exp_f32 v214, v214\n"
    "v_exp_f32 v215, v215\n"
    "v_smfmac_f32_32x32x32_bf16 v[232:247], v[112:115], v[200:207], v252\n"
    "s_nop 0\n"
    "v_smfmac_f32_32x32x32_bf16 v[232:247], v[176:179], v[208:215], v252\n"
    "v_mfma_f32_32x32x16_bf16 v[200:215], v[52:55], v[248:251], 0\n"
    "s_nop 11\n"
    "v_sub_f32 v200, v200, v253\n"
    "v_sub_f32 v201, v201, v253\n"
    "v_sub_f32 v202, v202, v253\n"
    "v_sub_f32 v203, v203, v253\n"
    "v_sub_f32 v204, v204, v253\n"
    "v_sub_f32 v205, v205, v253\n"
    "v_sub_f32 v206, v206, v253\n"
    "v_sub_f32 v207, v207, v253\n"
    "v_sub_f32 v208, v208, v253\n"
    "v_sub_f32 v209, v209, v253\n"
    "v_sub_f32 v210, v210, v253\n"
    "v_sub_f32 v211, v211, v253\n"
    "v_sub_f32 v212, v212, v253\n"
    "v_sub_f32 v213, v213, v253\n"
    "v_sub_f32 v214, v214, v253\n"
    "v_sub_f32 v215, v215, v253\n"
    "v_exp_f32 v200, v200\n"
    "v_exp_f32 v201, v201\n"
    "v_exp_f32 v202, v202\n"
    "v_exp_f32 v203, v203\n"
    "v_exp_f32 v204, v204\n"
    "v_exp_f32 v205, v205\n"
    "v_exp_f32 v206, v206\n"
    "v_exp_f32 v207, v207\n"
    "v_exp_f32 v208, v208\n"
    "v_exp_f32 v209, v209\n"
    "v_exp_f32 v210, v210\n"
    "v_exp_f32 v211, v211\n"
    "v_exp_f32 v212, v212\n"
    "v_exp_f32 v213, v213\n"
    "v_exp_f32 v214, v214\n"
    "v_exp_f32 v215, v215\n"
    "v_smfmac_f32_32x32x32_bf16 v[232:247], v[116:119], v[200:207], v252\n"
    "s_nop 0\n"
    "v_smfmac_f32_32x32x32_bf16 v[232:247], v[180:183], v[208:215], v252\n"
    "v_mfma_f32_32x32x16_bf16 v[200:215], v[56:59], v[248:251], 0\n"
    "s_nop 11\n"
    "v_sub_f32 v200, v200, v253\n"
    "v_sub_f32 v201, v201, v253\n"
    "v_sub_f32 v202, v202, v253\n"
    "v_sub_f32 v203, v203, v253\n"
    "v_sub_f32 v204, v204, v253\n"
    "v_sub_f32 v205, v205, v253\n"
    "v_sub_f32 v206, v206, v253\n"
    "v_sub_f32 v207, v207, v253\n"
    "v_sub_f32 v208, v208, v253\n"
    "v_sub_f32 v209, v209, v253\n"
    "v_sub_f32 v210, v210, v253\n"
    "v_sub_f32 v211, v211, v253\n"
    "v_sub_f32 v212, v212, v253\n"
    "v_sub_f32 v213, v213, v253\n"
    "v_sub_f32 v214, v214, v253\n"
    "v_sub_f32 v215, v215, v253\n"
    "v_exp_f32 v200, v200\n"
    "v_exp_f32 v201, v201\n"
    "v_exp_f32 v202, v202\n"
    "v_exp_f32 v203, v203\n"
    "v_exp_f32 v204, v204\n"
    "v_exp_f32 v205, v205\n"
    "v_exp_f32 v206, v206\n"
    "v_exp_f32 v207, v207\n"
    "v_exp_f32 v208, v208\n"
    "v_exp_f32 v209, v209\n"
    "v_exp_f32 v210, v210\n"
    "v_exp_f32 v211, v211\n"
    "v_exp_f32 v212, v212\n"
    "v_exp_f32 v213, v213\n"
    "v_exp_f32 v214, v214\n"
    "v_exp_f32 v215, v215\n"
    "v_smfmac_f32_32x32x32_bf16 v[232:247], v[120:123], v[200:207], v252\n"
    "s_nop 0\n"
    "v_smfmac_f32_32x32x32_bf16 v[232:247], v[184:187], v[208:215], v252\n"
    "v_mfma_f32_32x32x16_bf16 v[200:215], v[60:63], v[248:251], 0\n"
    "s_nop 11\n"
    "v_sub_f32 v200, v200, v253\n"
    "v_sub_f32 v201, v201, v253\n"
    "v_sub_f32 v202, v202, v253\n"
    "v_sub_f32 v203, v203, v253\n"
    "v_sub_f32 v204, v204, v253\n"
    "v_sub_f32 v205, v205, v253\n"
    "v_sub_f32 v206, v206, v253\n"
    "v_sub_f32 v207, v207, v253\n"
    "v_sub_f32 v208, v208, v253\n"
    "v_sub_f32 v209, v209, v253\n"
    "v_sub_f32 v210, v210, v253\n"
    "v_sub_f32 v211, v211, v253\n"
    "v_sub_f32 v212, v212, v253\n"
    "v_sub_f32 v213, v213, v253\n"
    "v_sub_f32 v214, v214, v253\n"
    "v_sub_f32 v215, v215, v253\n"
    "v_exp_f32 v200, v200\n"
    "v_exp_f32 v201, v201\n"
    "v_exp_f32 v202, v202\n"
    "v_exp_f32 v203, v203\n"
    "v_exp_f32 v204, v204\n"
    "v_exp_f32 v205, v205\n"
    "v_exp_f32 v206, v206\n"
    "v_exp_f32 v207, v207\n"
    "v_exp_f32 v208, v208\n"
    "v_exp_f32 v209, v209\n"
    "v_exp_f32 v210, v210\n"
    "v_exp_f32 v211, v211\n"
    "v_exp_f32 v212, v212\n"
    "v_exp_f32 v213, v213\n"
    "v_exp_f32 v214, v214\n"
    "v_exp_f32 v215, v215\n"
    "v_smfmac_f32_32x32x32_bf16 v[232:247], v[124:127], v[200:207], v252\n"
    "s_nop 0\n"
    "v_smfmac_f32_32x32x32_bf16 v[232:247], v[188:191], v[208:215], v252\n"
    "v_mfma_f32_32x32x16_bf16 v[200:215], v[64:67], v[248:251], 0\n"
    "s_nop 11\n"
    "v_sub_f32 v200, v200, v253\n"
    "v_sub_f32 v201, v201, v253\n"
    "v_sub_f32 v202, v202, v253\n"
    "v_sub_f32 v203, v203, v253\n"
    "v_sub_f32 v204, v204, v253\n"
    "v_sub_f32 v205, v205, v253\n"
    "v_sub_f32 v206, v206, v253\n"
    "v_sub_f32 v207, v207, v253\n"
    "v_sub_f32 v208, v208, v253\n"
    "v_sub_f32 v209, v209, v253\n"
    "v_sub_f32 v210, v210, v253\n"
    "v_sub_f32 v211, v211, v253\n"
    "v_sub_f32 v212, v212, v253\n"
    "v_sub_f32 v213, v213, v253\n"
    "v_sub_f32 v214, v214, v253\n"
    "v_sub_f32 v215, v215, v253\n"
    "v_exp_f32 v200, v200\n"
    "v_exp_f32 v201, v201\n"
    "v_exp_f32 v202, v202\n"
    "v_exp_f32 v203, v203\n"
    "v_exp_f32 v204, v204\n"
    "v_exp_f32 v205, v205\n"
    "v_exp_f32 v206, v206\n"
    "v_exp_f32 v207, v207\n"
    "v_exp_f32 v208, v208\n"
    "v_exp_f32 v209, v209\n"
    "v_exp_f32 v210, v210\n"
    "v_exp_f32 v211, v211\n"
    "v_exp_f32 v212, v212\n"
    "v_exp_f32 v213, v213\n"
    "v_exp_f32 v214, v214\n"
    "v_exp_f32 v215, v215\n"
    "v_smfmac_f32_32x32x32_bf16 v[232:247], v[128:131], v[200:207], v252\n"
    "s_nop 0\n"
    "v_smfmac_f32_32x32x32_bf16 v[232:247], v[192:195], v[208:215], v252\n"
    "v_mfma_f32_32x32x16_bf16 v[200:215], v[68:71], v[248:251], 0\n"
    "s_nop 11\n"
    "v_sub_f32 v200, v200, v253\n"
    "v_sub_f32 v201, v201, v253\n"
    "v_sub_f32 v202, v202, v253\n"
    "v_sub_f32 v203, v203, v253\n"
    "v_sub_f32 v204, v204, v253\n"
    "v_sub_f32 v205, v205, v253\n"
    "v_sub_f32 v206, v206, v253\n"
    "v_sub_f32 v207, v207, v253\n"
    "v_sub_f32 v208, v208, v253\n"
    "v_sub_f32 v209, v209, v253\n"
    "v_sub_f32 v210, v210, v253\n"
    "v_sub_f32 v211, v211, v253\n"
    "v_sub_f32 v212, v212, v253\n"
    "v_sub_f32 v213, v213, v253\n"
    "v_sub_f32 v214, v214, v253\n"
    "v_sub_f32 v215, v215, v253\n"
    "v_exp_f32 v200, v200\n"
    "v_exp_f32 v201, v201\n"
    "v_exp_f32 v202, v202\n"
    "v_exp_f32 v203, v203\n"
    "v_exp_f32 v204, v204\n"
    "v_exp_f32 v205, v205\n"
    "v_exp_f32 v206, v206\n"
    "v_exp_f32 v207, v207\n"
    "v_exp_f32 v208, v208\n"
    "v_exp_f32 v209, v209\n"
    "v_exp_f32 v210, v210\n"
    "v_exp_f32 v211, v211\n"
    "v_exp_f32 v212, v212\n"
    "v_exp_f32 v213, v213\n"
    "v_exp_f32 v214, v214\n"
    "v_exp_f32 v215, v215\n"
    "v_smfmac_f32_32x32x32_bf16 v[232:247], v[132:135], v[200:207], v252\n"
    "s_nop 0\n"
    "v_smfmac_f32_32x32x32_bf16 v[232:247], v[196:199], v[208:215], v252\n"
    "s_cmp_lt_u32 s88, 4\n"
    "s_cbranch_scc1 .L_sl3_k\n"
    "s_and_b32 s76, s88, 3\n"
    "s_lshl_b32 s76, s76, 2\n"
    "v_add_u32 v254, s76, v7\n"
    "s_mov_b32 s96, 131072\n"
    ".Lsp_21_k:\n"
    "ds_read_b32 v255, v254 offset:64\n"
    "s_waitcnt lgkmcnt(0)\n"
    "v_readfirstlane_b32 s94, v255\n"
    "s_cmp_eq_u32 s94, 0x1\n"
    "s_cbranch_scc1 .Lspd_22_k\n"
    "s_sleep 1\n"
    "s_sub_u32 s96, s96, 1\n"
    "s_cbranch_scc0 .Lsp_21_k\n"
    ".Lspd_22_k:\n"
    ".L_sl3_k:\n"
    "s_nop 5\n"
    "s_and_b32 s76, s88, 3\n"
    "s_mul_i32 s76, s76, 9216\n"
    "v_add_u32 v254, s76, v3\n"
    "v_add_u32 v255, s76, v4\n"
    "ds_write_b128 v254, v[232:235]\n"
    "ds_write_b128 v254, v[236:239] offset:1024\n"
    "s_mov_b32 exec_hi, 0\n"
    "ds_write_b32 v255, v240\n"
    "s_mov_b64 exec, -1\n"
    "s_add_u32 s88, s88, 1\n"
    "v_mov_b32 v254, s88\n"
    "ds_write_b8 v5, v254 offset:36\n"
    "v_add_u32 v2, 1024, v2\n"
    "v_add_u32 v6, 512, v6\n"
    "s_cmp_lt_u32 s88, 8\n"
    "s_cbranch_scc1 .L_sl2_k\n"
    "s_branch .L_end_k\n"
    ".L_gemm_k:\n"
    "s_mov_b32 s24, s54\n"
    "s_and_b32 s25, s55, 0xffff\n"
    "s_mov_b32 s26, 524288\n"
    "s_mov_b32 s27, 0x20000\n"
    "s_mov_b32 s48, s68\n"
    "s_and_b32 s49, s69, 0xffff\n"
    "s_mov_b32 s50, 12582912\n"
    "s_mov_b32 s51, 0x20000\n"
    "v_and_b32 v112, 6, v3\n"
    "v_xor_b32 v112, v112, v4\n"
    "v_lshlrev_b32 v112, 4, v112\n"
    "v_and_b32 v113, 3, v3\n"
    "v_lshrrev_b32 v114, 2, v3\n"
    "v_lshl_or_b32 v113, v114, 3, v113\n"
    "v_lshl_or_b32 v204, v113, 10, v112\n"
    "v_add_u32 v205, 15360, v204\n"
    "v_add_u32 v206, 2048, v204\n"
    "v_add_u32 v207, 17408, v204\n"
    "s_lshl_b32 s78, s75, 17\n"
    "s_lshl_b32 s77, s74, 7\n"
    "s_add_u32 s78, s78, s77\n"
    "s_xor_b32 s79, s75, 2\n"
    "s_lshl_b32 s79, s79, 14\n"
    "s_add_u32 s79, s79, 73472\n"
    "s_mov_b32 s36, s62\n"
    "s_and_b32 s37, s63, 0xffff\n"
    "s_mov_b32 s38, 1835008\n"
    "s_mov_b32 s39, 0x20000\n"
    "s_mov_b32 s32, s60\n"
    "s_and_b32 s33, s61, 0xffff\n"
    "s_mov_b32 s34, 524288\n"
    "s_mov_b32 s35, 0x20000\n"
    "s_mov_b32 s44, s66\n"
    "s_and_b32 s45, s67, 0xffff\n"
    "s_mov_b32 s46, 2097152\n"
    "s_mov_b32 s47, 0x20000\n"
    "s_mov_b32 s40, s64\n"
    "s_and_b32 s41, s65, 0xffff\n"
    "s_mov_b32 s42, 1835008\n"
    "s_mov_b32 s43, 0x20000\n"
    "s_movk_i32 s76, 3584\n"
    "v_mad_u32_u24 v224, v113, s76, v112\n"
    "v_add_u32 v225, 56320, v224\n"
    "v_add_u32 v226, 12288, v224\n"
    "v_add_u32 v227, 68608, v224\n"
    "v_lshl_or_b32 v228, v3, 10, v112\n"
    "v_add_u32 v229, 7168, v228\n"
    "v_add_u32 v230, 14336, v228\n"
    "v_add_u32 v231, 21504, v228\n"
    "v_mul_u32_u24 v253, s76, v1\n"
    "s_lshl_b32 s76, s74, 2\n"
    "s_mul_i32 s77, s74, 3\n"
    "s_add_u32 s77, s77, 4\n"
    "s_cmp_lt_u32 s74, 4\n"
    "s_cselect_b32 s82, s76, s77\n"
    "s_cselect_b32 s76, 7, 3\n"
    "s_add_u32 s82, s82, s75\n"
    "s_cmp_eq_u32 s75, s76\n"
    "s_cselect_b32 s82, 28, s82\n"
    "s_cselect_b32 s80, 0, s82\n"
    "s_lshl_b32 s80, s80, 7\n"
    "s_lshl_b32 s81, s75, 12\n"
    "s_add_u32 s81, s81, 106240\n"
    "s_and_b32 s77, s73, 7\n"
    "s_mul_i32 s77, s77, 229376\n"
    "s_add_u32 s83, s77, s80\n"
    "v_lshrrev_b32 v113, 4, v1\n"
    "v_and_b32 v236, 15, v1\n"
    "v_xor_b32 v236, v236, v113\n"
    "v_lshlrev_b32 v236, 4, v236\n"
    "v_lshl_or_b32 v236, v113, 10, v236\n"
    "v_xor_b32 v237, 64, v236\n"
    "v_lshrrev_b32 v113, 2, v1\n"
    "v_lshlrev_b32 v232, 7, v113\n"
    "v_bfe_u32 v113, v1, 3, 2\n"
    "v_lshl_or_b32 v232, v113, 5, v232\n"
    "v_and_b32 v113, 3, v1\n"
    "v_lshl_or_b32 v232, v113, 3, v232\n"
    "v_add_u32 v232, s81, v232\n"
    "v_xor_b32 v233, 32, v232\n"
    "v_xor_b32 v234, 64, v232\n"
    "v_xor_b32 v235, 96, v232\n"
    "v_lshlrev_b32 v244, 4, v1\n"
    "v_mov_b32 v243, 163584\n"
    "s_lshl_b32 s76, s75, 2\n"
    "s_add_u32 s76, s76, 163584\n"
    "v_mov_b32 v242, s76\n"
    "v_add_u32 v249, s75, v243\n"
    "s_add_u32 s76, s75, 0\n"
    "s_and_b32 s76, s76, 3\n"
    "s_lshl_b32 s76, s76, 10\n"
    "v_add_u32 v238, s76, v244\n"
    "s_add_u32 s76, s75, 1\n"
    "s_and_b32 s76, s76, 3\n"
    "s_lshl_b32 s76, s76, 10\n"
    "v_add_u32 v239, s76, v244\n"
    "s_add_u32 s76, s75, 2\n"
    "s_and_b32 s76, s76, 3\n"
    "s_lshl_b32 s76, s76, 10\n"
    "v_add_u32 v240, s76, v244\n"
    "s_add_u32 s76, s75, 3\n"
    "s_and_b32 s76, s76, 3\n"
    "s_lshl_b32 s76, s76, 10\n"
    "v_add_u32 v241, s76, v244\n"
    "v_and_b32 v112, 15, v1\n"
    "v_lshlrev_b32 v113, 10, v112\n"
    "v_lshrrev_b32 v114, 4, v1\n"
    "v_lshl_or_b32 v245, v114, 3, v113\n"
    "v_lshlrev_b32 v112, 4, v112\n"
    "s_mul_i32 s76, s75, 9216\n"
    "s_add_u32 s76, s76, 65536\n"
    "v_add_u32 v247, s76, v112\n"
    "v_bfe_u32 v113, v1, 4, 1\n"
    "v_lshl_or_b32 v112, v113, 9, v112\n"
    "v_lshrrev_b32 v113, 5, v1\n"
    "v_lshl_or_b32 v112, v113, 10, v112\n"
    "v_add_u32 v246, s76, v112\n"
    "v_and_b32 v112, 7, v1\n"
    "v_lshlrev_b32 v248, 3, v112\n"
    "s_lshl_b32 s76, s73, 2\n"
    "s_add_u32 s76, s76, s75\n"
    "s_lshl_b32 s85, s76, 14\n"
    "s_add_u32 s86, s85, 1048576\n"
    "s_add_u32 s93, s85, 3145728\n"
    "s_lshl_b32 s77, s74, 7\n"
    "s_add_u32 s85, s85, s77\n"
    "s_lshl_b32 s87, s73, 9\n"
    "s_lshl_b32 s76, s75, 6\n"
    "s_add_u32 s87, s87, s76\n"
    "s_add_u32 s87, s87, 16384\n"
    "s_mov_b32 s90, 0\n"
    "s_add_u32 s88, s75, 1\n"
    "s_mul_i32 s88, s88, 0x01010101\n"
    "buffer_load_dwordx4 v[8:11], v5, s[20:23], s14 offen offset:0\n"
    "buffer_load_dwordx4 v[12:15], v5, s[20:23], s14 offen offset:128\n"
    "buffer_load_dwordx4 v[16:19], v5, s[20:23], s14 offen offset:256\n"
    "buffer_load_dwordx4 v[20:23], v5, s[20:23], s14 offen offset:384\n"
    "buffer_load_dwordx4 v[24:27], v5, s[20:23], s14 offen offset:512\n"
    "buffer_load_dwordx4 v[28:31], v5, s[20:23], s14 offen offset:640\n"
    "buffer_load_dwordx4 v[32:35], v5, s[20:23], s14 offen offset:768\n"
    "buffer_load_dwordx4 v[36:39], v5, s[20:23], s14 offen offset:896\n"
    "s_add_u32 m0, s79, 0\n"
    "s_nop 0\n"
    "buffer_load_dwordx4 v204, s[24:27], s78 offen offset:0 lds\n"
    "buffer_load_dwordx4 v205, s[24:27], s78 offen offset:1024 lds\n"
    "buffer_load_dwordx4 v206, s[24:27], s78 offen offset:2048 lds\n"
    "buffer_load_dwordx4 v207, s[24:27], s78 offen offset:3072 lds\n"
    "s_add_u32 s78, s78, 0x8000\n"
    "s_add_u32 m0, s79, 4096\n"
    "s_nop 0\n"
    "buffer_load_dwordx4 v204, s[24:27], s78 offen offset:0 lds\n"
    "buffer_load_dwordx4 v205, s[24:27], s78 offen offset:1024 lds\n"
    "buffer_load_dwordx4 v206, s[24:27], s78 offen offset:2048 lds\n"
    "buffer_load_dwordx4 v207, s[24:27], s78 offen offset:3072 lds\n"
    "s_add_u32 s78, s78, 0x8000\n"
    "s_add_u32 m0, s79, 8192\n"
    "s_nop 0\n"
    "buffer_load_dwordx4 v204, s[24:27], s78 offen offset:0 lds\n"
    "buffer_load_dwordx4 v205, s[24:27], s78 offen offset:1024 lds\n"
    "buffer_load_dwordx4 v206, s[24:27], s78 offen offset:2048 lds\n"
    "buffer_load_dwordx4 v207, s[24:27], s78 offen offset:3072 lds\n"
    "s_add_u32 s78, s78, 0x8000\n"
    "s_add_u32 m0, s79, 12288\n"
    "s_nop 0\n"
    "buffer_load_dwordx4 v204, s[24:27], s78 offen offset:0 lds\n"
    "buffer_load_dwordx4 v205, s[24:27], s78 offen offset:1024 lds\n"
    "buffer_load_dwordx4 v206, s[24:27], s78 offen offset:2048 lds\n"
    "buffer_load_dwordx4 v207, s[24:27], s78 offen offset:3072 lds\n"
    "s_add_u32 s78, s78, 0x8000\n"
    "s_waitcnt vmcnt(16)\n"
    "s_waitcnt lgkmcnt(0)\n"
    "s_add_u32 s70, s70, 1\n"
    "s_addc_u32 s71, s71, 0\n"
    "s_mov_b32 s76, 0xffff0000\n"
    "v_lshlrev_b32 v40, 16, v8\n"
    "v_and_b32 v41, s76, v8\n"
    "v_lshlrev_b32 v42, 16, v9\n"
    "v_and_b32 v43, s76, v9\n"
    "v_lshlrev_b32 v44, 16, v10\n"
    "v_and_b32 v45, s76, v10\n"
    "v_lshlrev_b32 v46, 16, v11\n"
    "v_and_b32 v47, s76, v11\n"
    "v_mov_b64 v[104:105], v[40:41]\n"
    "v_pk_mul_f32 v[108:109], v[40:41], v[40:41]\n"
    "v_mov_b64 v[106:107], v[42:43]\n"
    "v_pk_mul_f32 v[110:111], v[42:43], v[42:43]\n"
    "v_pk_add_f32 v[104:105], v[104:105], v[44:45]\n"
    "v_pk_fma_f32 v[108:109], v[44:45], v[44:45], v[108:109]\n"
    "v_pk_add_f32 v[106:107], v[106:107], v[46:47]\n"
    "v_pk_fma_f32 v[110:111], v[46:47], v[46:47], v[110:111]\n"
    "v_lshlrev_b32 v48, 16, v12\n"
    "v_and_b32 v49, s76, v12\n"
    "v_lshlrev_b32 v50, 16, v13\n"
    "v_and_b32 v51, s76, v13\n"
    "v_lshlrev_b32 v52, 16, v14\n"
    "v_and_b32 v53, s76, v14\n"
    "v_lshlrev_b32 v54, 16, v15\n"
    "v_and_b32 v55, s76, v15\n"
    "v_pk_add_f32 v[104:105], v[104:105], v[48:49]\n"
    "v_pk_fma_f32 v[108:109], v[48:49], v[48:49], v[108:109]\n"
    "v_pk_add_f32 v[106:107], v[106:107], v[50:51]\n"
    "v_pk_fma_f32 v[110:111], v[50:51], v[50:51], v[110:111]\n"
    "v_pk_add_f32 v[104:105], v[104:105], v[52:53]\n"
    "v_pk_fma_f32 v[108:109], v[52:53], v[52:53], v[108:109]\n"
    "v_pk_add_f32 v[106:107], v[106:107], v[54:55]\n"
    "v_pk_fma_f32 v[110:111], v[54:55], v[54:55], v[110:111]\n"
    "v_lshlrev_b32 v56, 16, v16\n"
    "v_and_b32 v57, s76, v16\n"
    "v_lshlrev_b32 v58, 16, v17\n"
    "v_and_b32 v59, s76, v17\n"
    "v_lshlrev_b32 v60, 16, v18\n"
    "v_and_b32 v61, s76, v18\n"
    "v_lshlrev_b32 v62, 16, v19\n"
    "v_and_b32 v63, s76, v19\n"
    "v_pk_add_f32 v[104:105], v[104:105], v[56:57]\n"
    "v_pk_fma_f32 v[108:109], v[56:57], v[56:57], v[108:109]\n"
    "v_pk_add_f32 v[106:107], v[106:107], v[58:59]\n"
    "v_pk_fma_f32 v[110:111], v[58:59], v[58:59], v[110:111]\n"
    "v_pk_add_f32 v[104:105], v[104:105], v[60:61]\n"
    "v_pk_fma_f32 v[108:109], v[60:61], v[60:61], v[108:109]\n"
    "v_pk_add_f32 v[106:107], v[106:107], v[62:63]\n"
    "v_pk_fma_f32 v[110:111], v[62:63], v[62:63], v[110:111]\n"
    "v_lshlrev_b32 v64, 16, v20\n"
    "v_and_b32 v65, s76, v20\n"
    "v_lshlrev_b32 v66, 16, v21\n"
    "v_and_b32 v67, s76, v21\n"
    "v_lshlrev_b32 v68, 16, v22\n"
    "v_and_b32 v69, s76, v22\n"
    "v_lshlrev_b32 v70, 16, v23\n"
    "v_and_b32 v71, s76, v23\n"
    "v_pk_add_f32 v[104:105], v[104:105], v[64:65]\n"
    "v_pk_fma_f32 v[108:109], v[64:65], v[64:65], v[108:109]\n"
    "v_pk_add_f32 v[106:107], v[106:107], v[66:67]\n"
    "v_pk_fma_f32 v[110:111], v[66:67], v[66:67], v[110:111]\n"
    "v_pk_add_f32 v[104:105], v[104:105], v[68:69]\n"
    "v_pk_fma_f32 v[108:109], v[68:69], v[68:69], v[108:109]\n"
    "v_pk_add_f32 v[106:107], v[106:107], v[70:71]\n"
    "v_pk_fma_f32 v[110:111], v[70:71], v[70:71], v[110:111]\n"
    "v_lshlrev_b32 v72, 16, v24\n"
    "v_and_b32 v73, s76, v24\n"
    "v_lshlrev_b32 v74, 16, v25\n"
    "v_and_b32 v75, s76, v25\n"
    "v_lshlrev_b32 v76, 16, v26\n"
    "v_and_b32 v77, s76, v26\n"
    "v_lshlrev_b32 v78, 16, v27\n"
    "v_and_b32 v79, s76, v27\n"
    "v_pk_add_f32 v[104:105], v[104:105], v[72:73]\n"
    "v_pk_fma_f32 v[108:109], v[72:73], v[72:73], v[108:109]\n"
    "v_pk_add_f32 v[106:107], v[106:107], v[74:75]\n"
    "v_pk_fma_f32 v[110:111], v[74:75], v[74:75], v[110:111]\n"
    "v_pk_add_f32 v[104:105], v[104:105], v[76:77]\n"
    "v_pk_fma_f32 v[108:109], v[76:77], v[76:77], v[108:109]\n"
    "v_pk_add_f32 v[106:107], v[106:107], v[78:79]\n"
    "v_pk_fma_f32 v[110:111], v[78:79], v[78:79], v[110:111]\n"
    "v_lshlrev_b32 v80, 16, v28\n"
    "v_and_b32 v81, s76, v28\n"
    "v_lshlrev_b32 v82, 16, v29\n"
    "v_and_b32 v83, s76, v29\n"
    "v_lshlrev_b32 v84, 16, v30\n"
    "v_and_b32 v85, s76, v30\n"
    "v_lshlrev_b32 v86, 16, v31\n"
    "v_and_b32 v87, s76, v31\n"
    "v_pk_add_f32 v[104:105], v[104:105], v[80:81]\n"
    "v_pk_fma_f32 v[108:109], v[80:81], v[80:81], v[108:109]\n"
    "v_pk_add_f32 v[106:107], v[106:107], v[82:83]\n"
    "v_pk_fma_f32 v[110:111], v[82:83], v[82:83], v[110:111]\n"
    "v_pk_add_f32 v[104:105], v[104:105], v[84:85]\n"
    "v_pk_fma_f32 v[108:109], v[84:85], v[84:85], v[108:109]\n"
    "v_pk_add_f32 v[106:107], v[106:107], v[86:87]\n"
    "v_pk_fma_f32 v[110:111], v[86:87], v[86:87], v[110:111]\n"
    "v_lshlrev_b32 v88, 16, v32\n"
    "v_and_b32 v89, s76, v32\n"
    "v_lshlrev_b32 v90, 16, v33\n"
    "v_and_b32 v91, s76, v33\n"
    "v_lshlrev_b32 v92, 16, v34\n"
    "v_and_b32 v93, s76, v34\n"
    "v_lshlrev_b32 v94, 16, v35\n"
    "v_and_b32 v95, s76, v35\n"
    "v_pk_add_f32 v[104:105], v[104:105], v[88:89]\n"
    "v_pk_fma_f32 v[108:109], v[88:89], v[88:89], v[108:109]\n"
    "v_pk_add_f32 v[106:107], v[106:107], v[90:91]\n"
    "v_pk_fma_f32 v[110:111], v[90:91], v[90:91], v[110:111]\n"
    "v_pk_add_f32 v[104:105], v[104:105], v[92:93]\n"
    "v_pk_fma_f32 v[108:109], v[92:93], v[92:93], v[108:109]\n"
    "v_pk_add_f32 v[106:107], v[106:107], v[94:95]\n"
    "v_pk_fma_f32 v[110:111], v[94:95], v[94:95], v[110:111]\n"
    "v_lshlrev_b32 v96, 16, v36\n"
    "v_and_b32 v97, s76, v36\n"
    "v_lshlrev_b32 v98, 16, v37\n"
    "v_and_b32 v99, s76, v37\n"
    "v_lshlrev_b32 v100, 16, v38\n"
    "v_and_b32 v101, s76, v38\n"
    "v_lshlrev_b32 v102, 16, v39\n"
    "v_and_b32 v103, s76, v39\n"
    "v_pk_add_f32 v[104:105], v[104:105], v[96:97]\n"
    "v_pk_fma_f32 v[108:109], v[96:97], v[96:97], v[108:109]\n"
    "v_pk_add_f32 v[106:107], v[106:107], v[98:99]\n"
    "v_pk_fma_f32 v[110:111], v[98:99], v[98:99], v[110:111]\n"
    "v_pk_add_f32 v[104:105], v[104:105], v[100:101]\n"
    "v_pk_fma_f32 v[108:109], v[100:101], v[100:101], v[108:109]\n"
    "v_pk_add_f32 v[106:107], v[106:107], v[102:103]\n"
    "v_pk_fma_f32 v[110:111], v[102:103], v[102:103], v[110:111]\n"
    "v_pk_add_f32 v[104:105], v[104:105], v[106:107]\n"
    "v_pk_add_f32 v[108:109], v[108:109], v[110:111]\n"
    "v_add_f32 v104, v104, v105\n"
    "v_add_f32 v108, v108, v109\n"
    "s_nop 0\n"
    "v_add_f32_dpp v104, v104, v104 quad_perm:[1,0,3,2] row_mask:0xf bank_mask:0xf\n"
    "v_add_f32_dpp v108, v108, v108 quad_perm:[1,0,3,2] row_mask:0xf bank_mask:0xf\n"
    "s_nop 0\n"
    "v_add_f32_dpp v104, v104, v104 quad_perm:[2,3,0,1] row_mask:0xf bank_mask:0xf\n"
    "v_add_f32_dpp v108, v108, v108 quad_perm:[2,3,0,1] row_mask:0xf bank_mask:0xf\n"
    "s_nop 0\n"
    "v_add_f32_dpp v104, v104, v104 row_half_mirror row_mask:0xf bank_mask:0xf\n"
    "v_add_f32_dpp v108, v108, v108 row_half_mirror row_mask:0xf bank_mask:0xf\n"
    "v_mul_f32 v104, 0x3b000000, v104\n"
    "v_mul_f32 v108, 0x3b000000, v108\n"
    "v_fma_f32 v108, -v104, v104, v108\n"
    "v_max_f32 v108, 0, v108\n"
    "v_cvt_pk_bf16_f32 v104, v104, v108\n"
    "v_and_b32 v108, s76, v104\n"
    "v_lshlrev_b32 v104, 16, v104\n"
    "v_add_f32 v108, 0x3727c5ac, v108\n"
    "v_cvt_pk_bf16_f32 v108, v108, v108\n"
    "v_and_b32 v108, s76, v108\n"
    "v_sqrt_f32 v108, v108\n"
    "s_nop 0\n"
    "v_cvt_pk_bf16_f32 v108, v108, v108\n"
    "v_and_b32 v108, s76, v108\n"
    "v_rcp_f32 v108, v108\n"
    "s_nop 0\n"
    "v_mul_f32_e64 v104, -v104, v108\n"
    "v_pk_fma_f32 v[40:41], v[40:41], v[108:109], v[104:105] op_sel_hi:[1,0,0]\n"
    "v_pk_fma_f32 v[42:43], v[42:43], v[108:109], v[104:105] op_sel_hi:[1,0,0]\n"
    "v_pk_fma_f32 v[44:45], v[44:45], v[108:109], v[104:105] op_sel_hi:[1,0,0]\n"
    "v_pk_fma_f32 v[46:47], v[46:47], v[108:109], v[104:105] op_sel_hi:[1,0,0]\n"
    "v_cvt_pk_bf16_f32 v8, v40, v41\n"
    "v_cvt_pk_bf16_f32 v9, v42, v43\n"
    "v_cvt_pk_bf16_f32 v10, v44, v45\n"
    "v_cvt_pk_bf16_f32 v11, v46, v47\n"
    "ds_write_b128 v6, v[8:11] offset:0\n"
    "v_pk_fma_f32 v[48:49], v[48:49], v[108:109], v[104:105] op_sel_hi:[1,0,0]\n"
    "v_pk_fma_f32 v[50:51], v[50:51], v[108:109], v[104:105] op_sel_hi:[1,0,0]\n"
    "v_pk_fma_f32 v[52:53], v[52:53], v[108:109], v[104:105] op_sel_hi:[1,0,0]\n"
    "v_pk_fma_f32 v[54:55], v[54:55], v[108:109], v[104:105] op_sel_hi:[1,0,0]\n"
    "v_cvt_pk_bf16_f32 v12, v48, v49\n"
    "v_cvt_pk_bf16_f32 v13, v50, v51\n"
    "v_cvt_pk_bf16_f32 v14, v52, v53\n"
    "v_cvt_pk_bf16_f32 v15, v54, v55\n"
    "ds_write_b128 v6, v[12:15] offset:8192\n"
    "v_pk_fma_f32 v[56:57], v[56:57], v[108:109], v[104:105] op_sel_hi:[1,0,0]\n"
    "v_pk_fma_f32 v[58:59], v[58:59], v[108:109], v[104:105] op_sel_hi:[1,0,0]\n"
    "v_pk_fma_f32 v[60:61], v[60:61], v[108:109], v[104:105] op_sel_hi:[1,0,0]\n"
    "v_pk_fma_f32 v[62:63], v[62:63], v[108:109], v[104:105] op_sel_hi:[1,0,0]\n"
    "v_cvt_pk_bf16_f32 v16, v56, v57\n"
    "v_cvt_pk_bf16_f32 v17, v58, v59\n"
    "v_cvt_pk_bf16_f32 v18, v60, v61\n"
    "v_cvt_pk_bf16_f32 v19, v62, v63\n"
    "ds_write_b128 v6, v[16:19] offset:16384\n"
    "v_pk_fma_f32 v[64:65], v[64:65], v[108:109], v[104:105] op_sel_hi:[1,0,0]\n"
    "v_pk_fma_f32 v[66:67], v[66:67], v[108:109], v[104:105] op_sel_hi:[1,0,0]\n"
    "v_pk_fma_f32 v[68:69], v[68:69], v[108:109], v[104:105] op_sel_hi:[1,0,0]\n"
    "v_pk_fma_f32 v[70:71], v[70:71], v[108:109], v[104:105] op_sel_hi:[1,0,0]\n"
    "v_cvt_pk_bf16_f32 v20, v64, v65\n"
    "v_cvt_pk_bf16_f32 v21, v66, v67\n"
    "v_cvt_pk_bf16_f32 v22, v68, v69\n"
    "v_cvt_pk_bf16_f32 v23, v70, v71\n"
    "ds_write_b128 v6, v[20:23] offset:24576\n"
    "v_pk_fma_f32 v[72:73], v[72:73], v[108:109], v[104:105] op_sel_hi:[1,0,0]\n"
    "v_pk_fma_f32 v[74:75], v[74:75], v[108:109], v[104:105] op_sel_hi:[1,0,0]\n"
    "v_pk_fma_f32 v[76:77], v[76:77], v[108:109], v[104:105] op_sel_hi:[1,0,0]\n"
    "v_pk_fma_f32 v[78:79], v[78:79], v[108:109], v[104:105] op_sel_hi:[1,0,0]\n"
    "v_cvt_pk_bf16_f32 v24, v72, v73\n"
    "v_cvt_pk_bf16_f32 v25, v74, v75\n"
    "v_cvt_pk_bf16_f32 v26, v76, v77\n"
    "v_cvt_pk_bf16_f32 v27, v78, v79\n"
    "ds_write_b128 v6, v[24:27] offset:32768\n"
    "v_pk_fma_f32 v[80:81], v[80:81], v[108:109], v[104:105] op_sel_hi:[1,0,0]\n"
    "v_pk_fma_f32 v[82:83], v[82:83], v[108:109], v[104:105] op_sel_hi:[1,0,0]\n"
    "v_pk_fma_f32 v[84:85], v[84:85], v[108:109], v[104:105] op_sel_hi:[1,0,0]\n"
    "v_pk_fma_f32 v[86:87], v[86:87], v[108:109], v[104:105] op_sel_hi:[1,0,0]\n"
    "v_cvt_pk_bf16_f32 v28, v80, v81\n"
    "v_cvt_pk_bf16_f32 v29, v82, v83\n"
    "v_cvt_pk_bf16_f32 v30, v84, v85\n"
    "v_cvt_pk_bf16_f32 v31, v86, v87\n"
    "ds_write_b128 v6, v[28:31] offset:40960\n"
    "v_pk_fma_f32 v[88:89], v[88:89], v[108:109], v[104:105] op_sel_hi:[1,0,0]\n"
    "v_pk_fma_f32 v[90:91], v[90:91], v[108:109], v[104:105] op_sel_hi:[1,0,0]\n"
    "v_pk_fma_f32 v[92:93], v[92:93], v[108:109], v[104:105] op_sel_hi:[1,0,0]\n"
    "v_pk_fma_f32 v[94:95], v[94:95], v[108:109], v[104:105] op_sel_hi:[1,0,0]\n"
    "v_cvt_pk_bf16_f32 v32, v88, v89\n"
    "v_cvt_pk_bf16_f32 v33, v90, v91\n"
    "v_cvt_pk_bf16_f32 v34, v92, v93\n"
    "v_cvt_pk_bf16_f32 v35, v94, v95\n"
    "ds_write_b128 v6, v[32:35] offset:49152\n"
    "v_pk_fma_f32 v[96:97], v[96:97], v[108:109], v[104:105] op_sel_hi:[1,0,0]\n"
    "v_pk_fma_f32 v[98:99], v[98:99], v[108:109], v[104:105] op_sel_hi:[1,0,0]\n"
    "v_pk_fma_f32 v[100:101], v[100:101], v[108:109], v[104:105] op_sel_hi:[1,0,0]\n"
    "v_pk_fma_f32 v[102:103], v[102:103], v[108:109], v[104:105] op_sel_hi:[1,0,0]\n"
    "v_cvt_pk_bf16_f32 v36, v96, v97\n"
    "v_cvt_pk_bf16_f32 v37, v98, v99\n"
    "v_cvt_pk_bf16_f32 v38, v100, v101\n"
    "v_cvt_pk_bf16_f32 v39, v102, v103\n"
    "ds_write_b128 v6, v[36:39] offset:57344\n"
    "s_waitcnt vmcnt(0)\n"
    "s_waitcnt lgkmcnt(0)\n"
    "s_barrier\n"
    "s_setprio 2\n"
    "s_add_u32 m0, s81, 32768\n"
    "s_nop 0\n"
    "buffer_load_dwordx4 v224, s[36:39], s80 offen offset:0 lds\n"
    "buffer_load_dwordx4 v225, s[36:39], s80 offen offset:1024 lds\n"
    "buffer_load_dwordx4 v226, s[36:39], s80 offen offset:2048 lds\n"
    "buffer_load_dwordx4 v227, s[36:39], s80 offen offset:3072 lds\n"
    "s_add_u32 s80, s80, 114688\n"
    "s_mov_b32 s96, 131072\n"
    ".Lsp_23_k:\n"
    "ds_read_b32 v60, v243 offset:16\n"
    "s_waitcnt lgkmcnt(0)\n"
    "v_readfirstlane_b32 s94, v60\n"
    "s_cmp_eq_u32 s94, 0x1010101\n"
    "s_cbranch_scc1 .Lspd_24_k\n"
    "s_sleep 1\n"
    "s_sub_u32 s96, s96, 1\n"
    "s_cbranch_scc0 .Lsp_23_k\n"
    ".Lspd_24_k:\n"
    "s_add_u32 m0, s81, 0\n"
    "s_nop 0\n"
    "buffer_load_dwordx4 v224, s[36:39], s80 offen offset:0 lds\n"
    "buffer_load_dwordx4 v225, s[36:39], s80 offen offset:1024 lds\n"
    "buffer_load_dwordx4 v226, s[36:39], s80 offen offset:2048 lds\n"
    "buffer_load_dwordx4 v227, s[36:39], s80 offen offset:3072 lds\n"
    "s_add_u32 s80, s80, 114688\n"
    "s_add_u32 m0, s81, 16384\n"
    "s_nop 0\n"
    "buffer_load_dwordx4 v224, s[36:39], s80 offen offset:0 lds\n"
    "buffer_load_dwordx4 v225, s[36:39], s80 offen offset:1024 lds\n"
    "buffer_load_dwordx4 v226, s[36:39], s80 offen offset:2048 lds\n"
    "buffer_load_dwordx4 v227, s[36:39], s80 offen offset:3072 lds\n"
    "s_add_u32 s80, s80, 114688\n"
    "s_setprio 0\n"
    "s_cmp_ge_u32 s82, 28\n"
    "s_cbranch_scc1 .L_nof1_k\n"
    "s_waitcnt vmcnt(8)\n"
    "ds_read_b128 v[176:179], v236 offset:0\n"
    "ds_read_b128 v[180:183], v236 offset:256\n"
    "ds_read_b128 v[184:187], v236 offset:512\n"
    "ds_read_b128 v[188:191], v236 offset:768\n"
    "ds_read_b64_tr_b16 v[128:129], v232 offset:32768\n"
    "ds_read_b64_tr_b16 v[130:131], v232 offset:34816\n"
    "ds_read_b64_tr_b16 v[132:133], v233 offset:32768\n"
    "ds_read_b64_tr_b16 v[134:135], v233 offset:34816\n"
    "ds_read_b64_tr_b16 v[136:137], v234 offset:32768\n"
    "ds_read_b64_tr_b16 v[138:139], v234 offset:34816\n"
    "ds_read_b64_tr_b16 v[140:141], v235 offset:32768\n"
    "ds_read_b64_tr_b16 v[142:143], v235 offset:34816\n"
    "s_waitcnt lgkmcnt(0)\n"
    "s_waitcnt vmcnt(4)\n"
    "v_mfma_f32_16x16x32_bf16 v[64:67], v[128:131], v[176:179], 0\n"
    "ds_read_b128 v[192:195], v237 offset:4096\n"
    "s_add_u32 m0, s81, 32768\n"
    "s_nop 0\n"
    "buffer_load_dwordx4 v224, s[36:39], s80 offen offset:0 lds\n"
    "v_mfma_f32_16x16x32_bf16 v[68:71], v[128:131], v[180:183], 0\n"
    "ds_read_b128 v[196:199], v237 offset:4352\n"
    "buffer_load_dwordx4 v225, s[36:39], s80 offen offset:1024 lds\n"
    "v_mfma_f32_16x16x32_bf16 v[72:75], v[128:131], v[184:187], 0\n"
    "ds_read_b128 v[200:203], v237 offset:4608\n"
    "buffer_load_dwordx4 v226, s[36:39], s80 offen offset:2048 lds\n"
    "v_mfma_f32_16x16x32_bf16 v[76:79], v[128:131], v[188:191], 0\n"
    "ds_read_b128 v[204:207], v237 offset:4864\n"
    "buffer_load_dwordx4 v227, s[36:39], s80 offen offset:3072 lds\n"
    "s_add_u32 s80, s80, 114688\n"
    "v_mfma_f32_16x16x32_bf16 v[80:83], v[132:135], v[176:179], 0\n"
    "ds_read_b64_tr_b16 v[144:145], v232 offset:0\n"
    "v_mfma_f32_16x16x32_bf16 v[84:87], v[132:135], v[180:183], 0\n"
    "ds_read_b64_tr_b16 v[146:147], v232 offset:2048\n"
    "v_mfma_f32_16x16x32_bf16 v[88:91], v[132:135], v[184:187], 0\n"
    "ds_read_b64_tr_b16 v[148:149], v233 offset:0\n"
    "v_mfma_f32_16x16x32_bf16 v[92:95], v[132:135], v[188:191], 0\n"
    "ds_read_b64_tr_b16 v[150:151], v233 offset:2048\n"
    "v_mfma_f32_16x16x32_bf16 v[96:99], v[136:139], v[176:179], 0\n"
    "ds_read_b64_tr_b16 v[152:153], v234 offset:0\n"
    "v_mfma_f32_16x16x32_bf16 v[100:103], v[136:139], v[180:183], 0\n"
    "ds_read_b64_tr_b16 v[154:155], v234 offset:2048\n"
    "v_mfma_f32_16x16x32_bf16 v[104:107], v[136:139], v[184:187], 0\n"
    "ds_read_b64_tr_b16 v[156:157], v235 offset:0\n"
    "v_mfma_f32_16x16x32_bf16 v[108:111], v[136:139], v[188:191], 0\n"
    "ds_read_b64_tr_b16 v[158:159], v235 offset:2048\n"
    "v_mfma_f32_16x16x32_bf16 v[112:115], v[140:143], v[176:179], 0\n"
    "v_mfma_f32_16x16x32_bf16 v[116:119], v[140:143], v[180:183], 0\n"
    "v_mfma_f32_16x16x32_bf16 v[120:123], v[140:143], v[184:187], 0\n"
    "v_mfma_f32_16x16x32_bf16 v[124:127], v[140:143], v[188:191], 0\n"
    "s_waitcnt lgkmcnt(0)\n"
    "s_waitcnt vmcnt(4)\n"
    "v_mfma_f32_16x16x32_bf16 v[64:67], v[144:147], v[192:195], v[64:67]\n"
    "ds_read_b128 v[176:179], v236 offset:8192\n"
    "s_add_u32 m0, s81, 0\n"
    "s_nop 0\n"
    "buffer_load_dwordx4 v224, s[36:39], s80 offen offset:0 lds\n"
    "v_mfma_f32_16x16x32_bf16 v[68:71], v[144:147], v[196:199], v[68:71]\n"
    "ds_read_b128 v[180:183], v236 offset:8448\n"
    "buffer_load_dwordx4 v225, s[36:39], s80 offen offset:1024 lds\n"
    "v_mfma_f32_16x16x32_bf16 v[72:75], v[144:147], v[200:203], v[72:75]\n"
    "ds_read_b128 v[184:187], v236 offset:8704\n"
    "buffer_load_dwordx4 v226, s[36:39], s80 offen offset:2048 lds\n"
    "v_mfma_f32_16x16x32_bf16 v[76:79], v[144:147], v[204:207], v[76:79]\n"
    "ds_read_b128 v[188:191], v236 offset:8960\n"
    "buffer_load_dwordx4 v227, s[36:39], s80 offen offset:3072 lds\n"
    "s_add_u32 s80, s80, 114688\n"
    "v_mfma_f32_16x16x32_bf16 v[80:83], v[148:151], v[192:195], v[80:83]\n"
    "ds_read_b64_tr_b16 v[128:129], v232 offset:16384\n"
    "v_mfma_f32_16x16x32_bf16 v[84:87], v[148:151], v[196:199], v[84:87]\n"
    "ds_read_b64_tr_b16 v[130:131], v232 offset:18432\n"
    "v_mfma_f32_16x16x32_bf16 v[88:91], v[148:151], v[200:203], v[88:91]\n"
    "ds_read_b64_tr_b16 v[132:133], v233 offset:16384\n"
    "v_mfma_f32_16x16x32_bf16 v[92:95], v[148:151], v[204:207], v[92:95]\n"
    "ds_read_b64_tr_b16 v[134:135], v233 offset:18432\n"
    "v_mfma_f32_16x16x32_bf16 v[96:99], v[152:155], v[192:195], v[96:99]\n"
    "ds_read_b64_tr_b16 v[136:137], v234 offset:16384\n"
    "v_mfma_f32_16x16x32_bf16 v[100:103], v[152:155], v[196:199], v[100:103]\n"
    "ds_read_b64_tr_b16 v[138:139], v234 offset:18432\n"
    "v_mfma_f32_16x16x32_bf16 v[104:107], v[152:155], v[200:203], v[104:107]\n"
    "ds_read_b64_tr_b16 v[140:141], v235 offset:16384\n"
    "v_mfma_f32_16x16x32_bf16 v[108:111], v[152:155], v[204:207], v[108:111]\n"
    "ds_read_b64_tr_b16 v[142:143], v235 offset:18432\n"
    "v_mfma_f32_16x16x32_bf16 v[112:115], v[156:159], v[192:195], v[112:115]\n"
    "v_mfma_f32_16x16x32_bf16 v[116:119], v[156:159], v[196:199], v[116:119]\n"
    "v_mfma_f32_16x16x32_bf16 v[120:123], v[156:159], v[200:203], v[120:123]\n"
    "v_mfma_f32_16x16x32_bf16 v[124:127], v[156:159], v[204:207], v[124:127]\n"
    "s_waitcnt lgkmcnt(0)\n"
    "s_waitcnt vmcnt(4)\n"
    "v_mfma_f32_16x16x32_bf16 v[64:67], v[128:131], v[176:179], v[64:67]\n"
    "ds_read_b128 v[192:195], v237 offset:12288\n"
    "s_add_u32 m0, s81, 16384\n"
    "s_nop 0\n"
    "buffer_load_dwordx4 v224, s[36:39], s80 offen offset:0 lds\n"
    "v_mfma_f32_16x16x32_bf16 v[68:71], v[128:131], v[180:183], v[68:71]\n"
    "ds_read_b128 v[196:199], v237 offset:12544\n"
    "buffer_load_dwordx4 v225, s[36:39], s80 offen offset:1024 lds\n"
    "v_mfma_f32_16x16x32_bf16 v[72:75], v[128:131], v[184:187], v[72:75]\n"
    "ds_read_b128 v[200:203], v237 offset:12800\n"
    "buffer_load_dwordx4 v226, s[36:39], s80 offen offset:2048 lds\n"
    "v_mfma_f32_16x16x32_bf16 v[76:79], v[128:131], v[188:191], v[76:79]\n"
    "ds_read_b128 v[204:207], v237 offset:13056\n"
    "buffer_load_dwordx4 v227, s[36:39], s80 offen offset:3072 lds\n"
    "s_add_u32 s80, s80, 114688\n"
    "v_mfma_f32_16x16x32_bf16 v[80:83], v[132:135], v[176:179], v[80:83]\n"
    "ds_read_b64_tr_b16 v[144:145], v232 offset:32768\n"
    "v_mfma_f32_16x16x32_bf16 v[84:87], v[132:135], v[180:183], v[84:87]\n"
    "ds_read_b64_tr_b16 v[146:147], v232 offset:34816\n"
    "v_mfma_f32_16x16x32_bf16 v[88:91], v[132:135], v[184:187], v[88:91]\n"
    "ds_read_b64_tr_b16 v[148:149], v233 offset:32768\n"
    "v_mfma_f32_16x16x32_bf16 v[92:95], v[132:135], v[188:191], v[92:95]\n"
    "ds_read_b64_tr_b16 v[150:151], v233 offset:34816\n"
    "v_mfma_f32_16x16x32_bf16 v[96:99], v[136:139], v[176:179], v[96:99]\n"
    "ds_read_b64_tr_b16 v[152:153], v234 offset:32768\n"
    "v_mfma_f32_16x16x32_bf16 v[100:103], v[136:139], v[180:183], v[100:103]\n"
    "ds_read_b64_tr_b16 v[154:155], v234 offset:34816\n"
    "v_mfma_f32_16x16x32_bf16 v[104:107], v[136:139], v[184:187], v[104:107]\n"
    "ds_read_b64_tr_b16 v[156:157], v235 offset:32768\n"
    "v_mfma_f32_16x16x32_bf16 v[108:111], v[136:139], v[188:191], v[108:111]\n"
    "ds_read_b64_tr_b16 v[158:159], v235 offset:34816\n"
    "v_mfma_f32_16x16x32_bf16 v[112:115], v[140:143], v[176:179], v[112:115]\n"
    "v_mfma_f32_16x16x32_bf16 v[116:119], v[140:143], v[180:183], v[116:119]\n"
    "v_mfma_f32_16x16x32_bf16 v[120:123], v[140:143], v[184:187], v[120:123]\n"
    "v_mfma_f32_16x16x32_bf16 v[124:127], v[140:143], v[188:191], v[124:127]\n"
    "s_waitcnt lgkmcnt(0)\n"
    "s_waitcnt vmcnt(4)\n"
    "v_mfma_f32_16x16x32_bf16 v[64:67], v[144:147], v[192:195], v[64:67]\n"
    "ds_read_b128 v[176:179], v236 offset:16384\n"
    "s_add_u32 m0, s81, 32768\n"
    "s_nop 0\n"
    "buffer_load_dwordx4 v224, s[36:39], s80 offen offset:0 lds\n"
    "v_mfma_f32_16x16x32_bf16 v[68:71], v[144:147], v[196:199], v[68:71]\n"
    "ds_read_b128 v[180:183], v236 offset:16640\n"
    "buffer_load_dwordx4 v225, s[36:39], s80 offen offset:1024 lds\n"
    "v_mfma_f32_16x16x32_bf16 v[72:75], v[144:147], v[200:203], v[72:75]\n"
    "ds_read_b128 v[184:187], v236 offset:16896\n"
    "buffer_load_dwordx4 v226, s[36:39], s80 offen offset:2048 lds\n"
    "v_mfma_f32_16x16x32_bf16 v[76:79], v[144:147], v[204:207], v[76:79]\n"
    "ds_read_b128 v[188:191], v236 offset:17152\n"
    "buffer_load_dwordx4 v227, s[36:39], s80 offen offset:3072 lds\n"
    "s_add_u32 s80, s80, 114688\n"
    "v_mfma_f32_16x16x32_bf16 v[80:83], v[148:151], v[192:195], v[80:83]\n"
    "ds_read_b64_tr_b16 v[128:129], v232 offset:0\n"
    "v_mfma_f32_16x16x32_bf16 v[84:87], v[148:151], v[196:199], v[84:87]\n"
    "ds_read_b64_tr_b16 v[130:131], v232 offset:2048\n"
    "v_mfma_f32_16x16x32_bf16 v[88:91], v[148:151], v[200:203], v[88:91]\n"
    "ds_read_b64_tr_b16 v[132:133], v233 offset:0\n"
    "v_mfma_f32_16x16x32_bf16 v[92:95], v[148:151], v[204:207], v[92:95]\n"
    "ds_read_b64_tr_b16 v[134:135], v233 offset:2048\n"
    "v_mfma_f32_16x16x32_bf16 v[96:99], v[152:155], v[192:195], v[96:99]\n"
    "ds_read_b64_tr_b16 v[136:137], v234 offset:0\n"
    "v_mfma_f32_16x16x32_bf16 v[100:103], v[152:155], v[196:199], v[100:103]\n"
    "ds_read_b64_tr_b16 v[138:139], v234 offset:2048\n"
    "v_mfma_f32_16x16x32_bf16 v[104:107], v[152:155], v[200:203], v[104:107]\n"
    "ds_read_b64_tr_b16 v[140:141], v235 offset:0\n"
    "v_mfma_f32_16x16x32_bf16 v[108:111], v[152:155], v[204:207], v[108:111]\n"
    "ds_read_b64_tr_b16 v[142:143], v235 offset:2048\n"
    "v_mfma_f32_16x16x32_bf16 v[112:115], v[156:159], v[192:195], v[112:115]\n"
    "v_mfma_f32_16x16x32_bf16 v[116:119], v[156:159], v[196:199], v[116:119]\n"
    "v_mfma_f32_16x16x32_bf16 v[120:123], v[156:159], v[200:203], v[120:123]\n"
    "v_mfma_f32_16x16x32_bf16 v[124:127], v[156:159], v[204:207], v[124:127]\n"
    "s_waitcnt lgkmcnt(0)\n"
    "s_waitcnt vmcnt(4)\n"
    "v_mfma_f32_16x16x32_bf16 v[64:67], v[128:131], v[176:179], v[64:67]\n"
    "ds_read_b128 v[192:195], v237 offset:20480\n"
    "s_add_u32 m0, s81, 0\n"
    "s_nop 0\n"
    "buffer_load_dwordx4 v224, s[36:39], s80 offen offset:0 lds\n"
    "v_mfma_f32_16x16x32_bf16 v[68:71], v[128:131], v[180:183], v[68:71]\n"
    "ds_read_b128 v[196:199], v237 offset:20736\n"
    "buffer_load_dwordx4 v225, s[36:39], s80 offen offset:1024 lds\n"
    "v_mfma_f32_16x16x32_bf16 v[72:75], v[128:131], v[184:187], v[72:75]\n"
    "ds_read_b128 v[200:203], v237 offset:20992\n"
    "buffer_load_dwordx4 v226, s[36:39], s80 offen offset:2048 lds\n"
    "v_mfma_f32_16x16x32_bf16 v[76:79], v[128:131], v[188:191], v[76:79]\n"
    "ds_read_b128 v[204:207], v237 offset:21248\n"
    "buffer_load_dwordx4 v227, s[36:39], s80 offen offset:3072 lds\n"
    "s_add_u32 s80, s80, 114688\n"
    "v_mfma_f32_16x16x32_bf16 v[80:83], v[132:135], v[176:179], v[80:83]\n"
    "ds_read_b64_tr_b16 v[144:145], v232 offset:16384\n"
    "v_mfma_f32_16x16x32_bf16 v[84:87], v[132:135], v[180:183], v[84:87]\n"
    "ds_read_b64_tr_b16 v[146:147], v232 offset:18432\n"
    "v_mfma_f32_16x16x32_bf16 v[88:91], v[132:135], v[184:187], v[88:91]\n"
    "ds_read_b64_tr_b16 v[148:149], v233 offset:16384\n"
    "v_mfma_f32_16x16x32_bf16 v[92:95], v[132:135], v[188:191], v[92:95]\n"
    "ds_read_b64_tr_b16 v[150:151], v233 offset:18432\n"
    "v_mfma_f32_16x16x32_bf16 v[96:99], v[136:139], v[176:179], v[96:99]\n"
    "ds_read_b64_tr_b16 v[152:153], v234 offset:16384\n"
    "v_mfma_f32_16x16x32_bf16 v[100:103], v[136:139], v[180:183], v[100:103]\n"
    "ds_read_b64_tr_b16 v[154:155], v234 offset:18432\n"
    "v_mfma_f32_16x16x32_bf16 v[104:107], v[136:139], v[184:187], v[104:107]\n"
    "ds_read_b64_tr_b16 v[156:157], v235 offset:16384\n"
    "v_mfma_f32_16x16x32_bf16 v[108:111], v[136:139], v[188:191], v[108:111]\n"
    "ds_read_b64_tr_b16 v[158:159], v235 offset:18432\n"
    "v_mfma_f32_16x16x32_bf16 v[112:115], v[140:143], v[176:179], v[112:115]\n"
    "v_mfma_f32_16x16x32_bf16 v[116:119], v[140:143], v[180:183], v[116:119]\n"
    "v_mfma_f32_16x16x32_bf16 v[120:123], v[140:143], v[184:187], v[120:123]\n"
    "v_mfma_f32_16x16x32_bf16 v[124:127], v[140:143], v[188:191], v[124:127]\n"
    "s_waitcnt lgkmcnt(0)\n"
    "s_waitcnt vmcnt(4)\n"
    "v_mfma_f32_16x16x32_bf16 v[64:67], v[144:147], v[192:195], v[64:67]\n"
    "ds_read_b128 v[176:179], v236 offset:24576\n"
    "s_add_u32 m0, s81, 16384\n"
    "s_nop 0\n"
    "buffer_load_dwordx4 v224, s[36:39], s80 offen offset:0 lds\n"
    "v_mfma_f32_16x16x32_bf16 v[68:71], v[144:147], v[196:199], v[68:71]\n"
    "ds_read_b128 v[180:183], v236 offset:24832\n"
    "buffer_load_dwordx4 v225, s[36:39], s80 offen offset:1024 lds\n"
    "v_mfma_f32_16x16x32_bf16 v[72:75], v[144:147], v[200:203], v[72:75]\n"
    "ds_read_b128 v[184:187], v236 offset:25088\n"
    "buffer_load_dwordx4 v226, s[36:39], s80 offen offset:2048 lds\n"
    "v_mfma_f32_16x16x32_bf16 v[76:79], v[144:147], v[204:207], v[76:79]\n"
    "ds_read_b128 v[188:191], v236 offset:25344\n"
    "buffer_load_dwordx4 v227, s[36:39], s80 offen offset:3072 lds\n"
    "s_add_u32 s80, s80, 114688\n"
    "v_mfma_f32_16x16x32_bf16 v[80:83], v[148:151], v[192:195], v[80:83]\n"
    "ds_read_b64_tr_b16 v[128:129], v232 offset:32768\n"
    "v_mfma_f32_16x16x32_bf16 v[84:87], v[148:151], v[196:199], v[84:87]\n"
    "ds_read_b64_tr_b16 v[130:131], v232 offset:34816\n"
    "v_mfma_f32_16x16x32_bf16 v[88:91], v[148:151], v[200:203], v[88:91]\n"
    "ds_read_b64_tr_b16 v[132:133], v233 offset:32768\n"
    "v_mfma_f32_16x16x32_bf16 v[92:95], v[148:151], v[204:207], v[92:95]\n"
    "ds_read_b64_tr_b16 v[134:135], v233 offset:34816\n"
    "v_mfma_f32_16x16x32_bf16 v[96:99], v[152:155], v[192:195], v[96:99]\n"
    "ds_read_b64_tr_b16 v[136:137], v234 offset:32768\n"
    "v_mfma_f32_16x16x32_bf16 v[100:103], v[152:155], v[196:199], v[100:103]\n"
    "ds_read_b64_tr_b16 v[138:139], v234 offset:34816\n"
    "v_mfma_f32_16x16x32_bf16 v[104:107], v[152:155], v[200:203], v[104:107]\n"
    "ds_read_b64_tr_b16 v[140:141], v235 offset:32768\n"
    "v_mfma_f32_16x16x32_bf16 v[108:111], v[152:155], v[204:207], v[108:111]\n"
    "ds_read_b64_tr_b16 v[142:143], v235 offset:34816\n"
    "v_mfma_f32_16x16x32_bf16 v[112:115], v[156:159], v[192:195], v[112:115]\n"
    "v_mfma_f32_16x16x32_bf16 v[116:119], v[156:159], v[196:199], v[116:119]\n"
    "v_mfma_f32_16x16x32_bf16 v[120:123], v[156:159], v[200:203], v[120:123]\n"
    "v_mfma_f32_16x16x32_bf16 v[124:127], v[156:159], v[204:207], v[124:127]\n"
    "s_waitcnt lgkmcnt(0)\n"
    "s_waitcnt vmcnt(4)\n"
    "v_mfma_f32_16x16x32_bf16 v[64:67], v[128:131], v[176:179], v[64:67]\n"
    "ds_read_b128 v[192:195], v237 offset:28672\n"
    "s_add_u32 m0, s81, 32768\n"
    "s_nop 0\n"
    "buffer_load_dwordx4 v224, s[36:39], s80 offen offset:0 lds\n"
    "v_mfma_f32_16x16x32_bf16 v[68:71], v[128:131], v[180:183], v[68:71]\n"
    "ds_read_b128 v[196:199], v237 offset:28928\n"
    "buffer_load_dwordx4 v225, s[36:39], s80 offen offset:1024 lds\n"
    "v_mfma_f32_16x16x32_bf16 v[72:75], v[128:131], v[184:187], v[72:75]\n"
    "ds_read_b128 v[200:203], v237 offset:29184\n"
    "buffer_load_dwordx4 v226, s[36:39], s80 offen offset:2048 lds\n"
    "v_mfma_f32_16x16x32_bf16 v[76:79], v[128:131], v[188:191], v[76:79]\n"
    "ds_read_b128 v[204:207], v237 offset:29440\n"
    "buffer_load_dwordx4 v227, s[36:39], s80 offen offset:3072 lds\n"
    "s_add_u32 s80, s80, 114688\n"
    "v_mfma_f32_16x16x32_bf16 v[80:83], v[132:135], v[176:179], v[80:83]\n"
    "ds_read_b64_tr_b16 v[144:145], v232 offset:0\n"
    "v_mfma_f32_16x16x32_bf16 v[84:87], v[132:135], v[180:183], v[84:87]\n"
    "ds_read_b64_tr_b16 v[146:147], v232 offset:2048\n"
    "v_mfma_f32_16x16x32_bf16 v[88:91], v[132:135], v[184:187], v[88:91]\n"
    "ds_read_b64_tr_b16 v[148:149], v233 offset:0\n"
    "v_mfma_f32_16x16x32_bf16 v[92:95], v[132:135], v[188:191], v[92:95]\n"
    "ds_read_b64_tr_b16 v[150:151], v233 offset:2048\n"
    "v_mfma_f32_16x16x32_bf16 v[96:99], v[136:139], v[176:179], v[96:99]\n"
    "ds_read_b64_tr_b16 v[152:153], v234 offset:0\n"
    "v_mfma_f32_16x16x32_bf16 v[100:103], v[136:139], v[180:183], v[100:103]\n"
    "ds_read_b64_tr_b16 v[154:155], v234 offset:2048\n"
    "v_mfma_f32_16x16x32_bf16 v[104:107], v[136:139], v[184:187], v[104:107]\n"
    "ds_read_b64_tr_b16 v[156:157], v235 offset:0\n"
    "v_mfma_f32_16x16x32_bf16 v[108:111], v[136:139], v[188:191], v[108:111]\n"
    "ds_read_b64_tr_b16 v[158:159], v235 offset:2048\n"
    "v_mfma_f32_16x16x32_bf16 v[112:115], v[140:143], v[176:179], v[112:115]\n"
    "v_mfma_f32_16x16x32_bf16 v[116:119], v[140:143], v[180:183], v[116:119]\n"
    "v_mfma_f32_16x16x32_bf16 v[120:123], v[140:143], v[184:187], v[120:123]\n"
    "v_mfma_f32_16x16x32_bf16 v[124:127], v[140:143], v[188:191], v[124:127]\n"
    "s_waitcnt lgkmcnt(0)\n"
    "s_waitcnt vmcnt(4)\n"
    "v_mfma_f32_16x16x32_bf16 v[64:67], v[144:147], v[192:195], v[64:67]\n"
    "ds_read_b128 v[176:179], v236 offset:32768\n"
    "s_add_u32 m0, s81, 0\n"
    "s_nop 0\n"
    "buffer_load_dwordx4 v224, s[36:39], s80 offen offset:0 lds\n"
    "v_mfma_f32_16x16x32_bf16 v[68:71], v[144:147], v[196:199], v[68:71]\n"
    "ds_read_b128 v[180:183], v236 offset:33024\n"
    "buffer_load_dwordx4 v225, s[36:39], s80 offen offset:1024 lds\n"
    "v_mfma_f32_16x16x32_bf16 v[72:75], v[144:147], v[200:203], v[72:75]\n"
    "ds_read_b128 v[184:187], v236 offset:33280\n"
    "buffer_load_dwordx4 v226, s[36:39], s80 offen offset:2048 lds\n"
    "v_mfma_f32_16x16x32_bf16 v[76:79], v[144:147], v[204:207], v[76:79]\n"
    "ds_read_b128 v[188:191], v236 offset:33536\n"
    "buffer_load_dwordx4 v227, s[36:39], s80 offen offset:3072 lds\n"
    "s_add_u32 s80, s80, 114688\n"
    "v_mfma_f32_16x16x32_bf16 v[80:83], v[148:151], v[192:195], v[80:83]\n"
    "ds_read_b64_tr_b16 v[128:129], v232 offset:16384\n"
    "v_mfma_f32_16x16x32_bf16 v[84:87], v[148:151], v[196:199], v[84:87]\n"
    "ds_read_b64_tr_b16 v[130:131], v232 offset:18432\n"
    "v_mfma_f32_16x16x32_bf16 v[88:91], v[148:151], v[200:203], v[88:91]\n"
    "ds_read_b64_tr_b16 v[132:133], v233 offset:16384\n"
    "v_mfma_f32_16x16x32_bf16 v[92:95], v[148:151], v[204:207], v[92:95]\n"
    "ds_read_b64_tr_b16 v[134:135], v233 offset:18432\n"
    "v_mfma_f32_16x16x32_bf16 v[96:99], v[152:155], v[192:195], v[96:99]\n"
    "ds_read_b64_tr_b16 v[136:137], v234 offset:16384\n"
    "v_mfma_f32_16x16x32_bf16 v[100:103], v[152:155], v[196:199], v[100:103]\n"
    "ds_read_b64_tr_b16 v[138:139], v234 offset:18432\n"
    "v_mfma_f32_16x16x32_bf16 v[104:107], v[152:155], v[200:203], v[104:107]\n"
    "ds_read_b64_tr_b16 v[140:141], v235 offset:16384\n"
    "v_mfma_f32_16x16x32_bf16 v[108:111], v[152:155], v[204:207], v[108:111]\n"
    "ds_read_b64_tr_b16 v[142:143], v235 offset:18432\n"
    "v_mfma_f32_16x16x32_bf16 v[112:115], v[156:159], v[192:195], v[112:115]\n"
    "v_mfma_f32_16x16x32_bf16 v[116:119], v[156:159], v[196:199], v[116:119]\n"
    "v_mfma_f32_16x16x32_bf16 v[120:123], v[156:159], v[200:203], v[120:123]\n"
    "v_mfma_f32_16x16x32_bf16 v[124:127], v[156:159], v[204:207], v[124:127]\n"
    "s_waitcnt lgkmcnt(0)\n"
    "s_waitcnt vmcnt(4)\n"
    "v_mfma_f32_16x16x32_bf16 v[64:67], v[128:131], v[176:179], v[64:67]\n"
    "ds_read_b128 v[192:195], v237 offset:36864\n"
    "s_add_u32 m0, s81, 16384\n"
    "s_nop 0\n"
    "buffer_load_dwordx4 v224, s[36:39], s80 offen offset:0 lds\n"
    "v_mfma_f32_16x16x32_bf16 v[68:71], v[128:131], v[180:183], v[68:71]\n"
    "ds_read_b128 v[196:199], v237 offset:37120\n"
    "buffer_load_dwordx4 v225, s[36:39], s80 offen offset:1024 lds\n"
    "v_mfma_f32_16x16x32_bf16 v[72:75], v[128:131], v[184:187], v[72:75]\n"
    "ds_read_b128 v[200:203], v237 offset:37376\n"
    "buffer_load_dwordx4 v226, s[36:39], s80 offen offset:2048 lds\n"
    "v_mfma_f32_16x16x32_bf16 v[76:79], v[128:131], v[188:191], v[76:79]\n"
    "ds_read_b128 v[204:207], v237 offset:37632\n"
    "buffer_load_dwordx4 v227, s[36:39], s80 offen offset:3072 lds\n"
    "s_add_u32 s80, s80, 114688\n"
    "v_mfma_f32_16x16x32_bf16 v[80:83], v[132:135], v[176:179], v[80:83]\n"
    "ds_read_b64_tr_b16 v[144:145], v232 offset:32768\n"
    "v_mfma_f32_16x16x32_bf16 v[84:87], v[132:135], v[180:183], v[84:87]\n"
    "ds_read_b64_tr_b16 v[146:147], v232 offset:34816\n"
    "v_mfma_f32_16x16x32_bf16 v[88:91], v[132:135], v[184:187], v[88:91]\n"
    "ds_read_b64_tr_b16 v[148:149], v233 offset:32768\n"
    "v_mfma_f32_16x16x32_bf16 v[92:95], v[132:135], v[188:191], v[92:95]\n"
    "ds_read_b64_tr_b16 v[150:151], v233 offset:34816\n"
    "v_mfma_f32_16x16x32_bf16 v[96:99], v[136:139], v[176:179], v[96:99]\n"
    "ds_read_b64_tr_b16 v[152:153], v234 offset:32768\n"
    "v_mfma_f32_16x16x32_bf16 v[100:103], v[136:139], v[180:183], v[100:103]\n"
    "ds_read_b64_tr_b16 v[154:155], v234 offset:34816\n"
    "v_mfma_f32_16x16x32_bf16 v[104:107], v[136:139], v[184:187], v[104:107]\n"
    "ds_read_b64_tr_b16 v[156:157], v235 offset:32768\n"
    "v_mfma_f32_16x16x32_bf16 v[108:111], v[136:139], v[188:191], v[108:111]\n"
    "ds_read_b64_tr_b16 v[158:159], v235 offset:34816\n"
    "v_mfma_f32_16x16x32_bf16 v[112:115], v[140:143], v[176:179], v[112:115]\n"
    "v_mfma_f32_16x16x32_bf16 v[116:119], v[140:143], v[180:183], v[116:119]\n"
    "v_mfma_f32_16x16x32_bf16 v[120:123], v[140:143], v[184:187], v[120:123]\n"
    "v_mfma_f32_16x16x32_bf16 v[124:127], v[140:143], v[188:191], v[124:127]\n"
    "s_waitcnt lgkmcnt(0)\n"
    "s_waitcnt vmcnt(4)\n"
    "v_mfma_f32_16x16x32_bf16 v[64:67], v[144:147], v[192:195], v[64:67]\n"
    "ds_read_b128 v[176:179], v236 offset:40960\n"
    "s_add_u32 m0, s81, 32768\n"
    "s_nop 0\n"
    "buffer_load_dwordx4 v224, s[36:39], s80 offen offset:0 lds\n"
    "v_mfma_f32_16x16x32_bf16 v[68:71], v[144:147], v[196:199], v[68:71]\n"
    "ds_read_b128 v[180:183], v236 offset:41216\n"
    "buffer_load_dwordx4 v225, s[36:39], s80 offen offset:1024 lds\n"
    "v_mfma_f32_16x16x32_bf16 v[72:75], v[144:147], v[200:203], v[72:75]\n"
    "ds_read_b128 v[184:187], v236 offset:41472\n"
    "buffer_load_dwordx4 v226, s[36:39], s80 offen offset:2048 lds\n"
    "v_mfma_f32_16x16x32_bf16 v[76:79], v[144:147], v[204:207], v[76:79]\n"
    "ds_read_b128 v[188:191], v236 offset:41728\n"
    "buffer_load_dwordx4 v227, s[36:39], s80 offen offset:3072 lds\n"
    "s_add_u32 s80, s80, 114688\n"
    "v_mfma_f32_16x16x32_bf16 v[80:83], v[148:151], v[192:195], v[80:83]\n"
    "ds_read_b64_tr_b16 v[128:129], v232 offset:0\n"
    "v_mfma_f32_16x16x32_bf16 v[84:87], v[148:151], v[196:199], v[84:87]\n"
    "ds_read_b64_tr_b16 v[130:131], v232 offset:2048\n"
    "v_mfma_f32_16x16x32_bf16 v[88:91], v[148:151], v[200:203], v[88:91]\n"
    "ds_read_b64_tr_b16 v[132:133], v233 offset:0\n"
    "v_mfma_f32_16x16x32_bf16 v[92:95], v[148:151], v[204:207], v[92:95]\n"
    "ds_read_b64_tr_b16 v[134:135], v233 offset:2048\n"
    "v_mfma_f32_16x16x32_bf16 v[96:99], v[152:155], v[192:195], v[96:99]\n"
    "ds_read_b64_tr_b16 v[136:137], v234 offset:0\n"
    "v_mfma_f32_16x16x32_bf16 v[100:103], v[152:155], v[196:199], v[100:103]\n"
    "ds_read_b64_tr_b16 v[138:139], v234 offset:2048\n"
    "v_mfma_f32_16x16x32_bf16 v[104:107], v[152:155], v[200:203], v[104:107]\n"
    "ds_read_b64_tr_b16 v[140:141], v235 offset:0\n"
    "v_mfma_f32_16x16x32_bf16 v[108:111], v[152:155], v[204:207], v[108:111]\n"
    "ds_read_b64_tr_b16 v[142:143], v235 offset:2048\n"
    "v_mfma_f32_16x16x32_bf16 v[112:115], v[156:159], v[192:195], v[112:115]\n"
    "v_mfma_f32_16x16x32_bf16 v[116:119], v[156:159], v[196:199], v[116:119]\n"
    "v_mfma_f32_16x16x32_bf16 v[120:123], v[156:159], v[200:203], v[120:123]\n"
    "v_mfma_f32_16x16x32_bf16 v[124:127], v[156:159], v[204:207], v[124:127]\n"
    "s_waitcnt lgkmcnt(0)\n"
    "s_waitcnt vmcnt(4)\n"
    "v_mfma_f32_16x16x32_bf16 v[64:67], v[128:131], v[176:179], v[64:67]\n"
    "ds_read_b128 v[192:195], v237 offset:45056\n"
    "s_add_u32 m0, s81, 0\n"
    "s_nop 0\n"
    "buffer_load_dwordx4 v224, s[36:39], s80 offen offset:0 lds\n"
    "v_mfma_f32_16x16x32_bf16 v[68:71], v[128:131], v[180:183], v[68:71]\n"
    "ds_read_b128 v[196:199], v237 offset:45312\n"
    "buffer_load_dwordx4 v225, s[36:39], s80 offen offset:1024 lds\n"
    "v_mfma_f32_16x16x32_bf16 v[72:75], v[128:131], v[184:187], v[72:75]\n"
    "ds_read_b128 v[200:203], v237 offset:45568\n"
    "buffer_load_dwordx4 v226, s[36:39], s80 offen offset:2048 lds\n"
    "v_mfma_f32_16x16x32_bf16 v[76:79], v[128:131], v[188:191], v[76:79]\n"
    "ds_read_b128 v[204:207], v237 offset:45824\n"
    "buffer_load_dwordx4 v227, s[36:39], s80 offen offset:3072 lds\n"
    "s_add_u32 s80, s80, 114688\n"
    "v_mfma_f32_16x16x32_bf16 v[80:83], v[132:135], v[176:179], v[80:83]\n"
    "ds_read_b64_tr_b16 v[144:145], v232 offset:16384\n"
    "v_mfma_f32_16x16x32_bf16 v[84:87], v[132:135], v[180:183], v[84:87]\n"
    "ds_read_b64_tr_b16 v[146:147], v232 offset:18432\n"
    "v_mfma_f32_16x16x32_bf16 v[88:91], v[132:135], v[184:187], v[88:91]\n"
    "ds_read_b64_tr_b16 v[148:149], v233 offset:16384\n"
    "v_mfma_f32_16x16x32_bf16 v[92:95], v[132:135], v[188:191], v[92:95]\n"
    "ds_read_b64_tr_b16 v[150:151], v233 offset:18432\n"
    "v_mfma_f32_16x16x32_bf16 v[96:99], v[136:139], v[176:179], v[96:99]\n"
    "ds_read_b64_tr_b16 v[152:153], v234 offset:16384\n"
    "v_mfma_f32_16x16x32_bf16 v[100:103], v[136:139], v[180:183], v[100:103]\n"
    "ds_read_b64_tr_b16 v[154:155], v234 offset:18432\n"
    "v_mfma_f32_16x16x32_bf16 v[104:107], v[136:139], v[184:187], v[104:107]\n"
    "ds_read_b64_tr_b16 v[156:157], v235 offset:16384\n"
    "v_mfma_f32_16x16x32_bf16 v[108:111], v[136:139], v[188:191], v[108:111]\n"
    "ds_read_b64_tr_b16 v[158:159], v235 offset:18432\n"
    "v_mfma_f32_16x16x32_bf16 v[112:115], v[140:143], v[176:179], v[112:115]\n"
    "v_mfma_f32_16x16x32_bf16 v[116:119], v[140:143], v[180:183], v[116:119]\n"
    "v_mfma_f32_16x16x32_bf16 v[120:123], v[140:143], v[184:187], v[120:123]\n"
    "v_mfma_f32_16x16x32_bf16 v[124:127], v[140:143], v[188:191], v[124:127]\n"
    "s_waitcnt lgkmcnt(0)\n"
    "s_waitcnt vmcnt(4)\n"
    "v_mfma_f32_16x16x32_bf16 v[64:67], v[144:147], v[192:195], v[64:67]\n"
    "ds_read_b128 v[176:179], v236 offset:49152\n"
    "s_add_u32 m0, s81, 16384\n"
    "s_nop 0\n"
    "buffer_load_dwordx4 v224, s[36:39], s80 offen offset:0 lds\n"
    "v_mfma_f32_16x16x32_bf16 v[68:71], v[144:147], v[196:199], v[68:71]\n"
    "ds_read_b128 v[180:183], v236 offset:49408\n"
    "buffer_load_dwordx4 v225, s[36:39], s80 offen offset:1024 lds\n"
    "v_mfma_f32_16x16x32_bf16 v[72:75], v[144:147], v[200:203], v[72:75]\n"
    "ds_read_b128 v[184:187], v236 offset:49664\n"
    "buffer_load_dwordx4 v226, s[36:39], s80 offen offset:2048 lds\n"
    "v_mfma_f32_16x16x32_bf16 v[76:79], v[144:147], v[204:207], v[76:79]\n"
    "ds_read_b128 v[188:191], v236 offset:49920\n"
    "buffer_load_dwordx4 v227, s[36:39], s80 offen offset:3072 lds\n"
    "s_add_u32 s80, s80, 114688\n"
    "v_mfma_f32_16x16x32_bf16 v[80:83], v[148:151], v[192:195], v[80:83]\n"
    "ds_read_b64_tr_b16 v[128:129], v232 offset:32768\n"
    "v_mfma_f32_16x16x32_bf16 v[84:87], v[148:151], v[196:199], v[84:87]\n"
    "ds_read_b64_tr_b16 v[130:131], v232 offset:34816\n"
    "v_mfma_f32_16x16x32_bf16 v[88:91], v[148:151], v[200:203], v[88:91]\n"
    "ds_read_b64_tr_b16 v[132:133], v233 offset:32768\n"
    "v_mfma_f32_16x16x32_bf16 v[92:95], v[148:151], v[204:207], v[92:95]\n"
    "ds_read_b64_tr_b16 v[134:135], v233 offset:34816\n"
    "v_mfma_f32_16x16x32_bf16 v[96:99], v[152:155], v[192:195], v[96:99]\n"
    "ds_read_b64_tr_b16 v[136:137], v234 offset:32768\n"
    "v_mfma_f32_16x16x32_bf16 v[100:103], v[152:155], v[196:199], v[100:103]\n"
    "ds_read_b64_tr_b16 v[138:139], v234 offset:34816\n"
    "v_mfma_f32_16x16x32_bf16 v[104:107], v[152:155], v[200:203], v[104:107]\n"
    "ds_read_b64_tr_b16 v[140:141], v235 offset:32768\n"
    "v_mfma_f32_16x16x32_bf16 v[108:111], v[152:155], v[204:207], v[108:111]\n"
    "ds_read_b64_tr_b16 v[142:143], v235 offset:34816\n"
    "v_mfma_f32_16x16x32_bf16 v[112:115], v[156:159], v[192:195], v[112:115]\n"
    "v_mfma_f32_16x16x32_bf16 v[116:119], v[156:159], v[196:199], v[116:119]\n"
    "v_mfma_f32_16x16x32_bf16 v[120:123], v[156:159], v[200:203], v[120:123]\n"
    "v_mfma_f32_16x16x32_bf16 v[124:127], v[156:159], v[204:207], v[124:127]\n"
    "s_waitcnt lgkmcnt(0)\n"
    "s_waitcnt vmcnt(4)\n"
    "v_mfma_f32_16x16x32_bf16 v[64:67], v[128:131], v[176:179], v[64:67]\n"
    "ds_read_b128 v[192:195], v237 offset:53248\n"
    "s_add_u32 m0, s81, 32768\n"
    "s_nop 0\n"
    "buffer_load_dwordx4 v224, s[36:39], s80 offen offset:0 lds\n"
    "v_mfma_f32_16x16x32_bf16 v[68:71], v[128:131], v[180:183], v[68:71]\n"
    "ds_read_b128 v[196:199], v237 offset:53504\n"
    "buffer_load_dwordx4 v225, s[36:39], s80 offen offset:1024 lds\n"
    "v_mfma_f32_16x16x32_bf16 v[72:75], v[128:131], v[184:187], v[72:75]\n"
    "ds_read_b128 v[200:203], v237 offset:53760\n"
    "buffer_load_dwordx4 v226, s[36:39], s80 offen offset:2048 lds\n"
    "v_mfma_f32_16x16x32_bf16 v[76:79], v[128:131], v[188:191], v[76:79]\n"
    "ds_read_b128 v[204:207], v237 offset:54016\n"
    "buffer_load_dwordx4 v227, s[36:39], s80 offen offset:3072 lds\n"
    "s_add_u32 s80, s80, 114688\n"
    "v_mfma_f32_16x16x32_bf16 v[80:83], v[132:135], v[176:179], v[80:83]\n"
    "ds_read_b64_tr_b16 v[144:145], v232 offset:0\n"
    "v_mfma_f32_16x16x32_bf16 v[84:87], v[132:135], v[180:183], v[84:87]\n"
    "ds_read_b64_tr_b16 v[146:147], v232 offset:2048\n"
    "v_mfma_f32_16x16x32_bf16 v[88:91], v[132:135], v[184:187], v[88:91]\n"
    "ds_read_b64_tr_b16 v[148:149], v233 offset:0\n"
    "v_mfma_f32_16x16x32_bf16 v[92:95], v[132:135], v[188:191], v[92:95]\n"
    "ds_read_b64_tr_b16 v[150:151], v233 offset:2048\n"
    "v_mfma_f32_16x16x32_bf16 v[96:99], v[136:139], v[176:179], v[96:99]\n"
    "ds_read_b64_tr_b16 v[152:153], v234 offset:0\n"
    "v_mfma_f32_16x16x32_bf16 v[100:103], v[136:139], v[180:183], v[100:103]\n"
    "ds_read_b64_tr_b16 v[154:155], v234 offset:2048\n"
    "v_mfma_f32_16x16x32_bf16 v[104:107], v[136:139], v[184:187], v[104:107]\n"
    "ds_read_b64_tr_b16 v[156:157], v235 offset:0\n"
    "v_mfma_f32_16x16x32_bf16 v[108:111], v[136:139], v[188:191], v[108:111]\n"
    "ds_read_b64_tr_b16 v[158:159], v235 offset:2048\n"
    "v_mfma_f32_16x16x32_bf16 v[112:115], v[140:143], v[176:179], v[112:115]\n"
    "v_mfma_f32_16x16x32_bf16 v[116:119], v[140:143], v[180:183], v[116:119]\n"
    "v_mfma_f32_16x16x32_bf16 v[120:123], v[140:143], v[184:187], v[120:123]\n"
    "v_mfma_f32_16x16x32_bf16 v[124:127], v[140:143], v[188:191], v[124:127]\n"
    "s_waitcnt lgkmcnt(0)\n"
    "s_waitcnt vmcnt(4)\n"
    "v_mfma_f32_16x16x32_bf16 v[64:67], v[144:147], v[192:195], v[64:67]\n"
    "ds_read_b128 v[176:179], v236 offset:57344\n"
    "v_mfma_f32_16x16x32_bf16 v[68:71], v[144:147], v[196:199], v[68:71]\n"
    "ds_read_b128 v[180:183], v236 offset:57600\n"
    "v_mfma_f32_16x16x32_bf16 v[72:75], v[144:147], v[200:203], v[72:75]\n"
    "ds_read_b128 v[184:187], v236 offset:57856\n"
    "v_mfma_f32_16x16x32_bf16 v[76:79], v[144:147], v[204:207], v[76:79]\n"
    "ds_read_b128 v[188:191], v236 offset:58112\n"
    "v_mfma_f32_16x16x32_bf16 v[80:83], v[148:151], v[192:195], v[80:83]\n"
    "ds_read_b64_tr_b16 v[128:129], v232 offset:16384\n"
    "v_mfma_f32_16x16x32_bf16 v[84:87], v[148:151], v[196:199], v[84:87]\n"
    "ds_read_b64_tr_b16 v[130:131], v232 offset:18432\n"
    "v_mfma_f32_16x16x32_bf16 v[88:91], v[148:151], v[200:203], v[88:91]\n"
    "ds_read_b64_tr_b16 v[132:133], v233 offset:16384\n"
    "v_mfma_f32_16x16x32_bf16 v[92:95], v[148:151], v[204:207], v[92:95]\n"
    "ds_read_b64_tr_b16 v[134:135], v233 offset:18432\n"
    "v_mfma_f32_16x16x32_bf16 v[96:99], v[152:155], v[192:195], v[96:99]\n"
    "ds_read_b64_tr_b16 v[136:137], v234 offset:16384\n"
    "v_mfma_f32_16x16x32_bf16 v[100:103], v[152:155], v[196:199], v[100:103]\n"
    "ds_read_b64_tr_b16 v[138:139], v234 offset:18432\n"
    "v_mfma_f32_16x16x32_bf16 v[104:107], v[152:155], v[200:203], v[104:107]\n"
    "ds_read_b64_tr_b16 v[140:141], v235 offset:16384\n"
    "v_mfma_f32_16x16x32_bf16 v[108:111], v[152:155], v[204:207], v[108:111]\n"
    "ds_read_b64_tr_b16 v[142:143], v235 offset:18432\n"
    "v_mfma_f32_16x16x32_bf16 v[112:115], v[156:159], v[192:195], v[112:115]\n"
    "v_mfma_f32_16x16x32_bf16 v[116:119], v[156:159], v[196:199], v[116:119]\n"
    "v_mfma_f32_16x16x32_bf16 v[120:123], v[156:159], v[200:203], v[120:123]\n"
    "v_mfma_f32_16x16x32_bf16 v[124:127], v[156:159], v[204:207], v[124:127]\n"
    "s_waitcnt lgkmcnt(0)\n"
    "s_waitcnt vmcnt(0)\n"
    "v_mfma_f32_16x16x32_bf16 v[64:67], v[128:131], v[176:179], v[64:67]\n"
    "ds_read_b128 v[192:195], v237 offset:61440\n"
    "v_mfma_f32_16x16x32_bf16 v[68:71], v[128:131], v[180:183], v[68:71]\n"
    "ds_read_b128 v[196:199], v237 offset:61696\n"
    "v_mfma_f32_16x16x32_bf16 v[72:75], v[128:131], v[184:187], v[72:75]\n"
    "ds_read_b128 v[200:203], v237 offset:61952\n"
    "v_mfma_f32_16x16x32_bf16 v[76:79], v[128:131], v[188:191], v[76:79]\n"
    "ds_read_b128 v[204:207], v237 offset:62208\n"
    "v_mfma_f32_16x16x32_bf16 v[80:83], v[132:135], v[176:179], v[80:83]\n"
    "ds_read_b64_tr_b16 v[144:145], v232 offset:32768\n"
    "v_mfma_f32_16x16x32_bf16 v[84:87], v[132:135], v[180:183], v[84:87]\n"
    "ds_read_b64_tr_b16 v[146:147], v232 offset:34816\n"
    "v_mfma_f32_16x16x32_bf16 v[88:91], v[132:135], v[184:187], v[88:91]\n"
    "ds_read_b64_tr_b16 v[148:149], v233 offset:32768\n"
    "v_mfma_f32_16x16x32_bf16 v[92:95], v[132:135], v[188:191], v[92:95]\n"
    "ds_read_b64_tr_b16 v[150:151], v233 offset:34816\n"
    "v_mfma_f32_16x16x32_bf16 v[96:99], v[136:139], v[176:179], v[96:99]\n"
    "ds_read_b64_tr_b16 v[152:153], v234 offset:32768\n"
    "v_mfma_f32_16x16x32_bf16 v[100:103], v[136:139], v[180:183], v[100:103]\n"
    "ds_read_b64_tr_b16 v[154:155], v234 offset:34816\n"
    "v_mfma_f32_16x16x32_bf16 v[104:107], v[136:139], v[184:187], v[104:107]\n"
    "ds_read_b64_tr_b16 v[156:157], v235 offset:32768\n"
    "v_mfma_f32_16x16x32_bf16 v[108:111], v[136:139], v[188:191], v[108:111]\n"
    "ds_read_b64_tr_b16 v[158:159], v235 offset:34816\n"
    "v_mfma_f32_16x16x32_bf16 v[112:115], v[140:143], v[176:179], v[112:115]\n"
    "v_mfma_f32_16x16x32_bf16 v[116:119], v[140:143], v[180:183], v[116:119]\n"
    "v_mfma_f32_16x16x32_bf16 v[120:123], v[140:143], v[184:187], v[120:123]\n"
    "v_mfma_f32_16x16x32_bf16 v[124:127], v[140:143], v[188:191], v[124:127]\n"
    "s_waitcnt lgkmcnt(0)\n"
    "v_mfma_f32_16x16x32_bf16 v[64:67], v[144:147], v[192:195], v[64:67]\n"
    "v_mfma_f32_16x16x32_bf16 v[68:71], v[144:147], v[196:199], v[68:71]\n"
    "v_mfma_f32_16x16x32_bf16 v[72:75], v[144:147], v[200:203], v[72:75]\n"
    "v_mfma_f32_16x16x32_bf16 v[76:79], v[144:147], v[204:207], v[76:79]\n"
    "v_mfma_f32_16x16x32_bf16 v[80:83], v[148:151], v[192:195], v[80:83]\n"
    "v_mfma_f32_16x16x32_bf16 v[84:87], v[148:151], v[196:199], v[84:87]\n"
    "v_mfma_f32_16x16x32_bf16 v[88:91], v[148:151], v[200:203], v[88:91]\n"
    "v_mfma_f32_16x16x32_bf16 v[92:95], v[148:151], v[204:207], v[92:95]\n"
    "v_mfma_f32_16x16x32_bf16 v[96:99], v[152:155], v[192:195], v[96:99]\n"
    "v_mfma_f32_16x16x32_bf16 v[100:103], v[152:155], v[196:199], v[100:103]\n"
    "v_mfma_f32_16x16x32_bf16 v[104:107], v[152:155], v[200:203], v[104:107]\n"
    "v_mfma_f32_16x16x32_bf16 v[108:111], v[152:155], v[204:207], v[108:111]\n"
    "v_mfma_f32_16x16x32_bf16 v[112:115], v[156:159], v[192:195], v[112:115]\n"
    "v_mfma_f32_16x16x32_bf16 v[116:119], v[156:159], v[196:199], v[116:119]\n"
    "v_mfma_f32_16x16x32_bf16 v[120:123], v[156:159], v[200:203], v[120:123]\n"
    "v_mfma_f32_16x16x32_bf16 v[124:127], v[156:159], v[204:207], v[124:127]\n"
    "s_setprio 2\n"
    "s_mul_i32 s76, s73, 229376\n"
    "s_lshl_b32 s77, s82, 13\n"
    "s_add_u32 s76, s76, s77\n"
    "s_add_u32 s76, s76, 5242880\n"
    "s_add_u32 s77, s76, 4096\n"
    "v_cvt_pk_bf16_f32 v176, v64, v65\n"
    "v_cvt_pk_bf16_f32 v177, v66, v67\n"
    "v_cvt_pk_bf16_f32 v178, v80, v81\n"
    "v_cvt_pk_bf16_f32 v179, v82, v83\n"
    "v_pk_max_i16 v176, v176, 0\n"
    "v_pk_max_i16 v177, v177, 0\n"
    "v_pk_max_i16 v178, v178, 0\n"
    "v_pk_max_i16 v179, v179, 0\n"
    "buffer_store_dwordx4 v[176:179], v244, s[48:51], s76 offen offset:0 sc1\n"
    "v_cvt_pk_bf16_f32 v180, v68, v69\n"
    "v_cvt_pk_bf16_f32 v181, v70, v71\n"
    "v_cvt_pk_bf16_f32 v182, v84, v85\n"
    "v_cvt_pk_bf16_f32 v183, v86, v87\n"
    "v_pk_max_i16 v180, v180, 0\n"
    "v_pk_max_i16 v181, v181, 0\n"
    "v_pk_max_i16 v182, v182, 0\n"
    "v_pk_max_i16 v183, v183, 0\n"
    "buffer_store_dwordx4 v[180:183], v244, s[48:51], s76 offen offset:1024 sc1\n"
    "v_cvt_pk_bf16_f32 v184, v72, v73\n"
    "v_cvt_pk_bf16_f32 v185, v74, v75\n"
    "v_cvt_pk_bf16_f32 v186, v88, v89\n"
    "v_cvt_pk_bf16_f32 v187, v90, v91\n"
    "v_pk_max_i16 v184, v184, 0\n"
    "v_pk_max_i16 v185, v185, 0\n"
    "v_pk_max_i16 v186, v186, 0\n"
    "v_pk_max_i16 v187, v187, 0\n"
    "buffer_store_dwordx4 v[184:187], v244, s[48:51], s76 offen offset:2048 sc1\n"
    "v_cvt_pk_bf16_f32 v188, v76, v77\n"
    "v_cvt_pk_bf16_f32 v189, v78, v79\n"
    "v_cvt_pk_bf16_f32 v190, v92, v93\n"
    "v_cvt_pk_bf16_f32 v191, v94, v95\n"
    "v_pk_max_i16 v188, v188, 0\n"
    "v_pk_max_i16 v189, v189, 0\n"
    "v_pk_max_i16 v190, v190, 0\n"
    "v_pk_max_i16 v191, v191, 0\n"
    "buffer_store_dwordx4 v[188:191], v244, s[48:51], s76 offen offset:3072 sc1\n"
    "v_cvt_pk_bf16_f32 v192, v96, v97\n"
    "v_cvt_pk_bf16_f32 v193, v98, v99\n"
    "v_cvt_pk_bf16_f32 v194, v112, v113\n"
    "v_cvt_pk_bf16_f32 v195, v114, v115\n"
    "v_pk_max_i16 v192, v192, 0\n"
    "v_pk_max_i16 v193, v193, 0\n"
    "v_pk_max_i16 v194, v194, 0\n"
    "v_pk_max_i16 v195, v195, 0\n"
    "buffer_store_dwordx4 v[192:195], v244, s[48:51], s77 offen offset:0 sc1\n"
    "v_cvt_pk_bf16_f32 v196, v100, v101\n"
    "v_cvt_pk_bf16_f32 v197, v102, v103\n"
    "v_cvt_pk_bf16_f32 v198, v116, v117\n"
    "v_cvt_pk_bf16_f32 v199, v118, v119\n"
    "v_pk_max_i16 v196, v196, 0\n"
    "v_pk_max_i16 v197, v197, 0\n"
    "v_pk_max_i16 v198, v198, 0\n"
    "v_pk_max_i16 v199, v199, 0\n"
    "buffer_store_dwordx4 v[196:199], v244, s[48:51], s77 offen offset:1024 sc1\n"
    "v_cvt_pk_bf16_f32 v200, v104, v105\n"
    "v_cvt_pk_bf16_f32 v201, v106, v107\n"
    "v_cvt_pk_bf16_f32 v202, v120, v121\n"
    "v_cvt_pk_bf16_f32 v203, v122, v123\n"
    "v_pk_max_i16 v200, v200, 0\n"
    "v_pk_max_i16 v201, v201, 0\n"
    "v_pk_max_i16 v202, v202, 0\n"
    "v_pk_max_i16 v203, v203, 0\n"
    "buffer_store_dwordx4 v[200:203], v244, s[48:51], s77 offen offset:2048 sc1\n"
    "v_cvt_pk_bf16_f32 v204, v108, v109\n"
    "v_cvt_pk_bf16_f32 v205, v110, v111\n"
    "v_cvt_pk_bf16_f32 v206, v124, v125\n"
    "v_cvt_pk_bf16_f32 v207, v126, v127\n"
    "v_pk_max_i16 v204, v204, 0\n"
    "v_pk_max_i16 v205, v205, 0\n"
    "v_pk_max_i16 v206, v206, 0\n"
    "v_pk_max_i16 v207, v207, 0\n"
    "buffer_store_dwordx4 v[204:207], v244, s[48:51], s77 offen offset:3072 sc1\n"
    ".L_nof1_k:\n"
    "s_setprio 2\n"
    "s_mul_i32 s80, s75, 458752\n"
    "s_lshl_b32 s76, s74, 7\n"
    "s_add_u32 s80, s80, s76\n"
    "s_add_u32 s99, s80, 458752\n"
    "s_lshr_b32 s76, s74, 1\n"
    "s_mul_i32 s76, s76, 7\n"
    "s_lshr_b32 s19, s76, 1\n"
    "s_lshl_b32 s76, s19, 15\n"
    "s_add_u32 s80, s80, s76\n"
    "v_mov_b32 v20, s70\n"
    "v_mov_b32 v21, s71\n"
    "v_mov_b32 v22, 0\n"
    "v_min_u32 v23, 6, v1\n"
    "v_lshlrev_b32 v23, 3, v23\n"
    "v_mov_b32 v8, 1\n"
    "ds_write_b8 v249, v8 offset:40\n"
    "s_add_u32 m0, s81, 32768\n"
    "s_nop 0\n"
    "buffer_load_dwordx4 v228, s[40:43], s80 offen offset:0 lds\n"
    "buffer_load_dwordx4 v229, s[40:43], s80 offen offset:1024 lds\n"
    "buffer_load_dwordx4 v230, s[40:43], s80 offen offset:2048 lds\n"
    "buffer_load_dwordx4 v231, s[40:43], s80 offen offset:3072 lds\n"
    "s_add_u32 s80, s80, 32768\n"
    "s_cmp_eq_u32 s80, s99\n"
    "s_cselect_b32 s76, 458752, 0\n"
    "s_sub_u32 s80, s80, s76\n"
    "s_add_u32 m0, s81, 0\n"
    "s_nop 0\n"
    "buffer_load_dwordx4 v228, s[40:43], s80 offen offset:0 lds\n"
    "buffer_load_dwordx4 v229, s[40:43], s80 offen offset:1024 lds\n"
    "buffer_load_dwordx4 v230, s[40:43], s80 offen offset:2048 lds\n"
    "buffer_load_dwordx4 v231, s[40:43], s80 offen offset:3072 lds\n"
    "s_add_u32 s80, s80, 32768\n"
    "s_cmp_eq_u32 s80, s99\n"
    "s_cselect_b32 s76, 458752, 0\n"
    "s_sub_u32 s80, s80, s76\n"
    "s_add_u32 m0, s81, 16384\n"
    "s_nop 0\n"
    "buffer_load_dwordx4 v228, s[40:43], s80 offen offset:0 lds\n"
    "buffer_load_dwordx4 v229, s[40:43], s80 offen offset:1024 lds\n"
    "buffer_load_dwordx4 v230, s[40:43], s80 offen offset:2048 lds\n"
    "buffer_load_dwordx4 v231, s[40:43], s80 offen offset:3072 lds\n"
    "s_add_u32 s80, s80, 32768\n"
    "s_cmp_eq_u32 s80, s99\n"
    "s_cselect_b32 s76, 458752, 0\n"
    "s_sub_u32 s80, s80, s76\n"
    "s_cmp_ge_u32 s82, 28\n"
    "s_cbranch_scc1 .L_nofl_k\n"
    "s_lshl_b32 s76, s73, 8\n"
    "s_lshl_b32 s77, s82, 3\n"
    "s_add_u32 s76, s76, s77\n"
    "s_add_u32 s76, s76, 4096\n"
    "s_waitcnt vmcnt(12)\n"
    "s_mov_b64 exec, 1\n"
    "buffer_store_dwordx2 v[20:21], v22, s[48:51], s76 offen sc1\n"
    "s_mov_b64 exec, -1\n"
    ".L_nofl_k:\n"
    "s_and_b32 s76, s73, 7\n"
    "s_mul_i32 s77, s76, 57344\n"
    "s_add_u32 s77, s77, s99\n"
    "s_sub_u32 s77, s77, 458752\n"
    "v_min_u32 v10, 55, v1\n"
    "v_lshlrev_b32 v10, 10, v10\n"
    "buffer_load_dword v252, v10, s[40:43], s77 offen\n"
    "buffer_load_dwordx2 v[24:25], v245, s[20:23], s85 offen offset:0\n"
    "buffer_load_dwordx2 v[26:27], v245, s[20:23], s85 offen offset:32\n"
    "buffer_load_dwordx2 v[28:29], v245, s[20:23], s85 offen offset:64\n"
    "buffer_load_dwordx2 v[30:31], v245, s[20:23], s85 offen offset:96\n"
    "s_mov_b32 s96, 131072\n"
    "s_setprio 0\n"
    ".Lsp_25_k:\n"
    "ds_read_b32 v12, v243 offset:40\n"
    "s_waitcnt lgkmcnt(0)\n"
    "v_readfirstlane_b32 s94, v12\n"
    "s_cmp_eq_u32 s94, 0x1010101\n"
    "s_cbranch_scc1 .Lspd_26_k\n"
    "s_sleep 1\n"
    "s_sub_u32 s96, s96, 1\n"
    "s_cbranch_scc0 .Lsp_25_k\n"
    ".Lspd_26_k:\n"
    "s_mov_b32 s96, 131072\n"
    ".Lsp_27_k:\n"
    "ds_read_b32 v12, v243 offset:24\n"
    "s_waitcnt lgkmcnt(0)\n"
    "v_readfirstlane_b32 s94, v12\n"
    "s_cmp_eq_u32 s94, 0x1010101\n"
    "s_cbranch_scc1 .Lspd_28_k\n"
    "s_sleep 1\n"
    "s_sub_u32 s96, s96, 1\n"
    "s_cbranch_scc0 .Lsp_27_k\n"
    ".Lspd_28_k:\n"
    "s_lshl_b32 s84, s75, 17\n"
    "s_lshl_b32 s76, s74, 7\n"
    "s_add_u32 s84, s84, s76\n"
    "s_lshl_b32 s77, s75, 14\n"
    "s_add_u32 m0, s77, 0\n"
    "s_nop 0\n"
    "buffer_load_dwordx4 v228, s[32:35], s84 offen offset:0 lds\n"
    "buffer_load_dwordx4 v229, s[32:35], s84 offen offset:1024 lds\n"
    "buffer_load_dwordx4 v230, s[32:35], s84 offen offset:2048 lds\n"
    "buffer_load_dwordx4 v231, s[32:35], s84 offen offset:3072 lds\n"
    "s_add_u32 s84, s84, 32768\n"
    "s_add_u32 m0, s77, 4096\n"
    "s_nop 0\n"
    "buffer_load_dwordx4 v228, s[32:35], s84 offen offset:0 lds\n"
    "buffer_load_dwordx4 v229, s[32:35], s84 offen offset:1024 lds\n"
    "buffer_load_dwordx4 v230, s[32:35], s84 offen offset:2048 lds\n"
    "buffer_load_dwordx4 v231, s[32:35], s84 offen offset:3072 lds\n"
    "s_add_u32 s84, s84, 32768\n"
    "s_add_u32 m0, s77, 8192\n"
    "s_nop 0\n"
    "buffer_load_dwordx4 v228, s[32:35], s84 offen offset:0 lds\n"
    "buffer_load_dwordx4 v229, s[32:35], s84 offen offset:1024 lds\n"
    "buffer_load_dwordx4 v230, s[32:35], s84 offen offset:2048 lds\n"
    "buffer_load_dwordx4 v231, s[32:35], s84 offen offset:3072 lds\n"
    "s_add_u32 s84, s84, 32768\n"
    "s_add_u32 m0, s77, 12288\n"
    "s_nop 0\n"
    "buffer_load_dwordx4 v228, s[32:35], s84 offen offset:0 lds\n"
    "buffer_load_dwordx4 v229, s[32:35], s84 offen offset:1024 lds\n"
    "buffer_load_dwordx4 v230, s[32:35], s84 offen offset:2048 lds\n"
    "buffer_load_dwordx4 v231, s[32:35], s84 offen offset:3072 lds\n"
    "s_add_u32 s84, s84, 32768\n"
    "s_lshl_b32 s78, s73, 8\n"
    "s_mul_i32 s77, s75, 56\n"
    "s_add_u32 s78, s78, s77\n"
    "s_add_u32 s78, s78, 4096\n"
    "s_mul_i32 s83, s73, 229376\n"
    "s_mul_i32 s77, s75, 57344\n"
    "s_add_u32 s83, s83, s77\n"
    "s_add_u32 s83, s83, 5242880\n"
    "s_add_u32 s18, s83, 57344\n"
    "s_lshl_b32 s77, s19, 12\n"
    "s_add_u32 s83, s83, s77\n"
    "s_mov_b32 s89, 8192\n"
    ".L_hw_k:\n"
    "s_setprio 0\n"
    "buffer_load_dwordx2 v[8:9], v23, s[48:51], s78 offen sc1\n"
    "ds_read_b32 v12, v243 offset:36\n"
    "s_cmp_lg_u32 s90, 0\n"
    "s_waitcnt vmcnt(0) lgkmcnt(0)\n"
    "v_cmp_eq_u64 vcc, s[70:71], v[8:9]\n"
    "s_cbranch_scc1 .L_hw2_k\n"
    "v_readfirstlane_b32 s94, v12\n"
    "s_cmp_eq_u64 vcc, -1\n"
    "s_cselect_b32 s91, 3, 0\n"
    "s_or_b32 s94, s94, 0x80808080\n"
    "s_sub_u32 s94, s94, s88\n"
    "s_and_b32 s94, s94, 0x80808080\n"
    "s_cmp_eq_u32 s94, 0x80808080\n"
    "s_cbranch_scc1 .L_red1_k\n"
    ".L_hw2_k:\n"
    "s_cmp_lg_u32 s90, 3\n"
    "s_cbranch_scc1 .Lnf_29_k\n"
    "s_mov_b32 s90, 1\n"
    "s_mov_b64 exec, 1\n"
    "buffer_store_dwordx2 v[20:21], v22, s[48:51], s95 offen sc1\n"
    "s_mov_b64 exec, -1\n"
    ".Lnf_29_k:\n"
    "s_cmp_eq_u64 vcc, -1\n"
    "s_cbranch_scc1 .L_hgo_k\n"
    "s_sleep 1\n"
    "s_sub_u32 s89, s89, 1\n"
    "s_cbranch_scc0 .L_hw_k\n"
    ".L_hgo_k:\n"
    "s_setprio 2\n"
    "buffer_load_dwordx4 v[160:163], v238, s[48:51], s83 offen sc1\n"
    "buffer_load_dwordx4 v[164:167], v239, s[48:51], s83 offen sc1\n"
    "buffer_load_dwordx4 v[168:171], v240, s[48:51], s83 offen sc1\n"
    "buffer_load_dwordx4 v[172:175], v241, s[48:51], s83 offen sc1\n"
    "s_add_u32 s83, s83, 4096\n"
    "s_cmp_eq_u32 s83, s18\n"
    "s_cselect_b32 s76, 57344, 0\n"
    "s_sub_u32 s83, s83, s76\n"
    "buffer_load_dwordx4 v[176:179], v238, s[48:51], s83 offen sc1\n"
    "buffer_load_dwordx4 v[180:183], v239, s[48:51], s83 offen sc1\n"
    "buffer_load_dwordx4 v[184:187], v240, s[48:51], s83 offen sc1\n"
    "buffer_load_dwordx4 v[188:191], v241, s[48:51], s83 offen sc1\n"
    "s_add_u32 s83, s83, 4096\n"
    "s_cmp_eq_u32 s83, s18\n"
    "s_cselect_b32 s76, 57344, 0\n"
    "s_sub_u32 s83, s83, s76\n"
    "buffer_load_dwordx4 v[192:195], v238, s[48:51], s83 offen sc1\n"
    "buffer_load_dwordx4 v[196:199], v239, s[48:51], s83 offen sc1\n"
    "buffer_load_dwordx4 v[200:203], v240, s[48:51], s83 offen sc1\n"
    "buffer_load_dwordx4 v[204:207], v241, s[48:51], s83 offen sc1\n"
    "s_add_u32 s83, s83, 4096\n"
    "s_cmp_eq_u32 s83, s18\n"
    "s_cselect_b32 s76, 57344, 0\n"
    "s_sub_u32 s83, s83, s76\n"
    "v_mov_b32 v9, 1\n"
    "ds_write_b8 v249, v9 offset:48\n"
    "ds_read_b64_tr_b16 v[128:129], v232 offset:32768\n"
    "ds_read_b64_tr_b16 v[130:131], v232 offset:34816\n"
    "ds_read_b64_tr_b16 v[132:133], v233 offset:32768\n"
    "ds_read_b64_tr_b16 v[134:135], v233 offset:34816\n"
    "ds_read_b64_tr_b16 v[136:137], v234 offset:32768\n"
    "ds_read_b64_tr_b16 v[138:139], v234 offset:34816\n"
    "ds_read_b64_tr_b16 v[140:141], v235 offset:32768\n"
    "ds_read_b64_tr_b16 v[142:143], v235 offset:34816\n"
    "s_waitcnt lgkmcnt(0)\n"
    "s_setprio 0\n"
    "s_waitcnt vmcnt(8)\n"
    "s_cmp_lg_u32 s90, 3\n"
    "s_cbranch_scc1 .Lnf_30_k\n"
    "s_mov_b32 s90, 1\n"
    "s_mov_b64 exec, 1\n"
    "buffer_store_dwordx2 v[20:21], v22, s[48:51], s95 offen sc1\n"
    "s_mov_b64 exec, -1\n"
    ".Lnf_30_k:\n"
    "v_mfma_f32_16x16x32_bf16 v[64:67], v[128:131], v[160:163], 0\n"
    "ds_read_b64_tr_b16 v[144:145], v232 offset:0\n"
    "s_add_u32 m0, s81, 32768\n"
    "s_nop 0\n"
    "buffer_load_dwordx4 v228, s[40:43], s80 offen offset:0 lds\n"
    "v_mfma_f32_16x16x32_bf16 v[68:71], v[128:131], v[164:167], 0\n"
    "ds_read_b64_tr_b16 v[146:147], v232 offset:2048\n"
    "buffer_load_dwordx4 v229, s[40:43], s80 offen offset:1024 lds\n"
    "v_mfma_f32_16x16x32_bf16 v[72:75], v[128:131], v[168:171], 0\n"
    "ds_read_b64_tr_b16 v[148:149], v233 offset:0\n"
    "buffer_load_dwordx4 v230, s[40:43], s80 offen offset:2048 lds\n"
    "v_mfma_f32_16x16x32_bf16 v[76:79], v[128:131], v[172:175], 0\n"
    "ds_read_b64_tr_b16 v[150:151], v233 offset:2048\n"
    "buffer_load_dwordx4 v231, s[40:43], s80 offen offset:3072 lds\n"
    "s_add_u32 s80, s80, 32768\n"
    "s_cmp_eq_u32 s80, s99\n"
    "s_cselect_b32 s76, 458752, 0\n"
    "s_sub_u32 s80, s80, s76\n"
    "v_mfma_f32_16x16x32_bf16 v[80:83], v[132:135], v[160:163], 0\n"
    "ds_read_b64_tr_b16 v[152:153], v234 offset:0\n"
    "buffer_load_dwordx4 v[208:211], v238, s[48:51], s83 offen sc1\n"
    "v_mfma_f32_16x16x32_bf16 v[84:87], v[132:135], v[164:167], 0\n"
    "ds_read_b64_tr_b16 v[154:155], v234 offset:2048\n"
    "buffer_load_dwordx4 v[212:215], v239, s[48:51], s83 offen sc1\n"
    "v_mfma_f32_16x16x32_bf16 v[88:91], v[132:135], v[168:171], 0\n"
    "ds_read_b64_tr_b16 v[156:157], v235 offset:0\n"
    "buffer_load_dwordx4 v[216:219], v240, s[48:51], s83 offen sc1\n"
    "v_mfma_f32_16x16x32_bf16 v[92:95], v[132:135], v[172:175], 0\n"
    "ds_read_b64_tr_b16 v[158:159], v235 offset:2048\n"
    "buffer_load_dwordx4 v[220:223], v241, s[48:51], s83 offen sc1\n"
    "s_add_u32 s83, s83, 4096\n"
    "s_cmp_eq_u32 s83, s18\n"
    "s_cselect_b32 s76, 57344, 0\n"
    "s_sub_u32 s83, s83, s76\n"
    "v_mfma_f32_16x16x32_bf16 v[96:99], v[136:139], v[160:163], 0\n"
    "v_mfma_f32_16x16x32_bf16 v[100:103], v[136:139], v[164:167], 0\n"
    "v_mfma_f32_16x16x32_bf16 v[104:107], v[136:139], v[168:171], 0\n"
    "v_mfma_f32_16x16x32_bf16 v[108:111], v[136:139], v[172:175], 0\n"
    "v_mfma_f32_16x16x32_bf16 v[112:115], v[140:143], v[160:163], 0\n"
    "v_mfma_f32_16x16x32_bf16 v[116:119], v[140:143], v[164:167], 0\n"
    "v_mfma_f32_16x16x32_bf16 v[120:123], v[140:143], v[168:171], 0\n"
    "v_mfma_f32_16x16x32_bf16 v[124:127], v[140:143], v[172:175], 0\n"
    "s_waitcnt lgkmcnt(0)\n"
    "s_waitcnt vmcnt(12)\n"
    "v_mfma_f32_16x16x32_bf16 v[64:67], v[144:147], v[176:179], v[64:67]\n"
    "ds_read_b64_tr_b16 v[128:129], v232 offset:16384\n"
    "s_add_u32 m0, s81, 0\n"
    "s_nop 0\n"
    "buffer_load_dwordx4 v228, s[40:43], s80 offen offset:0 lds\n"
    "v_mfma_f32_16x16x32_bf16 v[68:71], v[144:147], v[180:183], v[68:71]\n"
    "ds_read_b64_tr_b16 v[130:131], v232 offset:18432\n"
    "buffer_load_dwordx4 v229, s[40:43], s80 offen offset:1024 lds\n"
    "v_mfma_f32_16x16x32_bf16 v[72:75], v[144:147], v[184:187], v[72:75]\n"
    "ds_read_b64_tr_b16 v[132:133], v233 offset:16384\n"
    "buffer_load_dwordx4 v230, s[40:43], s80 offen offset:2048 lds\n"
    "v_mfma_f32_16x16x32_bf16 v[76:79], v[144:147], v[188:191], v[76:79]\n"
    "ds_read_b64_tr_b16 v[134:135], v233 offset:18432\n"
    "buffer_load_dwordx4 v231, s[40:43], s80 offen offset:3072 lds\n"
    "s_add_u32 s80, s80, 32768\n"
    "s_cmp_eq_u32 s80, s99\n"
    "s_cselect_b32 s76, 458752, 0\n"
    "s_sub_u32 s80, s80, s76\n"
    "v_mfma_f32_16x16x32_bf16 v[80:83], v[148:151], v[176:179], v[80:83]\n"
    "ds_read_b64_tr_b16 v[136:137], v234 offset:16384\n"
    "buffer_load_dwordx4 v[160:163], v238, s[48:51], s83 offen sc1\n"
    "v_mfma_f32_16x16x32_bf16 v[84:87], v[148:151], v[180:183], v[84:87]\n"
    "ds_read_b64_tr_b16 v[138:139], v234 offset:18432\n"
    "buffer_load_dwordx4 v[164:167], v239, s[48:51], s83 offen sc1\n"
    "v_mfma_f32_16x16x32_bf16 v[88:91], v[148:151], v[184:187], v[88:91]\n"
    "ds_read_b64_tr_b16 v[140:141], v235 offset:16384\n"
    "buffer_load_dwordx4 v[168:171], v240, s[48:51], s83 offen sc1\n"
    "v_mfma_f32_16x16x32_bf16 v[92:95], v[148:151], v[188:191], v[92:95]\n"
    "ds_read_b64_tr_b16 v[142:143], v235 offset:18432\n"
    "buffer_load_dwordx4 v[172:175], v241, s[48:51], s83 offen sc1\n"
    "s_add_u32 s83, s83, 4096\n"
    "s_cmp_eq_u32 s83, s18\n"
    "s_cselect_b32 s76, 57344, 0\n"
    "s_sub_u32 s83, s83, s76\n"
    "v_mfma_f32_16x16x32_bf16 v[96:99], v[152:155], v[176:179], v[96:99]\n"
    "v_mfma_f32_16x16x32_bf16 v[100:103], v[152:155], v[180:183], v[100:103]\n"
    "v_mfma_f32_16x16x32_bf16 v[104:107], v[152:155], v[184:187], v[104:107]\n"
    "v_mfma_f32_16x16x32_bf16 v[108:111], v[152:155], v[188:191], v[108:111]\n"
    "v_mfma_f32_16x16x32_bf16 v[112:115], v[156:159], v[176:179], v[112:115]\n"
    "v_mfma_f32_16x16x32_bf16 v[116:119], v[156:159], v[180:183], v[116:119]\n"
    "v_mfma_f32_16x16x32_bf16 v[120:123], v[156:159], v[184:187], v[120:123]\n"
    "v_mfma_f32_16x16x32_bf16 v[124:127], v[156:159], v[188:191], v[124:127]\n"
    "s_waitcnt lgkmcnt(0)\n"
    "s_waitcnt vmcnt(12)\n"
    "s_cmp_lg_u32 s90, 0\n"
    "s_cbranch_scc1 .L_nc2_k\n"
    "ds_read_b32 v12, v243 offset:36\n"
    "s_mov_b32 s91, 10\n"
    "s_waitcnt lgkmcnt(0)\n"
    "v_readfirstlane_b32 s94, v12\n"
    "s_or_b32 s94, s94, 0x80808080\n"
    "s_sub_u32 s94, s94, s88\n"
    "s_and_b32 s94, s94, 0x80808080\n"
    "s_cmp_eq_u32 s94, 0x80808080\n"
    "s_cbranch_scc1 .L_red1_k\n"
    ".L_rc2_k:\n"
    "s_setprio 0\n"
    ".L_nc2_k:\n"
    "v_mfma_f32_16x16x32_bf16 v[64:67], v[128:131], v[192:195], v[64:67]\n"
    "ds_read_b64_tr_b16 v[144:145], v232 offset:32768\n"
    "s_add_u32 m0, s81, 16384\n"
    "s_nop 0\n"
    "buffer_load_dwordx4 v228, s[40:43], s80 offen offset:0 lds\n"
    "v_mfma_f32_16x16x32_bf16 v[68:71], v[128:131], v[196:199], v[68:71]\n"
    "ds_read_b64_tr_b16 v[146:147], v232 offset:34816\n"
    "buffer_load_dwordx4 v229, s[40:43], s80 offen offset:1024 lds\n"
    "v_mfma_f32_16x16x32_bf16 v[72:75], v[128:131], v[200:203], v[72:75]\n"
    "ds_read_b64_tr_b16 v[148:149], v233 offset:32768\n"
    "buffer_load_dwordx4 v230, s[40:43], s80 offen offset:2048 lds\n"
    "v_mfma_f32_16x16x32_bf16 v[76:79], v[128:131], v[204:207], v[76:79]\n"
    "ds_read_b64_tr_b16 v[150:151], v233 offset:34816\n"
    "buffer_load_dwordx4 v231, s[40:43], s80 offen offset:3072 lds\n"
    "s_add_u32 s80, s80, 32768\n"
    "s_cmp_eq_u32 s80, s99\n"
    "s_cselect_b32 s76, 458752, 0\n"
    "s_sub_u32 s80, s80, s76\n"
    "v_mfma_f32_16x16x32_bf16 v[80:83], v[132:135], v[192:195], v[80:83]\n"
    "ds_read_b64_tr_b16 v[152:153], v234 offset:32768\n"
    "buffer_load_dwordx4 v[176:179], v238, s[48:51], s83 offen sc1\n"
    "v_mfma_f32_16x16x32_bf16 v[84:87], v[132:135], v[196:199], v[84:87]\n"
    "ds_read_b64_tr_b16 v[154:155], v234 offset:34816\n"
    "buffer_load_dwordx4 v[180:183], v239, s[48:51], s83 offen sc1\n"
    "v_mfma_f32_16x16x32_bf16 v[88:91], v[132:135], v[200:203], v[88:91]\n"
    "ds_read_b64_tr_b16 v[156:157], v235 offset:32768\n"
    "buffer_load_dwordx4 v[184:187], v240, s[48:51], s83 offen sc1\n"
    "v_mfma_f32_16x16x32_bf16 v[92:95], v[132:135], v[204:207], v[92:95]\n"
    "ds_read_b64_tr_b16 v[158:159], v235 offset:34816\n"
    "buffer_load_dwordx4 v[188:191], v241, s[48:51], s83 offen sc1\n"
    "s_add_u32 s83, s83, 4096\n"
    "s_cmp_eq_u32 s83, s18\n"
    "s_cselect_b32 s76, 57344, 0\n"
    "s_sub_u32 s83, s83, s76\n"
    "v_mfma_f32_16x16x32_bf16 v[96:99], v[136:139], v[192:195], v[96:99]\n"
    "v_mfma_f32_16x16x32_bf16 v[100:103], v[136:139], v[196:199], v[100:103]\n"
    "v_mfma_f32_16x16x32_bf16 v[104:107], v[136:139], v[200:203], v[104:107]\n"
    "v_mfma_f32_16x16x32_bf16 v[108:111], v[136:139], v[204:207], v[108:111]\n"
    "v_mfma_f32_16x16x32_bf16 v[112:115], v[140:143], v[192:195], v[112:115]\n"
    "v_mfma_f32_16x16x32_bf16 v[116:119], v[140:143], v[196:199], v[116:119]\n"
    "v_mfma_f32_16x16x32_bf16 v[120:123], v[140:143], v[200:203], v[120:123]\n"
    "v_mfma_f32_16x16x32_bf16 v[124:127], v[140:143], v[204:207], v[124:127]\n"
    "s_waitcnt lgkmcnt(0)\n"
    "s_waitcnt vmcnt(12)\n"
    "v_mfma_f32_16x16x32_bf16 v[64:67], v[144:147], v[208:211], v[64:67]\n"
    "ds_read_b64_tr_b16 v[128:129], v232 offset:0\n"
    "s_add_u32 m0, s81, 32768\n"
    "s_nop 0\n"
    "buffer_load_dwordx4 v228, s[40:43], s80 offen offset:0 lds\n"
    "v_mfma_f32_16x16x32_bf16 v[68:71], v[144:147], v[212:215], v[68:71]\n"
    "ds_read_b64_tr_b16 v[130:131], v232 offset:2048\n"
    "buffer_load_dwordx4 v229, s[40:43], s80 offen offset:1024 lds\n"
    "v_mfma_f32_16x16x32_bf16 v[72:75], v[144:147], v[216:219], v[72:75]\n"
    "ds_read_b64_tr_b16 v[132:133], v233 offset:0\n"
    "buffer_load_dwordx4 v230, s[40:43], s80 offen offset:2048 lds\n"
    "v_mfma_f32_16x16x32_bf16 v[76:79], v[144:147], v[220:223], v[76:79]\n"
    "ds_read_b64_tr_b16 v[134:135], v233 offset:2048\n"
    "buffer_load_dwordx4 v231, s[40:43], s80 offen offset:3072 lds\n"
    "s_add_u32 s80, s80, 32768\n"
    "s_cmp_eq_u32 s80, s99\n"
    "s_cselect_b32 s76, 458752, 0\n"
    "s_sub_u32 s80, s80, s76\n"
    "v_mfma_f32_16x16x32_bf16 v[80:83], v[148:151], v[208:211], v[80:83]\n"
    "ds_read_b64_tr_b16 v[136:137], v234 offset:0\n"
    "buffer_load_dwordx4 v[192:195], v238, s[48:51], s83 offen sc1\n"
    "v_mfma_f32_16x16x32_bf16 v[84:87], v[148:151], v[212:215], v[84:87]\n"
    "ds_read_b64_tr_b16 v[138:139], v234 offset:2048\n"
    "buffer_load_dwordx4 v[196:199], v239, s[48:51], s83 offen sc1\n"
    "v_mfma_f32_16x16x32_bf16 v[88:91], v[148:151], v[216:219], v[88:91]\n"
    "ds_read_b64_tr_b16 v[140:141], v235 offset:0\n"
    "buffer_load_dwordx4 v[200:203], v240, s[48:51], s83 offen sc1\n"
    "v_mfma_f32_16x16x32_bf16 v[92:95], v[148:151], v[220:223], v[92:95]\n"
    "ds_read_b64_tr_b16 v[142:143], v235 offset:2048\n"
    "buffer_load_dwordx4 v[204:207], v241, s[48:51], s83 offen sc1\n"
    "s_add_u32 s83, s83, 4096\n"
    "s_cmp_eq_u32 s83, s18\n"
    "s_cselect_b32 s76, 57344, 0\n"
    "s_sub_u32 s83, s83, s76\n"
    "v_mfma_f32_16x16x32_bf16 v[96:99], v[152:155], v[208:211], v[96:99]\n"
    "v_mfma_f32_16x16x32_bf16 v[100:103], v[152:155], v[212:215], v[100:103]\n"
    "v_mfma_f32_16x16x32_bf16 v[104:107], v[152:155], v[216:219], v[104:107]\n"
    "v_mfma_f32_16x16x32_bf16 v[108:111], v[152:155], v[220:223], v[108:111]\n"
    "v_mfma_f32_16x16x32_bf16 v[112:115], v[156:159], v[208:211], v[112:115]\n"
    "v_mfma_f32_16x16x32_bf16 v[116:119], v[156:159], v[212:215], v[116:119]\n"
    "v_mfma_f32_16x16x32_bf16 v[120:123], v[156:159], v[216:219], v[120:123]\n"
    "v_mfma_f32_16x16x32_bf16 v[124:127], v[156:159], v[220:223], v[124:127]\n"
    "s_waitcnt lgkmcnt(0)\n"
    "s_waitcnt vmcnt(12)\n"
    "s_cmp_lg_u32 s90, 3\n"
    "s_cbranch_scc1 .Lnf_31_k\n"
    "s_mov_b32 s90, 1\n"
    "s_mov_b64 exec, 1\n"
    "buffer_store_dwordx2 v[20:21], v22, s[48:51], s95 offen sc1\n"
    "s_mov_b64 exec, -1\n"
    ".Lnf_31_k:\n"
    "v_mfma_f32_16x16x32_bf16 v[64:67], v[128:131], v[160:163], v[64:67]\n"
    "ds_read_b64_tr_b16 v[144:145], v232 offset:16384\n"
    "s_add_u32 m0, s81, 0\n"
    "s_nop 0\n"
    "buffer_load_dwordx4 v228, s[40:43], s80 offen offset:0 lds\n"
    "v_mfma_f32_16x16x32_bf16 v[68:71], v[128:131], v[164:167], v[68:71]\n"
    "ds_read_b64_tr_b16 v[146:147], v232 offset:18432\n"
    "buffer_load_dwordx4 v229, s[40:43], s80 offen offset:1024 lds\n"
    "v_mfma_f32_16x16x32_bf16 v[72:75], v[128:131], v[168:171], v[72:75]\n"
    "ds_read_b64_tr_b16 v[148:149], v233 offset:16384\n"
    "buffer_load_dwordx4 v230, s[40:43], s80 offen offset:2048 lds\n"
    "v_mfma_f32_16x16x32_bf16 v[76:79], v[128:131], v[172:175], v[76:79]\n"
    "ds_read_b64_tr_b16 v[150:151], v233 offset:18432\n"
    "buffer_load_dwordx4 v231, s[40:43], s80 offen offset:3072 lds\n"
    "s_add_u32 s80, s80, 32768\n"
    "s_cmp_eq_u32 s80, s99\n"
    "s_cselect_b32 s76, 458752, 0\n"
    "s_sub_u32 s80, s80, s76\n"
    "v_mfma_f32_16x16x32_bf16 v[80:83], v[132:135], v[160:163], v[80:83]\n"
    "ds_read_b64_tr_b16 v[152:153], v234 offset:16384\n"
    "buffer_load_dwordx4 v[208:211], v238, s[48:51], s83 offen sc1\n"
    "v_mfma_f32_16x16x32_bf16 v[84:87], v[132:135], v[164:167], v[84:87]\n"
    "ds_read_b64_tr_b16 v[154:155], v234 offset:18432\n"
    "buffer_load_dwordx4 v[212:215], v239, s[48:51], s83 offen sc1\n"
    "v_mfma_f32_16x16x32_bf16 v[88:91], v[132:135], v[168:171], v[88:91]\n"
    "ds_read_b64_tr_b16 v[156:157], v235 offset:16384\n"
    "buffer_load_dwordx4 v[216:219], v240, s[48:51], s83 offen sc1\n"
    "v_mfma_f32_16x16x32_bf16 v[92:95], v[132:135], v[172:175], v[92:95]\n"
    "ds_read_b64_tr_b16 v[158:159], v235 offset:18432\n"
    "buffer_load_dwordx4 v[220:223], v241, s[48:51], s83 offen sc1\n"
    "s_add_u32 s83, s83, 4096\n"
    "s_cmp_eq_u32 s83, s18\n"
    "s_cselect_b32 s76, 57344, 0\n"
    "s_sub_u32 s83, s83, s76\n"
    "v_mfma_f32_16x16x32_bf16 v[96:99], v[136:139], v[160:163], v[96:99]\n"
    "v_mfma_f32_16x16x32_bf16 v[100:103], v[136:139], v[164:167], v[100:103]\n"
    "v_mfma_f32_16x16x32_bf16 v[104:107], v[136:139], v[168:171], v[104:107]\n"
    "v_mfma_f32_16x16x32_bf16 v[108:111], v[136:139], v[172:175], v[108:111]\n"
    "v_mfma_f32_16x16x32_bf16 v[112:115], v[140:143], v[160:163], v[112:115]\n"
    "v_mfma_f32_16x16x32_bf16 v[116:119], v[140:143], v[164:167], v[116:119]\n"
    "v_mfma_f32_16x16x32_bf16 v[120:123], v[140:143], v[168:171], v[120:123]\n"
    "v_mfma_f32_16x16x32_bf16 v[124:127], v[140:143], v[172:175], v[124:127]\n"
    "s_waitcnt lgkmcnt(0)\n"
    "s_waitcnt vmcnt(12)\n"
    "v_mfma_f32_16x16x32_bf16 v[64:67], v[144:147], v[176:179], v[64:67]\n"
    "ds_read_b64_tr_b16 v[128:129], v232 offset:32768\n"
    "s_add_u32 m0, s81, 16384\n"
    "s_nop 0\n"
    "buffer_load_dwordx4 v228, s[40:43], s80 offen offset:0 lds\n"
    "v_mfma_f32_16x16x32_bf16 v[68:71], v[144:147], v[180:183], v[68:71]\n"
    "ds_read_b64_tr_b16 v[130:131], v232 offset:34816\n"
    "buffer_load_dwordx4 v229, s[40:43], s80 offen offset:1024 lds\n"
    "v_mfma_f32_16x16x32_bf16 v[72:75], v[144:147], v[184:187], v[72:75]\n"
    "ds_read_b64_tr_b16 v[132:133], v233 offset:32768\n"
    "buffer_load_dwordx4 v230, s[40:43], s80 offen offset:2048 lds\n"
    "v_mfma_f32_16x16x32_bf16 v[76:79], v[144:147], v[188:191], v[76:79]\n"
    "ds_read_b64_tr_b16 v[134:135], v233 offset:34816\n"
    "buffer_load_dwordx4 v231, s[40:43], s80 offen offset:3072 lds\n"
    "s_add_u32 s80, s80, 32768\n"
    "s_cmp_eq_u32 s80, s99\n"
    "s_cselect_b32 s76, 458752, 0\n"
    "s_sub_u32 s80, s80, s76\n"
    "v_mfma_f32_16x16x32_bf16 v[80:83], v[148:151], v[176:179], v[80:83]\n"
    "ds_read_b64_tr_b16 v[136:137], v234 offset:32768\n"
    "buffer_load_dwordx4 v[160:163], v238, s[48:51], s83 offen sc1\n"
    "v_mfma_f32_16x16x32_bf16 v[84:87], v[148:151], v[180:183], v[84:87]\n"
    "ds_read_b64_tr_b16 v[138:139], v234 offset:34816\n"
    "buffer_load_dwordx4 v[164:167], v239, s[48:51], s83 offen sc1\n"
    "v_mfma_f32_16x16x32_bf16 v[88:91], v[148:151], v[184:187], v[88:91]\n"
    "ds_read_b64_tr_b16 v[140:141], v235 offset:32768\n"
    "buffer_load_dwordx4 v[168:171], v240, s[48:51], s83 offen sc1\n"
    "v_mfma_f32_16x16x32_bf16 v[92:95], v[148:151], v[188:191], v[92:95]\n"
    "ds_read_b64_tr_b16 v[142:143], v235 offset:34816\n"
    "buffer_load_dwordx4 v[172:175], v241, s[48:51], s83 offen sc1\n"
    "s_add_u32 s83, s83, 4096\n"
    "s_cmp_eq_u32 s83, s18\n"
    "s_cselect_b32 s76, 57344, 0\n"
    "s_sub_u32 s83, s83, s76\n"
    "v_mfma_f32_16x16x32_bf16 v[96:99], v[152:155], v[176:179], v[96:99]\n"
    "v_mfma_f32_16x16x32_bf16 v[100:103], v[152:155], v[180:183], v[100:103]\n"
    "v_mfma_f32_16x16x32_bf16 v[104:107], v[152:155], v[184:187], v[104:107]\n"
    "v_mfma_f32_16x16x32_bf16 v[108:111], v[152:155], v[188:191], v[108:111]\n"
    "v_mfma_f32_16x16x32_bf16 v[112:115], v[156:159], v[176:179], v[112:115]\n"
    "v_mfma_f32_16x16x32_bf16 v[116:119], v[156:159], v[180:183], v[116:119]\n"
    "v_mfma_f32_16x16x32_bf16 v[120:123], v[156:159], v[184:187], v[120:123]\n"
    "v_mfma_f32_16x16x32_bf16 v[124:127], v[156:159], v[188:191], v[124:127]\n"
    "s_waitcnt lgkmcnt(0)\n"
    "s_waitcnt vmcnt(12)\n"
    "s_cmp_lg_u32 s90, 0\n"
    "s_cbranch_scc1 .L_nc6_k\n"
    "ds_read_b32 v12, v243 offset:36\n"
    "s_mov_b32 s91, 14\n"
    "s_waitcnt lgkmcnt(0)\n"
    "v_readfirstlane_b32 s94, v12\n"
    "s_or_b32 s94, s94, 0x80808080\n"
    "s_sub_u32 s94, s94, s88\n"
    "s_and_b32 s94, s94, 0x80808080\n"
    "s_cmp_eq_u32 s94, 0x80808080\n"
    "s_cbranch_scc1 .L_red1_k\n"
    ".L_rc6_k:\n"
    "s_setprio 0\n"
    ".L_nc6_k:\n"
    "v_mfma_f32_16x16x32_bf16 v[64:67], v[128:131], v[192:195], v[64:67]\n"
    "ds_read_b64_tr_b16 v[144:145], v232 offset:0\n"
    "s_add_u32 m0, s81, 32768\n"
    "s_nop 0\n"
    "buffer_load_dwordx4 v228, s[40:43], s80 offen offset:0 lds\n"
    "v_mfma_f32_16x16x32_bf16 v[68:71], v[128:131], v[196:199], v[68:71]\n"
    "ds_read_b64_tr_b16 v[146:147], v232 offset:2048\n"
    "buffer_load_dwordx4 v229, s[40:43], s80 offen offset:1024 lds\n"
    "v_mfma_f32_16x16x32_bf16 v[72:75], v[128:131], v[200:203], v[72:75]\n"
    "ds_read_b64_tr_b16 v[148:149], v233 offset:0\n"
    "buffer_load_dwordx4 v230, s[40:43], s80 offen offset:2048 lds\n"
    "v_mfma_f32_16x16x32_bf16 v[76:79], v[128:131], v[204:207], v[76:79]\n"
    "ds_read_b64_tr_b16 v[150:151], v233 offset:2048\n"
    "buffer_load_dwordx4 v231, s[40:43], s80 offen offset:3072 lds\n"
    "s_add_u32 s80, s80, 32768\n"
    "s_cmp_eq_u32 s80, s99\n"
    "s_cselect_b32 s76, 458752, 0\n"
    "s_sub_u32 s80, s80, s76\n"
    "v_mfma_f32_16x16x32_bf16 v[80:83], v[132:135], v[192:195], v[80:83]\n"
    "ds_read_b64_tr_b16 v[152:153], v234 offset:0\n"
    "buffer_load_dwordx4 v[176:179], v238, s[48:51], s83 offen sc1\n"
    "v_mfma_f32_16x16x32_bf16 v[84:87], v[132:135], v[196:199], v[84:87]\n"
    "ds_read_b64_tr_b16 v[154:155], v234 offset:2048\n"
    "buffer_load_dwordx4 v[180:183], v239, s[48:51], s83 offen sc1\n"
    "v_mfma_f32_16x16x32_bf16 v[88:91], v[132:135], v[200:203], v[88:91]\n"
    "ds_read_b64_tr_b16 v[156:157], v235 offset:0\n"
    "buffer_load_dwordx4 v[184:187], v240, s[48:51], s83 offen sc1\n"
    "v_mfma_f32_16x16x32_bf16 v[92:95], v[132:135], v[204:207], v[92:95]\n"
    "ds_read_b64_tr_b16 v[158:159], v235 offset:2048\n"
    "buffer_load_dwordx4 v[188:191], v241, s[48:51], s83 offen sc1\n"
    "s_add_u32 s83, s83, 4096\n"
    "s_cmp_eq_u32 s83, s18\n"
    "s_cselect_b32 s76, 57344, 0\n"
    "s_sub_u32 s83, s83, s76\n"
    "v_mfma_f32_16x16x32_bf16 v[96:99], v[136:139], v[192:195], v[96:99]\n"
    "v_mfma_f32_16x16x32_bf16 v[100:103], v[136:139], v[196:199], v[100:103]\n"
    "v_mfma_f32_16x16x32_bf16 v[104:107], v[136:139], v[200:203], v[104:107]\n"
    "v_mfma_f32_16x16x32_bf16 v[108:111], v[136:139], v[204:207], v[108:111]\n"
    "v_mfma_f32_16x16x32_bf16 v[112:115], v[140:143], v[192:195], v[112:115]\n"
    "v_mfma_f32_16x16x32_bf16 v[116:119], v[140:143], v[196:199], v[116:119]\n"
    "v_mfma_f32_16x16x32_bf16 v[120:123], v[140:143], v[200:203], v[120:123]\n"
    "v_mfma_f32_16x16x32_bf16 v[124:127], v[140:143], v[204:207], v[124:127]\n"
    "s_waitcnt lgkmcnt(0)\n"
    "s_waitcnt vmcnt(12)\n"
    "v_mfma_f32_16x16x32_bf16 v[64:67], v[144:147], v[208:211], v[64:67]\n"
    "ds_read_b64_tr_b16 v[128:129], v232 offset:16384\n"
    "s_add_u32 m0, s81, 0\n"
    "s_nop 0\n"
    "buffer_load_dwordx4 v228, s[40:43], s80 offen offset:0 lds\n"
    "v_mfma_f32_16x16x32_bf16 v[68:71], v[144:147], v[212:215], v[68:71]\n"
    "ds_read_b64_tr_b16 v[130:131], v232 offset:18432\n"
    "buffer_load_dwordx4 v229, s[40:43], s80 offen offset:1024 lds\n"
    "v_mfma_f32_16x16x32_bf16 v[72:75], v[144:147], v[216:219], v[72:75]\n"
    "ds_read_b64_tr_b16 v[132:133], v233 offset:16384\n"
    "buffer_load_dwordx4 v230, s[40:43], s80 offen offset:2048 lds\n"
    "v_mfma_f32_16x16x32_bf16 v[76:79], v[144:147], v[220:223], v[76:79]\n"
    "ds_read_b64_tr_b16 v[134:135], v233 offset:18432\n"
    "buffer_load_dwordx4 v231, s[40:43], s80 offen offset:3072 lds\n"
    "s_add_u32 s80, s80, 32768\n"
    "s_cmp_eq_u32 s80, s99\n"
    "s_cselect_b32 s76, 458752, 0\n"
    "s_sub_u32 s80, s80, s76\n"
    "v_mfma_f32_16x16x32_bf16 v[80:83], v[148:151], v[208:211], v[80:83]\n"
    "ds_read_b64_tr_b16 v[136:137], v234 offset:16384\n"
    "buffer_load_dwordx4 v[192:195], v238, s[48:51], s83 offen sc1\n"
    "v_mfma_f32_16x16x32_bf16 v[84:87], v[148:151], v[212:215], v[84:87]\n"
    "ds_read_b64_tr_b16 v[138:139], v234 offset:18432\n"
    "buffer_load_dwordx4 v[196:199], v239, s[48:51], s83 offen sc1\n"
    "v_mfma_f32_16x16x32_bf16 v[88:91], v[148:151], v[216:219], v[88:91]\n"
    "ds_read_b64_tr_b16 v[140:141], v235 offset:16384\n"
    "buffer_load_dwordx4 v[200:203], v240, s[48:51], s83 offen sc1\n"
    "v_mfma_f32_16x16x32_bf16 v[92:95], v[148:151], v[220:223], v[92:95]\n"
    "ds_read_b64_tr_b16 v[142:143], v235 offset:18432\n"
    "buffer_load_dwordx4 v[204:207], v241, s[48:51], s83 offen sc1\n"
    "s_add_u32 s83, s83, 4096\n"
    "s_cmp_eq_u32 s83, s18\n"
    "s_cselect_b32 s76, 57344, 0\n"
    "s_sub_u32 s83, s83, s76\n"
    "v_mfma_f32_16x16x32_bf16 v[96:99], v[152:155], v[208:211], v[96:99]\n"
    "v_mfma_f32_16x16x32_bf16 v[100:103], v[152:155], v[212:215], v[100:103]\n"
    "v_mfma_f32_16x16x32_bf16 v[104:107], v[152:155], v[216:219], v[104:107]\n"
    "v_mfma_f32_16x16x32_bf16 v[108:111], v[152:155], v[220:223], v[108:111]\n"
    "v_mfma_f32_16x16x32_bf16 v[112:115], v[156:159], v[208:211], v[112:115]\n"
    "v_mfma_f32_16x16x32_bf16 v[116:119], v[156:159], v[212:215], v[116:119]\n"
    "v_mfma_f32_16x16x32_bf16 v[120:123], v[156:159], v[216:219], v[120:123]\n"
    "v_mfma_f32_16x16x32_bf16 v[124:127], v[156:159], v[220:223], v[124:127]\n"
    "s_waitcnt lgkmcnt(0)\n"
    "s_waitcnt vmcnt(12)\n"
    "s_cmp_lg_u32 s90, 3\n"
    "s_cbranch_scc1 .Lnf_32_k\n"
    "s_mov_b32 s90, 1\n"
    "s_mov_b64 exec, 1\n"
    "buffer_store_dwordx2 v[20:21], v22, s[48:51], s95 offen sc1\n"
    "s_mov_b64 exec, -1\n"
    ".Lnf_32_k:\n"
    "v_mfma_f32_16x16x32_bf16 v[64:67], v[128:131], v[160:163], v[64:67]\n"
    "ds_read_b64_tr_b16 v[144:145], v232 offset:32768\n"
    "s_add_u32 m0, s81, 16384\n"
    "s_nop 0\n"
    "buffer_load_dwordx4 v228, s[40:43], s80 offen offset:0 lds\n"
    "v_mfma_f32_16x16x32_bf16 v[68:71], v[128:131], v[164:167], v[68:71]\n"
    "ds_read_b64_tr_b16 v[146:147], v232 offset:34816\n"
    "buffer_load_dwordx4 v229, s[40:43], s80 offen offset:1024 lds\n"
    "v_mfma_f32_16x16x32_bf16 v[72:75], v[128:131], v[168:171], v[72:75]\n"
    "ds_read_b64_tr_b16 v[148:149], v233 offset:32768\n"
    "buffer_load_dwordx4 v230, s[40:43], s80 offen offset:2048 lds\n"
    "v_mfma_f32_16x16x32_bf16 v[76:79], v[128:131], v[172:175], v[76:79]\n"
    "ds_read_b64_tr_b16 v[150:151], v233 offset:34816\n"
    "buffer_load_dwordx4 v231, s[40:43], s80 offen offset:3072 lds\n"
    "s_add_u32 s80, s80, 32768\n"
    "s_cmp_eq_u32 s80, s99\n"
    "s_cselect_b32 s76, 458752, 0\n"
    "s_sub_u32 s80, s80, s76\n"
    "v_mfma_f32_16x16x32_bf16 v[80:83], v[132:135], v[160:163], v[80:83]\n"
    "ds_read_b64_tr_b16 v[152:153], v234 offset:32768\n"
    "buffer_load_dwordx4 v[208:211], v238, s[48:51], s83 offen sc1\n"
    "v_mfma_f32_16x16x32_bf16 v[84:87], v[132:135], v[164:167], v[84:87]\n"
    "ds_read_b64_tr_b16 v[154:155], v234 offset:34816\n"
    "buffer_load_dwordx4 v[212:215], v239, s[48:51], s83 offen sc1\n"
    "v_mfma_f32_16x16x32_bf16 v[88:91], v[132:135], v[168:171], v[88:91]\n"
    "ds_read_b64_tr_b16 v[156:157], v235 offset:32768\n"
    "buffer_load_dwordx4 v[216:219], v240, s[48:51], s83 offen sc1\n"
    "v_mfma_f32_16x16x32_bf16 v[92:95], v[132:135], v[172:175], v[92:95]\n"
    "ds_read_b64_tr_b16 v[158:159], v235 offset:34816\n"
    "buffer_load_dwordx4 v[220:223], v241, s[48:51], s83 offen sc1\n"
    "s_add_u32 s83, s83, 4096\n"
    "s_cmp_eq_u32 s83, s18\n"
    "s_cselect_b32 s76, 57344, 0\n"
    "s_sub_u32 s83, s83, s76\n"
    "v_mfma_f32_16x16x32_bf16 v[96:99], v[136:139], v[160:163], v[96:99]\n"
    "v_mfma_f32_16x16x32_bf16 v[100:103], v[136:139], v[164:167], v[100:103]\n"
    "v_mfma_f32_16x16x32_bf16 v[104:107], v[136:139], v[168:171], v[104:107]\n"
    "v_mfma_f32_16x16x32_bf16 v[108:111], v[136:139], v[172:175], v[108:111]\n"
    "v_mfma_f32_16x16x32_bf16 v[112:115], v[140:143], v[160:163], v[112:115]\n"
    "v_mfma_f32_16x16x32_bf16 v[116:119], v[140:143], v[164:167], v[116:119]\n"
    "v_mfma_f32_16x16x32_bf16 v[120:123], v[140:143], v[168:171], v[120:123]\n"
    "v_mfma_f32_16x16x32_bf16 v[124:127], v[140:143], v[172:175], v[124:127]\n"
    "s_waitcnt lgkmcnt(0)\n"
    "s_waitcnt vmcnt(12)\n"
    "v_mfma_f32_16x16x32_bf16 v[64:67], v[144:147], v[176:179], v[64:67]\n"
    "ds_read_b64_tr_b16 v[128:129], v232 offset:0\n"
    "s_add_u32 m0, s81, 32768\n"
    "s_nop 0\n"
    "buffer_load_dwordx4 v228, s[40:43], s80 offen offset:0 lds\n"
    "v_mfma_f32_16x16x32_bf16 v[68:71], v[144:147], v[180:183], v[68:71]\n"
    "ds_read_b64_tr_b16 v[130:131], v232 offset:2048\n"
    "buffer_load_dwordx4 v229, s[40:43], s80 offen offset:1024 lds\n"
    "v_mfma_f32_16x16x32_bf16 v[72:75], v[144:147], v[184:187], v[72:75]\n"
    "ds_read_b64_tr_b16 v[132:133], v233 offset:0\n"
    "buffer_load_dwordx4 v230, s[40:43], s80 offen offset:2048 lds\n"
    "v_mfma_f32_16x16x32_bf16 v[76:79], v[144:147], v[188:191], v[76:79]\n"
    "ds_read_b64_tr_b16 v[134:135], v233 offset:2048\n"
    "buffer_load_dwordx4 v231, s[40:43], s80 offen offset:3072 lds\n"
    "s_add_u32 s80, s80, 32768\n"
    "s_cmp_eq_u32 s80, s99\n"
    "s_cselect_b32 s76, 458752, 0\n"
    "s_sub_u32 s80, s80, s76\n"
    "v_mfma_f32_16x16x32_bf16 v[80:83], v[148:151], v[176:179], v[80:83]\n"
    "ds_read_b64_tr_b16 v[136:137], v234 offset:0\n"
    "buffer_load_dwordx4 v[160:163], v238, s[48:51], s83 offen sc1\n"
    "v_mfma_f32_16x16x32_bf16 v[84:87], v[148:151], v[180:183], v[84:87]\n"
    "ds_read_b64_tr_b16 v[138:139], v234 offset:2048\n"
    "buffer_load_dwordx4 v[164:167], v239, s[48:51], s83 offen sc1\n"
    "v_mfma_f32_16x16x32_bf16 v[88:91], v[148:151], v[184:187], v[88:91]\n"
    "ds_read_b64_tr_b16 v[140:141], v235 offset:0\n"
    "buffer_load_dwordx4 v[168:171], v240, s[48:51], s83 offen sc1\n"
    "v_mfma_f32_16x16x32_bf16 v[92:95], v[148:151], v[188:191], v[92:95]\n"
    "ds_read_b64_tr_b16 v[142:143], v235 offset:2048\n"
    "buffer_load_dwordx4 v[172:175], v241, s[48:51], s83 offen sc1\n"
    "s_add_u32 s83, s83, 4096\n"
    "s_cmp_eq_u32 s83, s18\n"
    "s_cselect_b32 s76, 57344, 0\n"
    "s_sub_u32 s83, s83, s76\n"
    "v_mfma_f32_16x16x32_bf16 v[96:99], v[152:155], v[176:179], v[96:99]\n"
    "v_mfma_f32_16x16x32_bf16 v[100:103], v[152:155], v[180:183], v[100:103]\n"
    "v_mfma_f32_16x16x32_bf16 v[104:107], v[152:155], v[184:187], v[104:107]\n"
    "v_mfma_f32_16x16x32_bf16 v[108:111], v[152:155], v[188:191], v[108:111]\n"
    "v_mfma_f32_16x16x32_bf16 v[112:115], v[156:159], v[176:179], v[112:115]\n"
    "v_mfma_f32_16x16x32_bf16 v[116:119], v[156:159], v[180:183], v[116:119]\n"
    "v_mfma_f32_16x16x32_bf16 v[120:123], v[156:159], v[184:187], v[120:123]\n"
    "v_mfma_f32_16x16x32_bf16 v[124:127], v[156:159], v[188:191], v[124:127]\n"
    "s_waitcnt lgkmcnt(0)\n"
    "s_waitcnt vmcnt(12)\n"
    "v_mfma_f32_16x16x32_bf16 v[64:67], v[128:131], v[192:195], v[64:67]\n"
    "ds_read_b64_tr_b16 v[144:145], v232 offset:16384\n"
    "s_add_u32 m0, s81, 0\n"
    "s_nop 0\n"
    "buffer_load_dwordx4 v228, s[40:43], s80 offen offset:0 lds\n"
    "v_mfma_f32_16x16x32_bf16 v[68:71], v[128:131], v[196:199], v[68:71]\n"
    "ds_read_b64_tr_b16 v[146:147], v232 offset:18432\n"
    "buffer_load_dwordx4 v229, s[40:43], s80 offen offset:1024 lds\n"
    "v_mfma_f32_16x16x32_bf16 v[72:75], v[128:131], v[200:203], v[72:75]\n"
    "ds_read_b64_tr_b16 v[148:149], v233 offset:16384\n"
    "buffer_load_dwordx4 v230, s[40:43], s80 offen offset:2048 lds\n"
    "v_mfma_f32_16x16x32_bf16 v[76:79], v[128:131], v[204:207], v[76:79]\n"
    "ds_read_b64_tr_b16 v[150:151], v233 offset:18432\n"
    "buffer_load_dwordx4 v231, s[40:43], s80 offen offset:3072 lds\n"
    "s_add_u32 s80, s80, 32768\n"
    "s_cmp_eq_u32 s80, s99\n"
    "s_cselect_b32 s76, 458752, 0\n"
    "s_sub_u32 s80, s80, s76\n"
    "v_mfma_f32_16x16x32_bf16 v[80:83], v[132:135], v[192:195], v[80:83]\n"
    "ds_read_b64_tr_b16 v[152:153], v234 offset:16384\n"
    "buffer_load_dwordx4 v[176:179], v238, s[48:51], s83 offen sc1\n"
    "v_mfma_f32_16x16x32_bf16 v[84:87], v[132:135], v[196:199], v[84:87]\n"
    "ds_read_b64_tr_b16 v[154:155], v234 offset:18432\n"
    "buffer_load_dwordx4 v[180:183], v239, s[48:51], s83 offen sc1\n"
    "v_mfma_f32_16x16x32_bf16 v[88:91], v[132:135], v[200:203], v[88:91]\n"
    "ds_read_b64_tr_b16 v[156:157], v235 offset:16384\n"
    "buffer_load_dwordx4 v[184:187], v240, s[48:51], s83 offen sc1\n"
    "v_mfma_f32_16x16x32_bf16 v[92:95], v[132:135], v[204:207], v[92:95]\n"
    "ds_read_b64_tr_b16 v[158:159], v235 offset:18432\n"
    "buffer_load_dwordx4 v[188:191], v241, s[48:51], s83 offen sc1\n"
    "s_add_u32 s83, s83, 4096\n"
    "s_cmp_eq_u32 s83, s18\n"
    "s_cselect_b32 s76, 57344, 0\n"
    "s_sub_u32 s83, s83, s76\n"
    "v_mfma_f32_16x16x32_bf16 v[96:99], v[136:139], v[192:195], v[96:99]\n"
    "v_mfma_f32_16x16x32_bf16 v[100:103], v[136:139], v[196:199], v[100:103]\n"
    "v_mfma_f32_16x16x32_bf16 v[104:107], v[136:139], v[200:203], v[104:107]\n"
    "v_mfma_f32_16x16x32_bf16 v[108:111], v[136:139], v[204:207], v[108:111]\n"
    "v_mfma_f32_16x16x32_bf16 v[112:115], v[140:143], v[192:195], v[112:115]\n"
    "v_mfma_f32_16x16x32_bf16 v[116:119], v[140:143], v[196:199], v[116:119]\n"
    "v_mfma_f32_16x16x32_bf16 v[120:123], v[140:143], v[200:203], v[120:123]\n"
    "v_mfma_f32_16x16x32_bf16 v[124:127], v[140:143], v[204:207], v[124:127]\n"
    "s_waitcnt lgkmcnt(0)\n"
    "s_waitcnt vmcnt(12)\n"
    "v_mfma_f32_16x16x32_bf16 v[64:67], v[144:147], v[208:211], v[64:67]\n"
    "ds_read_b64_tr_b16 v[128:129], v232 offset:32768\n"
    "buffer_load_dwordx2 v[10:11], v248, s[48:51], s87 offen sc1\n"
    "v_mfma_f32_16x16x32_bf16 v[68:71], v[144:147], v[212:215], v[68:71]\n"
    "ds_read_b64_tr_b16 v[130:131], v232 offset:34816\n"
    "v_mfma_f32_16x16x32_bf16 v[72:75], v[144:147], v[216:219], v[72:75]\n"
    "ds_read_b64_tr_b16 v[132:133], v233 offset:32768\n"
    "v_mfma_f32_16x16x32_bf16 v[76:79], v[144:147], v[220:223], v[76:79]\n"
    "ds_read_b64_tr_b16 v[134:135], v233 offset:34816\n"
    "v_mfma_f32_16x16x32_bf16 v[80:83], v[148:151], v[208:211], v[80:83]\n"
    "ds_read_b64_tr_b16 v[136:137], v234 offset:32768\n"
    "v_mfma_f32_16x16x32_bf16 v[84:87], v[148:151], v[212:215], v[84:87]\n"
    "ds_read_b64_tr_b16 v[138:139], v234 offset:34816\n"
    "v_mfma_f32_16x16x32_bf16 v[88:91], v[148:151], v[216:219], v[88:91]\n"
    "ds_read_b64_tr_b16 v[140:141], v235 offset:32768\n"
    "v_mfma_f32_16x16x32_bf16 v[92:95], v[148:151], v[220:223], v[92:95]\n"
    "ds_read_b64_tr_b16 v[142:143], v235 offset:34816\n"
    "v_mfma_f32_16x16x32_bf16 v[96:99], v[152:155], v[208:211], v[96:99]\n"
    "v_mfma_f32_16x16x32_bf16 v[100:103], v[152:155], v[212:215], v[100:103]\n"
    "v_mfma_f32_16x16x32_bf16 v[104:107], v[152:155], v[216:219], v[104:107]\n"
    "v_mfma_f32_16x16x32_bf16 v[108:111], v[152:155], v[220:223], v[108:111]\n"
    "v_mfma_f32_16x16x32_bf16 v[112:115], v[156:159], v[208:211], v[112:115]\n"
    "v_mfma_f32_16x16x32_bf16 v[116:119], v[156:159], v[212:215], v[116:119]\n"
    "v_mfma_f32_16x16x32_bf16 v[120:123], v[156:159], v[216:219], v[120:123]\n"
    "v_mfma_f32_16x16x32_bf16 v[124:127], v[156:159], v[220:223], v[124:127]\n"
    "s_waitcnt lgkmcnt(0)\n"
    "s_waitcnt vmcnt(5)\n"
    "v_mfma_f32_16x16x32_bf16 v[64:67], v[128:131], v[160:163], v[64:67]\n"
    "ds_read_b64_tr_b16 v[144:145], v232 offset:0\n"
    "v_mfma_f32_16x16x32_bf16 v[68:71], v[128:131], v[164:167], v[68:71]\n"
    "ds_read_b64_tr_b16 v[146:147], v232 offset:2048\n"
    "v_mfma_f32_16x16x32_bf16 v[72:75], v[128:131], v[168:171], v[72:75]\n"
    "ds_read_b64_tr_b16 v[148:149], v233 offset:0\n"
    "v_mfma_f32_16x16x32_bf16 v[76:79], v[128:131], v[172:175], v[76:79]\n"
    "ds_read_b64_tr_b16 v[150:151], v233 offset:2048\n"
    "v_mfma_f32_16x16x32_bf16 v[80:83], v[132:135], v[160:163], v[80:83]\n"
    "ds_read_b64_tr_b16 v[152:153], v234 offset:0\n"
    "v_mfma_f32_16x16x32_bf16 v[84:87], v[132:135], v[164:167], v[84:87]\n"
    "ds_read_b64_tr_b16 v[154:155], v234 offset:2048\n"
    "v_mfma_f32_16x16x32_bf16 v[88:91], v[132:135], v[168:171], v[88:91]\n"
    "ds_read_b64_tr_b16 v[156:157], v235 offset:0\n"
    "v_mfma_f32_16x16x32_bf16 v[92:95], v[132:135], v[172:175], v[92:95]\n"
    "ds_read_b64_tr_b16 v[158:159], v235 offset:2048\n"
    "v_mfma_f32_16x16x32_bf16 v[96:99], v[136:139], v[160:163], v[96:99]\n"
    "v_mfma_f32_16x16x32_bf16 v[100:103], v[136:139], v[164:167], v[100:103]\n"
    "v_mfma_f32_16x16x32_bf16 v[104:107], v[136:139], v[168:171], v[104:107]\n"
    "v_mfma_f32_16x16x32_bf16 v[108:111], v[136:139], v[172:175], v[108:111]\n"
    "v_mfma_f32_16x16x32_bf16 v[112:115], v[140:143], v[160:163], v[112:115]\n"
    "v_mfma_f32_16x16x32_bf16 v[116:119], v[140:143], v[164:167], v[116:119]\n"
    "v_mfma_f32_16x16x32_bf16 v[120:123], v[140:143], v[168:171], v[120:123]\n"
    "v_mfma_f32_16x16x32_bf16 v[124:127], v[140:143], v[172:175], v[124:127]\n"
    "s_waitcnt lgkmcnt(0)\n"
    "s_waitcnt vmcnt(1)\n"
    "v_mfma_f32_16x16x32_bf16 v[64:67], v[144:147], v[176:179], v[64:67]\n"
    "v_mfma_f32_16x16x32_bf16 v[68:71], v[144:147], v[180:183], v[68:71]\n"
    "v_mfma_f32_16x16x32_bf16 v[72:75], v[144:147], v[184:187], v[72:75]\n"
    "v_mfma_f32_16x16x32_bf16 v[76:79], v[144:147], v[188:191], v[76:79]\n"
    "v_mfma_f32_16x16x32_bf16 v[80:83], v[148:151], v[176:179], v[80:83]\n"
    "v_mfma_f32_16x16x32_bf16 v[84:87], v[148:151], v[180:183], v[84:87]\n"
    "v_mfma_f32_16x16x32_bf16 v[88:91], v[148:151], v[184:187], v[88:91]\n"
    "v_mfma_f32_16x16x32_bf16 v[92:95], v[148:151], v[188:191], v[92:95]\n"
    "v_mfma_f32_16x16x32_bf16 v[96:99], v[152:155], v[176:179], v[96:99]\n"
    "v_mfma_f32_16x16x32_bf16 v[100:103], v[152:155], v[180:183], v[100:103]\n"
    "v_mfma_f32_16x16x32_bf16 v[104:107], v[152:155], v[184:187], v[104:107]\n"
    "v_mfma_f32_16x16x32_bf16 v[108:111], v[152:155], v[188:191], v[108:111]\n"
    "v_mfma_f32_16x16x32_bf16 v[112:115], v[156:159], v[176:179], v[112:115]\n"
    "v_mfma_f32_16x16x32_bf16 v[116:119], v[156:159], v[180:183], v[116:119]\n"
    "v_mfma_f32_16x16x32_bf16 v[120:123], v[156:159], v[184:187], v[120:123]\n"
    "v_mfma_f32_16x16x32_bf16 v[124:127], v[156:159], v[188:191], v[124:127]\n"
    "s_setprio 2\n"
    "v_add_u32 v8, s81, v244\n"
    "ds_write_b128 v8, v[68:71] offset:32768\n"
    "ds_write_b128 v8, v[84:87] offset:33792\n"
    "ds_write_b128 v8, v[100:103] offset:34816\n"
    "s_nop 0\n"
    "ds_write_b128 v8, v[116:119] offset:35840\n"
    "ds_write_b128 v8, v[72:75] offset:0\n"
    "ds_write_b128 v8, v[88:91] offset:1024\n"
    "ds_write_b128 v8, v[104:107] offset:2048\n"
    "ds_write_b128 v8, v[120:123] offset:3072\n"
    "ds_write_b128 v8, v[76:79] offset:16384\n"
    "ds_write_b128 v8, v[92:95] offset:17408\n"
    "ds_write_b128 v8, v[108:111] offset:18432\n"
    "ds_write_b128 v8, v[124:127] offset:19456\n"
    "v_mov_b32 v9, 1\n"
    "ds_write_b8 v249, v9 offset:44\n"
    "s_sub_u32 s76, s75, 1\n"
    "s_and_b32 s76, s76, 3\n"
    "s_lshl_b32 s76, s76, 12\n"
    "s_add_u32 s76, s76, 139008\n"
    "v_add_u32 v253, s76, v244\n"
    "s_sub_u32 s76, s75, 2\n"
    "s_and_b32 s76, s76, 3\n"
    "s_lshl_b32 s76, s76, 12\n"
    "s_add_u32 s76, s76, 106240\n"
    "v_add_u32 v254, s76, v244\n"
    "s_sub_u32 s76, s75, 3\n"
    "s_and_b32 s76, s76, 3\n"
    "s_lshl_b32 s76, s76, 12\n"
    "s_add_u32 s76, s76, 122624\n"
    "v_add_u32 v255, s76, v244\n"
    "v_subrev_u32 v232, s81, v232\n"
    "v_subrev_u32 v233, s81, v233\n"
    "v_subrev_u32 v234, s81, v234\n"
    "v_subrev_u32 v235, s81, v235\n"
    "s_waitcnt vmcnt(0)\n"
    "v_cmp_eq_u64 s[12:13], s[70:71], v[10:11]\n"
    "s_cmp_eq_u32 s90, 1\n"
    "s_cbranch_scc1 .L_ret1_k\n"
    "s_mov_b32 s96, 131072\n"
    "s_setprio 0\n"
    ".Lsp_33_k:\n"
    "ds_read_b32 v12, v243 offset:36\n"
    "s_waitcnt lgkmcnt(0)\n"
    "v_readfirstlane_b32 s94, v12\n"
    "s_or_b32 s94, s94, 0x80808080\n"
    "s_sub_u32 s94, s94, s88\n"
    "s_and_b32 s94, s94, 0x80808080\n"
    "s_cmp_eq_u32 s94, 0x80808080\n"
    "s_cbranch_scc1 .Lspd_34_k\n"
    "s_sleep 1\n"
    "s_sub_u32 s96, s96, 1\n"
    "s_cbranch_scc0 .Lsp_33_k\n"
    ".Lspd_34_k:\n"
    "s_mov_b32 s91, 1\n"
    "s_branch .L_red1_k\n"
    ".L_ret1_k:\n"
    "s_add_u32 s88, s88, 0x04040404\n"
    "s_add_u32 s17, s86, 8192\n"
    "s_add_u32 s16, s87, 256\n"
    "s_mov_b32 s90, 0\n"
    "v_mov_b64 v[32:33], v[64:65]\n"
    "v_mov_b64 v[34:35], v[66:67]\n"
    "v_mov_b64 v[36:37], v[80:81]\n"
    "v_mov_b64 v[38:39], v[82:83]\n"
    "v_mov_b64 v[40:41], v[96:97]\n"
    "v_mov_b64 v[42:43], v[98:99]\n"
    "v_mov_b64 v[44:45], v[112:113]\n"
    "v_mov_b64 v[46:47], v[114:115]\n"
    "s_cmp_lg_u32 s90, 0\n"
    "s_cbranch_scc1 .L_ret4_k\n"
    "ds_read_b32 v12, v243 offset:36\n"
    "s_mov_b32 s91, 4\n"
    "s_waitcnt lgkmcnt(0)\n"
    "v_readfirstlane_b32 s94, v12\n"
    "s_or_b32 s94, s94, 0x80808080\n"
    "s_sub_u32 s94, s94, s88\n"
    "s_and_b32 s94, s94, 0x80808080\n"
    "s_cmp_eq_u32 s94, 0x80808080\n"
    "s_cbranch_scc1 .L_red2_k\n"
    ".L_ret4_k:\n"
    "s_mov_b32 s96, 131072\n"
    ".Lsp_35_k:\n"
    "ds_read_b32 v12, v243 offset:48\n"
    "s_waitcnt lgkmcnt(0)\n"
    "v_readfirstlane_b32 s94, v12\n"
    "s_cmp_eq_u32 s94, 0x1010101\n"
    "s_cbranch_scc1 .Lspd_36_k\n"
    "s_sleep 1\n"
    "s_sub_u32 s96, s96, 1\n"
    "s_cbranch_scc0 .Lsp_35_k\n"
    ".Lspd_36_k:\n"
    "s_cmp_eq_u64 s[12:13], -1\n"
    "s_cbranch_scc1 .L_of0_k\n"
    "s_mov_b32 s96, 8192\n"
    "s_setprio 0\n"
    ".Lsp_37_k:\n"
    "buffer_load_dwordx2 v[12:13], v248, s[48:51], s87 offen sc1\n"
    "s_waitcnt vmcnt(0)\n"
    "v_cmp_eq_u64 vcc, s[70:71], v[12:13]\n"
    "s_cmp_eq_u64 vcc, -1\n"
    "s_cbranch_scc1 .Lspd_38_k\n"
    "s_sleep 1\n"
    "s_sub_u32 s96, s96, 1\n"
    "s_cbranch_scc0 .Lsp_37_k\n"
    ".Lspd_38_k:\n"
    ".L_of0_k:\n"
    "s_setprio 2\n"
    "s_add_u32 s76, s86, 4096\n"
    "buffer_load_dwordx4 v[192:195], v244, s[48:51], s86 offen offset:0 sc1\n"
    "buffer_load_dwordx4 v[196:199], v244, s[48:51], s86 offen offset:1024 sc1\n"
    "buffer_load_dwordx4 v[200:203], v244, s[48:51], s86 offen offset:2048 sc1\n"
    "buffer_load_dwordx4 v[204:207], v244, s[48:51], s86 offen offset:3072 sc1\n"
    "buffer_load_dwordx4 v[208:211], v244, s[48:51], s76 offen offset:0 sc1\n"
    "buffer_load_dwordx4 v[212:215], v244, s[48:51], s76 offen offset:1024 sc1\n"
    "buffer_load_dwordx4 v[216:219], v244, s[48:51], s76 offen offset:2048 sc1\n"
    "buffer_load_dwordx4 v[220:223], v244, s[48:51], s76 offen offset:3072 sc1\n"
    "ds_read_b64_tr_b16 v[64:65], v232 offset:0\n"
    "ds_read_b64_tr_b16 v[66:67], v232 offset:2048\n"
    "ds_read_b64_tr_b16 v[68:69], v233 offset:0\n"
    "ds_read_b64_tr_b16 v[70:71], v233 offset:2048\n"
    "ds_read_b64_tr_b16 v[72:73], v234 offset:0\n"
    "ds_read_b64_tr_b16 v[74:75], v234 offset:2048\n"
    "ds_read_b64_tr_b16 v[76:77], v235 offset:0\n"
    "ds_read_b64_tr_b16 v[78:79], v235 offset:2048\n"
    "ds_read_b64_tr_b16 v[80:81], v232 offset:8192\n"
    "ds_read_b64_tr_b16 v[82:83], v232 offset:10240\n"
    "ds_read_b64_tr_b16 v[84:85], v233 offset:8192\n"
    "ds_read_b64_tr_b16 v[86:87], v233 offset:10240\n"
    "ds_read_b64_tr_b16 v[88:89], v234 offset:8192\n"
    "ds_read_b64_tr_b16 v[90:91], v234 offset:10240\n"
    "ds_read_b64_tr_b16 v[92:93], v235 offset:8192\n"
    "ds_read_b64_tr_b16 v[94:95], v235 offset:10240\n"
    "ds_read_b64_tr_b16 v[96:97], v232 offset:16384\n"
    "ds_read_b64_tr_b16 v[98:99], v232 offset:18432\n"
    "ds_read_b64_tr_b16 v[100:101], v233 offset:16384\n"
    "ds_read_b64_tr_b16 v[102:103], v233 offset:18432\n"
    "ds_read_b64_tr_b16 v[104:105], v234 offset:16384\n"
    "ds_read_b64_tr_b16 v[106:107], v234 offset:18432\n"
    "ds_read_b64_tr_b16 v[108:109], v235 offset:16384\n"
    "ds_read_b64_tr_b16 v[110:111], v235 offset:18432\n"
    "ds_read_b64_tr_b16 v[112:113], v232 offset:24576\n"
    "ds_read_b64_tr_b16 v[114:115], v232 offset:26624\n"
    "ds_read_b64_tr_b16 v[116:117], v233 offset:24576\n"
    "ds_read_b64_tr_b16 v[118:119], v233 offset:26624\n"
    "ds_read_b64_tr_b16 v[120:121], v234 offset:24576\n"
    "ds_read_b64_tr_b16 v[122:123], v234 offset:26624\n"
    "ds_read_b64_tr_b16 v[124:125], v235 offset:24576\n"
    "ds_read_b64_tr_b16 v[126:127], v235 offset:26624\n"
    "ds_read_b64_tr_b16 v[128:129], v232 offset:32768\n"
    "ds_read_b64_tr_b16 v[130:131], v232 offset:34816\n"
    "ds_read_b64_tr_b16 v[132:133], v233 offset:32768\n"
    "ds_read_b64_tr_b16 v[134:135], v233 offset:34816\n"
    "ds_read_b64_tr_b16 v[136:137], v234 offset:32768\n"
    "ds_read_b64_tr_b16 v[138:139], v234 offset:34816\n"
    "ds_read_b64_tr_b16 v[140:141], v235 offset:32768\n"
    "ds_read_b64_tr_b16 v[142:143], v235 offset:34816\n"
    "ds_read_b64_tr_b16 v[144:145], v232 offset:40960\n"
    "ds_read_b64_tr_b16 v[146:147], v232 offset:43008\n"
    "ds_read_b64_tr_b16 v[148:149], v233 offset:40960\n"
    "ds_read_b64_tr_b16 v[150:151], v233 offset:43008\n"
    "ds_read_b64_tr_b16 v[152:153], v234 offset:40960\n"
    "ds_read_b64_tr_b16 v[154:155], v234 offset:43008\n"
    "ds_read_b64_tr_b16 v[156:157], v235 offset:40960\n"
    "ds_read_b64_tr_b16 v[158:159], v235 offset:43008\n"
    "ds_read_b64_tr_b16 v[160:161], v232 offset:49152\n"
    "ds_read_b64_tr_b16 v[162:163], v232 offset:51200\n"
    "ds_read_b64_tr_b16 v[164:165], v233 offset:49152\n"
    "ds_read_b64_tr_b16 v[166:167], v233 offset:51200\n"
    "ds_read_b64_tr_b16 v[168:169], v234 offset:49152\n"
    "ds_read_b64_tr_b16 v[170:171], v234 offset:51200\n"
    "ds_read_b64_tr_b16 v[172:173], v235 offset:49152\n"
    "ds_read_b64_tr_b16 v[174:175], v235 offset:51200\n"
    "ds_read_b64_tr_b16 v[176:177], v232 offset:57344\n"
    "ds_read_b64_tr_b16 v[178:179], v232 offset:59392\n"
    "ds_read_b64_tr_b16 v[180:181], v233 offset:57344\n"
    "ds_read_b64_tr_b16 v[182:183], v233 offset:59392\n"
    "ds_read_b64_tr_b16 v[184:185], v234 offset:57344\n"
    "ds_read_b64_tr_b16 v[186:187], v234 offset:59392\n"
    "ds_read_b64_tr_b16 v[188:189], v235 offset:57344\n"
    "ds_read_b64_tr_b16 v[190:191], v235 offset:59392\n"
    "s_waitcnt lgkmcnt(0)\n"
    "s_setprio 0\n"
    "s_waitcnt vmcnt(7)\n"
    "v_mfma_f32_16x16x32_bf16 v[48:51], v[64:67], v[192:195], 0\n"
    "v_mfma_f32_16x16x32_bf16 v[52:55], v[68:71], v[192:195], 0\n"
    "v_mfma_f32_16x16x32_bf16 v[56:59], v[72:75], v[192:195], 0\n"
    "v_mfma_f32_16x16x32_bf16 v[60:63], v[76:79], v[192:195], 0\n"
    "s_waitcnt vmcnt(6)\n"
    "v_mfma_f32_16x16x32_bf16 v[48:51], v[80:83], v[196:199], v[48:51]\n"
    "v_mfma_f32_16x16x32_bf16 v[52:55], v[84:87], v[196:199], v[52:55]\n"
    "v_mfma_f32_16x16x32_bf16 v[56:59], v[88:91], v[196:199], v[56:59]\n"
    "v_mfma_f32_16x16x32_bf16 v[60:63], v[92:95], v[196:199], v[60:63]\n"
    "s_waitcnt vmcnt(5)\n"
    "v_mfma_f32_16x16x32_bf16 v[48:51], v[96:99], v[200:203], v[48:51]\n"
    "v_mfma_f32_16x16x32_bf16 v[52:55], v[100:103], v[200:203], v[52:55]\n"
    "v_mfma_f32_16x16x32_bf16 v[56:59], v[104:107], v[200:203], v[56:59]\n"
    "v_mfma_f32_16x16x32_bf16 v[60:63], v[108:111], v[200:203], v[60:63]\n"
    "s_waitcnt vmcnt(4)\n"
    "v_mfma_f32_16x16x32_bf16 v[48:51], v[112:115], v[204:207], v[48:51]\n"
    "v_mfma_f32_16x16x32_bf16 v[52:55], v[116:119], v[204:207], v[52:55]\n"
    "v_mfma_f32_16x16x32_bf16 v[56:59], v[120:123], v[204:207], v[56:59]\n"
    "v_mfma_f32_16x16x32_bf16 v[60:63], v[124:127], v[204:207], v[60:63]\n"
    "s_waitcnt vmcnt(3)\n"
    "v_mfma_f32_16x16x32_bf16 v[48:51], v[128:131], v[208:211], v[48:51]\n"
    "v_mfma_f32_16x16x32_bf16 v[52:55], v[132:135], v[208:211], v[52:55]\n"
    "v_mfma_f32_16x16x32_bf16 v[56:59], v[136:139], v[208:211], v[56:59]\n"
    "v_mfma_f32_16x16x32_bf16 v[60:63], v[140:143], v[208:211], v[60:63]\n"
    "s_waitcnt vmcnt(2)\n"
    "v_mfma_f32_16x16x32_bf16 v[48:51], v[144:147], v[212:215], v[48:51]\n"
    "v_mfma_f32_16x16x32_bf16 v[52:55], v[148:151], v[212:215], v[52:55]\n"
    "v_mfma_f32_16x16x32_bf16 v[56:59], v[152:155], v[212:215], v[56:59]\n"
    "v_mfma_f32_16x16x32_bf16 v[60:63], v[156:159], v[212:215], v[60:63]\n"
    "s_waitcnt vmcnt(1)\n"
    "v_mfma_f32_16x16x32_bf16 v[48:51], v[160:163], v[216:219], v[48:51]\n"
    "v_mfma_f32_16x16x32_bf16 v[52:55], v[164:167], v[216:219], v[52:55]\n"
    "v_mfma_f32_16x16x32_bf16 v[56:59], v[168:171], v[216:219], v[56:59]\n"
    "v_mfma_f32_16x16x32_bf16 v[60:63], v[172:175], v[216:219], v[60:63]\n"
    "s_waitcnt vmcnt(0)\n"
    "v_mfma_f32_16x16x32_bf16 v[48:51], v[176:179], v[220:223], v[48:51]\n"
    "v_mfma_f32_16x16x32_bf16 v[52:55], v[180:183], v[220:223], v[52:55]\n"
    "v_mfma_f32_16x16x32_bf16 v[56:59], v[184:187], v[220:223], v[56:59]\n"
    "v_mfma_f32_16x16x32_bf16 v[60:63], v[188:191], v[220:223], v[60:63]\n"
    "s_mov_b32 s86, s17\n"
    "s_mov_b32 s87, s16\n"
    "s_cmp_lg_u32 s90, 0\n"
    "s_cbranch_scc1 .L_ret5_k\n"
    "ds_read_b32 v12, v243 offset:36\n"
    "s_mov_b32 s91, 5\n"
    "s_waitcnt lgkmcnt(0)\n"
    "v_readfirstlane_b32 s94, v12\n"
    "s_or_b32 s94, s94, 0x80808080\n"
    "s_sub_u32 s94, s94, s88\n"
    "s_and_b32 s94, s94, 0x80808080\n"
    "s_cmp_eq_u32 s94, 0x80808080\n"
    "s_cbranch_scc1 .L_red2_k\n"
    ".L_ret5_k:\n"
    "s_mov_b32 s96, 131072\n"
    "s_setprio 0\n"
    ".Lsp_39_k:\n"
    "ds_read_b32 v12, v243 offset:44\n"
    "s_waitcnt lgkmcnt(0)\n"
    "v_readfirstlane_b32 s94, v12\n"
    "s_cmp_eq_u32 s94, 0x1010101\n"
    "s_cbranch_scc1 .Lspd_40_k\n"
    "s_sleep 1\n"
    "s_sub_u32 s96, s96, 1\n"
    "s_cbranch_scc0 .Lsp_39_k\n"
    ".Lspd_40_k:\n"
    "s_setprio 2\n"
    "ds_read_b128 v[192:195], v253 offset:0\n"
    "ds_read_b128 v[196:199], v253 offset:1024\n"
    "ds_read_b128 v[200:203], v253 offset:2048\n"
    "ds_read_b128 v[204:207], v253 offset:3072\n"
    "ds_read_b128 v[208:211], v254 offset:0\n"
    "ds_read_b128 v[212:215], v254 offset:1024\n"
    "ds_read_b128 v[216:219], v254 offset:2048\n"
    "ds_read_b128 v[220:223], v254 offset:3072\n"
    "ds_read_b128 v[144:147], v255 offset:0\n"
    "ds_read_b128 v[148:151], v255 offset:1024\n"
    "ds_read_b128 v[152:155], v255 offset:2048\n"
    "ds_read_b128 v[156:159], v255 offset:3072\n"
    "s_waitcnt lgkmcnt(8)\n"
    "v_pk_add_f32 v[32:33], v[32:33], v[192:193]\n"
    "v_pk_add_f32 v[34:35], v[34:35], v[194:195]\n"
    "v_pk_add_f32 v[36:37], v[36:37], v[196:197]\n"
    "v_pk_add_f32 v[38:39], v[38:39], v[198:199]\n"
    "v_pk_add_f32 v[40:41], v[40:41], v[200:201]\n"
    "v_pk_add_f32 v[42:43], v[42:43], v[202:203]\n"
    "v_pk_add_f32 v[44:45], v[44:45], v[204:205]\n"
    "v_pk_add_f32 v[46:47], v[46:47], v[206:207]\n"
    "s_waitcnt lgkmcnt(4)\n"
    "v_pk_add_f32 v[32:33], v[32:33], v[208:209]\n"
    "v_pk_add_f32 v[34:35], v[34:35], v[210:211]\n"
    "v_pk_add_f32 v[36:37], v[36:37], v[212:213]\n"
    "v_pk_add_f32 v[38:39], v[38:39], v[214:215]\n"
    "v_pk_add_f32 v[40:41], v[40:41], v[216:217]\n"
    "v_pk_add_f32 v[42:43], v[42:43], v[218:219]\n"
    "v_pk_add_f32 v[44:45], v[44:45], v[220:221]\n"
    "v_pk_add_f32 v[46:47], v[46:47], v[222:223]\n"
    "s_waitcnt lgkmcnt(0)\n"
    "v_pk_add_f32 v[32:33], v[32:33], v[144:145]\n"
    "v_pk_add_f32 v[34:35], v[34:35], v[146:147]\n"
    "v_pk_add_f32 v[36:37], v[36:37], v[148:149]\n"
    "v_pk_add_f32 v[38:39], v[38:39], v[150:151]\n"
    "v_pk_add_f32 v[40:41], v[40:41], v[152:153]\n"
    "v_pk_add_f32 v[42:43], v[42:43], v[154:155]\n"
    "v_pk_add_f32 v[44:45], v[44:45], v[156:157]\n"
    "v_pk_add_f32 v[46:47], v[46:47], v[158:159]\n"
    "s_cmp_lg_u32 s90, 0\n"
    "s_cbranch_scc1 .L_ret6_k\n"
    "ds_read_b32 v12, v243 offset:36\n"
    "s_mov_b32 s91, 6\n"
    "s_waitcnt lgkmcnt(0)\n"
    "v_readfirstlane_b32 s94, v12\n"
    "s_or_b32 s94, s94, 0x80808080\n"
    "s_sub_u32 s94, s94, s88\n"
    "s_and_b32 s94, s94, 0x80808080\n"
    "s_cmp_eq_u32 s94, 0x80808080\n"
    "s_cbranch_scc1 .L_red2_k\n"
    ".L_ret6_k:\n"
    "ds_read_b64_tr_b16 v[64:65], v232 offset:4096\n"
    "ds_read_b64_tr_b16 v[66:67], v232 offset:6144\n"
    "ds_read_b64_tr_b16 v[68:69], v233 offset:4096\n"
    "ds_read_b64_tr_b16 v[70:71], v233 offset:6144\n"
    "ds_read_b64_tr_b16 v[72:73], v234 offset:4096\n"
    "ds_read_b64_tr_b16 v[74:75], v234 offset:6144\n"
    "ds_read_b64_tr_b16 v[76:77], v235 offset:4096\n"
    "ds_read_b64_tr_b16 v[78:79], v235 offset:6144\n"
    "ds_read_b64_tr_b16 v[80:81], v232 offset:12288\n"
    "ds_read_b64_tr_b16 v[82:83], v232 offset:14336\n"
    "ds_read_b64_tr_b16 v[84:85], v233 offset:12288\n"
    "ds_read_b64_tr_b16 v[86:87], v233 offset:14336\n"
    "ds_read_b64_tr_b16 v[88:89], v234 offset:12288\n"
    "ds_read_b64_tr_b16 v[90:91], v234 offset:14336\n"
    "ds_read_b64_tr_b16 v[92:93], v235 offset:12288\n"
    "ds_read_b64_tr_b16 v[94:95], v235 offset:14336\n"
    "ds_read_b64_tr_b16 v[96:97], v232 offset:20480\n"
    "ds_read_b64_tr_b16 v[98:99], v232 offset:22528\n"
    "ds_read_b64_tr_b16 v[100:101], v233 offset:20480\n"
    "ds_read_b64_tr_b16 v[102:103], v233 offset:22528\n"
    "ds_read_b64_tr_b16 v[104:105], v234 offset:20480\n"
    "ds_read_b64_tr_b16 v[106:107], v234 offset:22528\n"
    "ds_read_b64_tr_b16 v[108:109], v235 offset:20480\n"
    "ds_read_b64_tr_b16 v[110:111], v235 offset:22528\n"
    "ds_read_b64_tr_b16 v[112:113], v232 offset:28672\n"
    "ds_read_b64_tr_b16 v[114:115], v232 offset:30720\n"
    "ds_read_b64_tr_b16 v[116:117], v233 offset:28672\n"
    "ds_read_b64_tr_b16 v[118:119], v233 offset:30720\n"
    "ds_read_b64_tr_b16 v[120:121], v234 offset:28672\n"
    "ds_read_b64_tr_b16 v[122:123], v234 offset:30720\n"
    "ds_read_b64_tr_b16 v[124:125], v235 offset:28672\n"
    "ds_read_b64_tr_b16 v[126:127], v235 offset:30720\n"
    "ds_read_b64_tr_b16 v[128:129], v232 offset:36864\n"
    "ds_read_b64_tr_b16 v[130:131], v232 offset:38912\n"
    "ds_read_b64_tr_b16 v[132:133], v233 offset:36864\n"
    "ds_read_b64_tr_b16 v[134:135], v233 offset:38912\n"
    "ds_read_b64_tr_b16 v[136:137], v234 offset:36864\n"
    "ds_read_b64_tr_b16 v[138:139], v234 offset:38912\n"
    "ds_read_b64_tr_b16 v[140:141], v235 offset:36864\n"
    "ds_read_b64_tr_b16 v[142:143], v235 offset:38912\n"
    "ds_read_b64_tr_b16 v[144:145], v232 offset:45056\n"
    "ds_read_b64_tr_b16 v[146:147], v232 offset:47104\n"
    "ds_read_b64_tr_b16 v[148:149], v233 offset:45056\n"
    "ds_read_b64_tr_b16 v[150:151], v233 offset:47104\n"
    "ds_read_b64_tr_b16 v[152:153], v234 offset:45056\n"
    "ds_read_b64_tr_b16 v[154:155], v234 offset:47104\n"
    "ds_read_b64_tr_b16 v[156:157], v235 offset:45056\n"
    "ds_read_b64_tr_b16 v[158:159], v235 offset:47104\n"
    "ds_read_b64_tr_b16 v[160:161], v232 offset:53248\n"
    "ds_read_b64_tr_b16 v[162:163], v232 offset:55296\n"
    "ds_read_b64_tr_b16 v[164:165], v233 offset:53248\n"
    "ds_read_b64_tr_b16 v[166:167], v233 offset:55296\n"
    "ds_read_b64_tr_b16 v[168:169], v234 offset:53248\n"
    "ds_read_b64_tr_b16 v[170:171], v234 offset:55296\n"
    "ds_read_b64_tr_b16 v[172:173], v235 offset:53248\n"
    "ds_read_b64_tr_b16 v[174:175], v235 offset:55296\n"
    "ds_read_b64_tr_b16 v[176:177], v232 offset:61440\n"
    "ds_read_b64_tr_b16 v[178:179], v232 offset:63488\n"
    "ds_read_b64_tr_b16 v[180:181], v233 offset:61440\n"
    "ds_read_b64_tr_b16 v[182:183], v233 offset:63488\n"
    "ds_read_b64_tr_b16 v[184:185], v234 offset:61440\n"
    "ds_read_b64_tr_b16 v[186:187], v234 offset:63488\n"
    "ds_read_b64_tr_b16 v[188:189], v235 offset:61440\n"
    "ds_read_b64_tr_b16 v[190:191], v235 offset:63488\n"
    "s_mov_b32 s76, 0xbfb8aa3b\n"
    "v_mul_f32 v192, s76, v32\n"
    "v_mul_f32 v193, s76, v33\n"
    "v_mul_f32 v194, s76, v34\n"
    "v_mul_f32 v195, s76, v35\n"
    "v_mul_f32 v196, s76, v36\n"
    "v_mul_f32 v197, s76, v37\n"
    "v_mul_f32 v198, s76, v38\n"
    "v_mul_f32 v199, s76, v39\n"
    "v_mul_f32 v200, s76, v40\n"
    "v_mul_f32 v201, s76, v41\n"
    "v_mul_f32 v202, s76, v42\n"
    "v_mul_f32 v203, s76, v43\n"
    "v_mul_f32 v204, s76, v44\n"
    "v_mul_f32 v205, s76, v45\n"
    "v_mul_f32 v206, s76, v46\n"
    "v_mul_f32 v207, s76, v47\n"
    "v_exp_f32 v192, v192\n"
    "v_exp_f32 v193, v193\n"
    "v_exp_f32 v194, v194\n"
    "v_exp_f32 v195, v195\n"
    "v_exp_f32 v196, v196\n"
    "v_exp_f32 v197, v197\n"
    "v_exp_f32 v198, v198\n"
    "v_exp_f32 v199, v199\n"
    "v_exp_f32 v200, v200\n"
    "v_exp_f32 v201, v201\n"
    "v_exp_f32 v202, v202\n"
    "v_exp_f32 v203, v203\n"
    "v_exp_f32 v204, v204\n"
    "v_exp_f32 v205, v205\n"
    "v_exp_f32 v206, v206\n"
    "v_exp_f32 v207, v207\n"
    "v_add_f32 v192, 1.0, v192\n"
    "v_add_f32 v193, 1.0, v193\n"
    "v_add_f32 v194, 1.0, v194\n"
    "v_add_f32 v195, 1.0, v195\n"
    "v_add_f32 v196, 1.0, v196\n"
    "v_add_f32 v197, 1.0, v197\n"
    "v_add_f32 v198, 1.0, v198\n"
    "v_add_f32 v199, 1.0, v199\n"
    "v_add_f32 v200, 1.0, v200\n"
    "v_add_f32 v201, 1.0, v201\n"
    "v_add_f32 v202, 1.0, v202\n"
    "v_add_f32 v203, 1.0, v203\n"
    "v_add_f32 v204, 1.0, v204\n"
    "v_add_f32 v205, 1.0, v205\n"
    "v_add_f32 v206, 1.0, v206\n"
    "v_add_f32 v207, 1.0, v207\n"
    "v_rcp_f32 v192, v192\n"
    "v_rcp_f32 v193, v193\n"
    "v_rcp_f32 v194, v194\n"
    "v_rcp_f32 v195, v195\n"
    "v_rcp_f32 v196, v196\n"
    "v_rcp_f32 v197, v197\n"
    "v_rcp_f32 v198, v198\n"
    "v_rcp_f32 v199, v199\n"
    "v_rcp_f32 v200, v200\n"
    "v_rcp_f32 v201, v201\n"
    "v_rcp_f32 v202, v202\n"
    "v_rcp_f32 v203, v203\n"
    "v_rcp_f32 v204, v204\n"
    "v_rcp_f32 v205, v205\n"
    "v_rcp_f32 v206, v206\n"
    "v_rcp_f32 v207, v207\n"
    "v_mul_f32 v32, v32, v192\n"
    "v_mul_f32 v33, v33, v193\n"
    "v_mul_f32 v34, v34, v194\n"
    "v_mul_f32 v35, v35, v195\n"
    "v_mul_f32 v36, v36, v196\n"
    "v_mul_f32 v37, v37, v197\n"
    "v_mul_f32 v38, v38, v198\n"
    "v_mul_f32 v39, v39, v199\n"
    "v_mul_f32 v40, v40, v200\n"
    "v_mul_f32 v41, v41, v201\n"
    "v_mul_f32 v42, v42, v202\n"
    "v_mul_f32 v43, v43, v203\n"
    "v_mul_f32 v44, v44, v204\n"
    "v_mul_f32 v45, v45, v205\n"
    "v_mul_f32 v46, v46, v206\n"
    "v_mul_f32 v47, v47, v207\n"
    "s_cmp_lg_u32 s90, 0\n"
    "s_cbranch_scc1 .L_ret2_k\n"
    "s_mov_b32 s96, 131072\n"
    "s_setprio 0\n"
    ".Lsp_41_k:\n"
    "ds_read_b32 v12, v243 offset:36\n"
    "s_waitcnt lgkmcnt(0)\n"
    "v_readfirstlane_b32 s94, v12\n"
    "s_or_b32 s94, s94, 0x80808080\n"
    "s_sub_u32 s94, s94, s88\n"
    "s_and_b32 s94, s94, 0x80808080\n"
    "s_cmp_eq_u32 s94, 0x80808080\n"
    "s_cbranch_scc1 .Lspd_42_k\n"
    "s_sleep 1\n"
    "s_sub_u32 s96, s96, 1\n"
    "s_cbranch_scc0 .Lsp_41_k\n"
    ".Lspd_42_k:\n"
    "s_mov_b32 s91, 2\n"
    "s_branch .L_red2_k\n"
    ".L_ret2_k:\n"
    "s_setprio 0\n"
    "s_mov_b32 s96, 8192\n"
    "s_add_u32 s76, s93, 4096\n"
    "s_add_u32 s77, s93, 8192\n"
    "s_add_u32 s78, s93, 12288\n"
    ".Lsp_43_k:\n"
    "buffer_load_dwordx4 v[192:195], v244, s[48:51], s93 offen offset:0 sc1\n"
    "buffer_load_dwordx4 v[196:199], v244, s[48:51], s93 offen offset:1024 sc1\n"
    "buffer_load_dwordx4 v[200:203], v244, s[48:51], s93 offen offset:2048 sc1\n"
    "buffer_load_dwordx4 v[204:207], v244, s[48:51], s93 offen offset:3072 sc1\n"
    "buffer_load_dwordx4 v[208:211], v244, s[48:51], s76 offen offset:0 sc1\n"
    "buffer_load_dwordx4 v[212:215], v244, s[48:51], s76 offen offset:1024 sc1\n"
    "buffer_load_dwordx4 v[216:219], v244, s[48:51], s76 offen offset:2048 sc1\n"
    "buffer_load_dwordx4 v[220:223], v244, s[48:51], s76 offen offset:3072 sc1\n"
    "buffer_load_dwordx4 v[224:227], v244, s[48:51], s77 offen offset:0 sc1\n"
    "buffer_load_dwordx4 v[228:231], v244, s[48:51], s77 offen offset:1024 sc1\n"
    "buffer_load_dwordx4 v[232:235], v244, s[48:51], s77 offen offset:2048 sc1\n"
    "buffer_load_dwordx4 v[236:239], v244, s[48:51], s77 offen offset:3072 sc1\n"
    "buffer_load_dwordx4 v[8:11], v244, s[48:51], s78 offen offset:0 sc1\n"
    "buffer_load_dwordx4 v[12:15], v244, s[48:51], s78 offen offset:1024 sc1\n"
    "buffer_load_dwordx4 v[16:19], v244, s[48:51], s78 offen offset:2048 sc1\n"
    "buffer_load_dwordx4 v[20:23], v244, s[48:51], s78 offen offset:3072 sc1\n"
    "s_waitcnt vmcnt(14)\n"
    "v_xor_b32 v240, s70, v193\n"
    "v_bitop3_b32 v240, v240, v195, s70 bitop3:0xf6\n"
    "v_bitop3_b32 v240, v240, v197, s70 bitop3:0xf6\n"
    "v_bitop3_b32 v240, v240, v199, s70 bitop3:0xf6\n"
    "s_waitcnt vmcnt(12)\n"
    "v_bitop3_b32 v240, v240, v201, s70 bitop3:0xf6\n"
    "v_bitop3_b32 v240, v240, v203, s70 bitop3:0xf6\n"
    "v_bitop3_b32 v240, v240, v205, s70 bitop3:0xf6\n"
    "v_bitop3_b32 v240, v240, v207, s70 bitop3:0xf6\n"
    "s_waitcnt vmcnt(10)\n"
    "v_bitop3_b32 v240, v240, v209, s70 bitop3:0xf6\n"
    "v_bitop3_b32 v240, v240, v211, s70 bitop3:0xf6\n"
    "v_bitop3_b32 v240, v240, v213, s70 bitop3:0xf6\n"
    "v_bitop3_b32 v240, v240, v215, s70 bitop3:0xf6\n"
    "s_waitcnt vmcnt(8)\n"
    "v_bitop3_b32 v240, v240, v217, s70 bitop3:0xf6\n"
    "v_bitop3_b32 v240, v240, v219, s70 bitop3:0xf6\n"
    "v_bitop3_b32 v240, v240, v221, s70 bitop3:0xf6\n"
    "v_bitop3_b32 v240, v240, v223, s70 bitop3:0xf6\n"
    "s_waitcnt vmcnt(6)\n"
    "v_bitop3_b32 v240, v240, v225, s70 bitop3:0xf6\n"
    "v_bitop3_b32 v240, v240, v227, s70 bitop3:0xf6\n"
    "v_bitop3_b32 v240, v240, v229, s70 bitop3:0xf6\n"
    "v_bitop3_b32 v240, v240, v231, s70 bitop3:0xf6\n"
    "s_waitcnt vmcnt(4)\n"
    "v_bitop3_b32 v240, v240, v233, s70 bitop3:0xf6\n"
    "v_bitop3_b32 v240, v240, v235, s70 bitop3:0xf6\n"
    "v_bitop3_b32 v240, v240, v237, s70 bitop3:0xf6\n"
    "v_bitop3_b32 v240, v240, v239, s70 bitop3:0xf6\n"
    "s_waitcnt vmcnt(2)\n"
    "v_bitop3_b32 v240, v240, v9, s70 bitop3:0xf6\n"
    "v_bitop3_b32 v240, v240, v11, s70 bitop3:0xf6\n"
    "v_bitop3_b32 v240, v240, v13, s70 bitop3:0xf6\n"
    "v_bitop3_b32 v240, v240, v15, s70 bitop3:0xf6\n"
    "s_waitcnt vmcnt(0)\n"
    "v_bitop3_b32 v240, v240, v17, s70 bitop3:0xf6\n"
    "v_bitop3_b32 v240, v240, v19, s70 bitop3:0xf6\n"
    "v_bitop3_b32 v240, v240, v21, s70 bitop3:0xf6\n"
    "v_bitop3_b32 v240, v240, v23, s70 bitop3:0xf6\n"
    "v_cmp_eq_u32 vcc, 0, v240\n"
    "s_cmp_eq_u64 vcc, -1\n"
    "s_cbranch_scc1 .Lspd_44_k\n"
    "s_sub_u32 s96, s96, 1\n"
    "s_cbranch_scc0 .Lsp_43_k\n"
    ".Lspd_44_k:\n"
    "s_waitcnt lgkmcnt(0)\n"
    "v_mov_b32 v193, v194\n"
    "v_mov_b32 v194, v196\n"
    "v_mov_b32 v195, v198\n"
    "v_mov_b32 v201, v202\n"
    "v_mov_b32 v202, v204\n"
    "v_mov_b32 v203, v206\n"
    "v_mfma_f32_16x16x32_bf16 v[48:51], v[64:67], v[192:195], v[48:51]\n"
    "v_mfma_f32_16x16x32_bf16 v[52:55], v[68:71], v[192:195], v[52:55]\n"
    "v_mfma_f32_16x16x32_bf16 v[56:59], v[72:75], v[192:195], v[56:59]\n"
    "v_mfma_f32_16x16x32_bf16 v[60:63], v[76:79], v[192:195], v[60:63]\n"
    "v_mov_b32 v209, v210\n"
    "v_mov_b32 v210, v212\n"
    "v_mov_b32 v211, v214\n"
    "v_mfma_f32_16x16x32_bf16 v[48:51], v[80:83], v[200:203], v[48:51]\n"
    "v_mfma_f32_16x16x32_bf16 v[52:55], v[84:87], v[200:203], v[52:55]\n"
    "v_mfma_f32_16x16x32_bf16 v[56:59], v[88:91], v[200:203], v[56:59]\n"
    "v_mfma_f32_16x16x32_bf16 v[60:63], v[92:95], v[200:203], v[60:63]\n"
    "v_mov_b32 v217, v218\n"
    "v_mov_b32 v218, v220\n"
    "v_mov_b32 v219, v222\n"
    "v_mfma_f32_16x16x32_bf16 v[48:51], v[96:99], v[208:211], v[48:51]\n"
    "v_mfma_f32_16x16x32_bf16 v[52:55], v[100:103], v[208:211], v[52:55]\n"
    "v_mfma_f32_16x16x32_bf16 v[56:59], v[104:107], v[208:211], v[56:59]\n"
    "v_mfma_f32_16x16x32_bf16 v[60:63], v[108:111], v[208:211], v[60:63]\n"
    "v_mov_b32 v225, v226\n"
    "v_mov_b32 v226, v228\n"
    "v_mov_b32 v227, v230\n"
    "v_mfma_f32_16x16x32_bf16 v[48:51], v[112:115], v[216:219], v[48:51]\n"
    "v_mfma_f32_16x16x32_bf16 v[52:55], v[116:119], v[216:219], v[52:55]\n"
    "v_mfma_f32_16x16x32_bf16 v[56:59], v[120:123], v[216:219], v[56:59]\n"
    "v_mfma_f32_16x16x32_bf16 v[60:63], v[124:127], v[216:219], v[60:63]\n"
    "v_mov_b32 v233, v234\n"
    "v_mov_b32 v234, v236\n"
    "v_mov_b32 v235, v238\n"
    "v_mfma_f32_16x16x32_bf16 v[48:51], v[128:131], v[224:227], v[48:51]\n"
    "v_mfma_f32_16x16x32_bf16 v[52:55], v[132:135], v[224:227], v[52:55]\n"
    "v_mfma_f32_16x16x32_bf16 v[56:59], v[136:139], v[224:227], v[56:59]\n"
    "v_mfma_f32_16x16x32_bf16 v[60:63], v[140:143], v[224:227], v[60:63]\n"
    "v_mov_b32 v9, v10\n"
    "v_mov_b32 v10, v12\n"
    "v_mov_b32 v11, v14\n"
    "v_mfma_f32_16x16x32_bf16 v[48:51], v[144:147], v[232:235], v[48:51]\n"
    "v_mfma_f32_16x16x32_bf16 v[52:55], v[148:151], v[232:235], v[52:55]\n"
    "v_mfma_f32_16x16x32_bf16 v[56:59], v[152:155], v[232:235], v[56:59]\n"
    "v_mfma_f32_16x16x32_bf16 v[60:63], v[156:159], v[232:235], v[60:63]\n"
    "v_mov_b32 v17, v18\n"
    "v_mov_b32 v18, v20\n"
    "v_mov_b32 v19, v22\n"
    "v_mfma_f32_16x16x32_bf16 v[48:51], v[160:163], v[8:11], v[48:51]\n"
    "v_mfma_f32_16x16x32_bf16 v[52:55], v[164:167], v[8:11], v[52:55]\n"
    "v_mfma_f32_16x16x32_bf16 v[56:59], v[168:171], v[8:11], v[56:59]\n"
    "v_mfma_f32_16x16x32_bf16 v[60:63], v[172:175], v[8:11], v[60:63]\n"
    "v_mfma_f32_16x16x32_bf16 v[48:51], v[176:179], v[16:19], v[48:51]\n"
    "v_mfma_f32_16x16x32_bf16 v[52:55], v[180:183], v[16:19], v[52:55]\n"
    "v_mfma_f32_16x16x32_bf16 v[56:59], v[184:187], v[16:19], v[56:59]\n"
    "v_mfma_f32_16x16x32_bf16 v[60:63], v[188:191], v[16:19], v[60:63]\n"
    "s_setprio 2\n"
    "s_mov_b32 s76, 0xffff0000\n"
    "v_lshlrev_b32 v224, 16, v24\n"
    "v_and_b32 v225, s76, v24\n"
    "v_lshlrev_b32 v226, 16, v25\n"
    "v_and_b32 v227, s76, v25\n"
    "v_lshlrev_b32 v228, 16, v26\n"
    "v_and_b32 v229, s76, v26\n"
    "v_lshlrev_b32 v230, 16, v27\n"
    "v_and_b32 v231, s76, v27\n"
    "v_lshlrev_b32 v232, 16, v28\n"
    "v_and_b32 v233, s76, v28\n"
    "v_lshlrev_b32 v234, 16, v29\n"
    "v_and_b32 v235, s76, v29\n"
    "v_lshlrev_b32 v236, 16, v30\n"
    "v_and_b32 v237, s76, v30\n"
    "v_lshlrev_b32 v238, 16, v31\n"
    "v_and_b32 v239, s76, v31\n"
    "v_pk_fma_f32 v[224:225], v[48:49], v[32:33], v[224:225]\n"
    "v_pk_fma_f32 v[226:227], v[50:51], v[34:35], v[226:227]\n"
    "v_cvt_pk_bf16_f32 v24, v224, v225\n"
    "v_cvt_pk_bf16_f32 v25, v226, v227\n"
    "buffer_store_dwordx2 v[24:25], v245, s[44:47], s85 offen offset:0\n"
    "v_pk_fma_f32 v[228:229], v[52:53], v[36:37], v[228:229]\n"
    "v_pk_fma_f32 v[230:231], v[54:55], v[38:39], v[230:231]\n"
    "v_cvt_pk_bf16_f32 v26, v228, v229\n"
    "v_cvt_pk_bf16_f32 v27, v230, v231\n"
    "buffer_store_dwordx2 v[26:27], v245, s[44:47], s85 offen offset:32\n"
    "v_pk_fma_f32 v[232:233], v[56:57], v[40:41], v[232:233]\n"
    "v_pk_fma_f32 v[234:235], v[58:59], v[42:43], v[234:235]\n"
    "v_cvt_pk_bf16_f32 v28, v232, v233\n"
    "v_cvt_pk_bf16_f32 v29, v234, v235\n"
    "buffer_store_dwordx2 v[28:29], v245, s[44:47], s85 offen offset:64\n"
    "v_pk_fma_f32 v[236:237], v[60:61], v[44:45], v[236:237]\n"
    "v_pk_fma_f32 v[238:239], v[62:63], v[46:47], v[238:239]\n"
    "v_cvt_pk_bf16_f32 v30, v236, v237\n"
    "v_cvt_pk_bf16_f32 v31, v238, v239\n"
    "buffer_store_dwordx2 v[30:31], v245, s[44:47], s85 offen offset:96\n"
    "s_branch .L_end_k\n"
    ".L_red1_k:\n"
    "s_setprio 2\n"
    "ds_read_b128 v[32:35], v246 offset:0\n"
    "ds_read_b128 v[36:39], v246 offset:256\n"
    "ds_read_b128 v[40:43], v246 offset:2048\n"
    "ds_read_b128 v[44:47], v246 offset:2304\n"
    "ds_read_b128 v[48:51], v246 offset:4096\n"
    "ds_read_b128 v[52:55], v246 offset:4352\n"
    "ds_read_b128 v[56:59], v246 offset:6144\n"
    "ds_read_b128 v[60:63], v246 offset:6400\n"
    "ds_read_b128 v[12:15], v247 offset:8192\n"
    "ds_read_b128 v[16:19], v247 offset:8448\n"
    "s_lshl_b32 s79, s74, 11\n"
    "s_add_u32 s79, s79, s93\n"
    "s_waitcnt lgkmcnt(0)\n"
    "v_add_f32 v12, v12, v13\n"
    "v_add_f32 v14, v14, v15\n"
    "v_add_f32 v12, v12, v14\n"
    "v_rcp_f32 v12, v12\n"
    "v_add_f32 v16, v16, v17\n"
    "v_add_f32 v18, v18, v19\n"
    "v_add_f32 v16, v16, v18\n"
    "v_rcp_f32 v16, v16\n"
    "v_pk_add_f32 v[32:33], v[32:33], v[40:41]\n"
    "v_pk_add_f32 v[48:49], v[48:49], v[56:57]\n"
    "v_pk_add_f32 v[34:35], v[34:35], v[42:43]\n"
    "v_pk_add_f32 v[50:51], v[50:51], v[58:59]\n"
    "v_pk_add_f32 v[36:37], v[36:37], v[44:45]\n"
    "v_pk_add_f32 v[52:53], v[52:53], v[60:61]\n"
    "v_pk_add_f32 v[38:39], v[38:39], v[46:47]\n"
    "v_pk_add_f32 v[54:55], v[54:55], v[62:63]\n"
    "v_pk_add_f32 v[32:33], v[32:33], v[48:49]\n"
    "v_pk_add_f32 v[34:35], v[34:35], v[50:51]\n"
    "v_pk_add_f32 v[36:37], v[36:37], v[52:53]\n"
    "v_pk_add_f32 v[38:39], v[38:39], v[54:55]\n"
    "v_pk_mul_f32 v[32:33], v[32:33], v[12:13] op_sel_hi:[1,0]\n"
    "v_pk_mul_f32 v[34:35], v[34:35], v[12:13] op_sel_hi:[1,0]\n"
    "v_pk_mul_f32 v[36:37], v[36:37], v[16:17] op_sel_hi:[1,0]\n"
    "v_pk_mul_f32 v[38:39], v[38:39], v[16:17] op_sel_hi:[1,0]\n"
    "v_mov_b32 v13, 1\n"
    "ds_write_b32 v242, v13 offset:64\n"
    "v_cvt_pk_bf16_f32 v16, v32, v33\n"
    "v_cvt_pk_bf16_f32 v17, v34, v35\n"
    "v_cvt_pk_bf16_f32 v18, v36, v37\n"
    "v_cvt_pk_bf16_f32 v19, v38, v39\n"
    "s_lshl_b32 s76, s74, 10\n"
    "s_add_u32 s76, s76, s86\n"
    "s_lshl_b32 s95, s74, 3\n"
    "s_add_u32 s95, s95, s87\n"
    "s_mov_b32 s90, 3\n"
    "buffer_store_dwordx4 v[16:19], v244, s[48:51], s76 offen sc1\n"
    "v_not_b32 v12, v20\n"
    "v_not_b32 v13, v20\n"
    "v_not_b32 v14, v20\n"
    "v_not_b32 v15, v20\n"
    "buffer_store_dwordx4 v[12:15], v244, s[48:51], s79 offen sc1\n"
    "buffer_store_dwordx4 v[12:15], v244, s[48:51], s79 offen offset:1024 sc1\n"
    "s_cmp_eq_u32 s91, 0\n"
    "s_cbranch_scc1 .L_hw_k\n"
    "s_cmp_eq_u32 s91, 3\n"
    "s_cbranch_scc1 .L_hgo_k\n"
    "s_cmp_eq_u32 s91, 10\n"
    "s_cbranch_scc1 .L_rc2_k\n"
    "s_cmp_eq_u32 s91, 14\n"
    "s_cbranch_scc1 .L_rc6_k\n"
    "s_setprio 0\n"
    "s_waitcnt vmcnt(0)\n"
    "s_cmp_lg_u32 s90, 3\n"
    "s_cbranch_scc1 .Lnf_45_k\n"
    "s_mov_b32 s90, 1\n"
    "s_mov_b64 exec, 1\n"
    "buffer_store_dwordx2 v[20:21], v22, s[48:51], s95 offen sc1\n"
    "s_mov_b64 exec, -1\n"
    ".Lnf_45_k:\n"
    "s_setprio 2\n"
    "s_branch .L_ret1_k\n"
    ".L_red2_k:\n"
    "s_setprio 2\n"
    "ds_read_b128 v[192:195], v246 offset:0\n"
    "ds_read_b128 v[196:199], v246 offset:256\n"
    "ds_read_b128 v[200:203], v246 offset:2048\n"
    "ds_read_b128 v[204:207], v246 offset:2304\n"
    "ds_read_b128 v[208:211], v246 offset:4096\n"
    "ds_read_b128 v[212:215], v246 offset:4352\n"
    "ds_read_b128 v[216:219], v246 offset:6144\n"
    "ds_read_b128 v[220:223], v246 offset:6400\n"
    "ds_read_b128 v[12:15], v247 offset:8192\n"
    "ds_read_b128 v[16:19], v247 offset:8448\n"
    "s_lshl_b32 s79, s74, 11\n"
    "s_add_u32 s79, s79, s93\n"
    "s_waitcnt lgkmcnt(0)\n"
    "v_add_f32 v12, v12, v13\n"
    "v_add_f32 v14, v14, v15\n"
    "v_add_f32 v12, v12, v14\n"
    "v_rcp_f32 v12, v12\n"
    "v_add_f32 v16, v16, v17\n"
    "v_add_f32 v18, v18, v19\n"
    "v_add_f32 v16, v16, v18\n"
    "v_rcp_f32 v16, v16\n"
    "v_pk_add_f32 v[192:193], v[192:193], v[200:201]\n"
    "v_pk_add_f32 v[208:209], v[208:209], v[216:217]\n"
    "v_pk_add_f32 v[194:195], v[194:195], v[202:203]\n"
    "v_pk_add_f32 v[210:211], v[210:211], v[218:219]\n"
    "v_pk_add_f32 v[196:197], v[196:197], v[204:205]\n"
    "v_pk_add_f32 v[212:213], v[212:213], v[220:221]\n"
    "v_pk_add_f32 v[198:199], v[198:199], v[206:207]\n"
    "v_pk_add_f32 v[214:215], v[214:215], v[222:223]\n"
    "v_pk_add_f32 v[192:193], v[192:193], v[208:209]\n"
    "v_pk_add_f32 v[194:195], v[194:195], v[210:211]\n"
    "v_pk_add_f32 v[196:197], v[196:197], v[212:213]\n"
    "v_pk_add_f32 v[198:199], v[198:199], v[214:215]\n"
    "v_pk_mul_f32 v[192:193], v[192:193], v[12:13] op_sel_hi:[1,0]\n"
    "v_pk_mul_f32 v[194:195], v[194:195], v[12:13] op_sel_hi:[1,0]\n"
    "v_pk_mul_f32 v[196:197], v[196:197], v[16:17] op_sel_hi:[1,0]\n"
    "v_pk_mul_f32 v[198:199], v[198:199], v[16:17] op_sel_hi:[1,0]\n"
    "v_cvt_pk_bf16_f32 v12, v192, v193\n"
    "v_mov_b32 v13, s70\n"
    "v_cvt_pk_bf16_f32 v14, v194, v195\n"
    "v_mov_b32 v15, s70\n"
    "v_cvt_pk_bf16_f32 v16, v196, v197\n"
    "v_mov_b32 v17, s70\n"
    "v_cvt_pk_bf16_f32 v18, v198, v199\n"
    "v_mov_b32 v19, s70\n"
    "buffer_store_dwordx4 v[12:15], v244, s[48:51], s79 offen sc1\n"
    "buffer_store_dwordx4 v[16:19], v244, s[48:51], s79 offen offset:1024 sc1\n"
    "s_mov_b32 s90, 1\n"
    "s_cmp_eq_u32 s91, 4\n"
    "s_cbranch_scc1 .L_ret4_k\n"
    "s_cmp_eq_u32 s91, 5\n"
    "s_cbranch_scc1 .L_ret5_k\n"
    "s_cmp_eq_u32 s91, 6\n"
    "s_cbranch_scc1 .L_ret6_k\n"
    "s_branch .L_ret2_k\n"
    ".L_end_k:\n"
    :
    : "s"(__builtin_amdgcn_kernarg_segment_ptr()), "s"(blockIdx.x), "v"(threadIdx.x), "s"((unsigned)(size_t)lds)
    : "v1", "v2", "v3", "v4", "v5", "v6", "v7", "v8", "v9", "v10", "v11", "v12", "v13", "v14", "v15", "v16", "v17", "v18", "v19", "v20", "v21", "v22", "v23", "v24", "v25", "v26", "v27", "v28", "v29", "v30", "v31", "v32", "v33", "v34", "v35", "v36", "v37", "v38", "v39", "v40", "v41", "v42", "v43", "v44", "v45", "v46", "v47", "v48", "v49", "v50", "v51", "v52", "v53", "v54", "v55", "v56", "v57", "v58", "v59", "v60", "v61", "v62", "v63", "v64", "v65", "v66", "v67", "v68", "v69", "v70", "v71", "v72", "v73", "v74", "v75", "v76", "v77", "v78", "v79", "v80", "v81", "v82", "v83", "v84", "v85", "v86", "v87", "v88", "v89", "v90", "v91", "v92", "v93", "v94", "v95", "v96", "v97", "v98", "v99", "v100", "v101", "v102", "v103", "v104", "v105", "v106", "v107", "v108", "v109", "v110", "v111", "v112", "v113", "v114", "v115", "v116", "v117", "v118", "v119", "v120", "v121", "v122", "v123", "v124", "v125", "v126", "v127", "v128", "v129", "v130", "v131", "v132", "v133", "v134", "v135", "v136", "v137", "v138", "v139", "v140", "v141", "v142", "v143", "v144", "v145", "v146", "v147", "v148", "v149", "v150", "v151", "v152", "v153", "v154", "v155", "v156", "v157", "v158", "v159", "v160", "v161", "v162", "v163", "v164", "v165", "v166", "v167", "v168", "v169", "v170", "v171", "v172", "v173", "v174", "v175", "v176", "v177", "v178", "v179", "v180", "v181", "v182", "v183", "v184", "v185", "v186", "v187", "v188", "v189", "v190", "v191", "v192", "v193", "v194", "v195", "v196", "v197", "v198", "v199", "v200", "v201", "v202", "v203", "v204", "v205", "v206", "v207", "v208", "v209", "v210", "v211", "v212", "v213", "v214", "v215", "v216", "v217", "v218", "v219", "v220", "v221", "v222", "v223", "v224", "v225", "v226", "v227", "v228", "v229", "v230", "v231", "v232", "v233", "v234", "v235", "v236", "v237", "v238", "v239", "v240", "v241", "v242", "v243", "v244", "v245", "v246", "v247", "v248", "v249", "v250", "v251", "v252", "v253", "v254", "v255", "s12", "s13", "s14", "s15", "s16", "s17", "s18", "s19", "s20", "s21", "s22", "s23", "s24", "s25", "s26", "s27", "s28", "s29", "s30", "s31", "s33", "s34", "s35", "s36", "s37", "s38", "s39", "s40", "s41", "s42", "s43", "s44", "s45", "s46", "s47", "s48", "s49", "s50", "s51", "s52", "s53", "s54", "s55", "s56", "s57", "s58", "s59", "s60", "s61", "s62", "s63", "s64", "s65", "s66", "s67", "s68", "s69", "s70", "s71", "s72", "s73", "s74", "s75", "s76", "s77", "s78", "s79", "s80", "s81", "s82", "s83", "s84", "s85", "s86", "s87", "s88", "s89", "s90", "s91", "s92", "s93", "s94", "s95", "s96", "s97", "s98", "s99", "vcc", "scc", "memory");
}


extern "C" void kernel_launch(void* const* d_in, const int* in_sizes, int n_in, void* d_out, int out_size,
                              void* d_ws, size_t ws_size, hipStream_t stream) {

  recurrent_scan__grouped_kv_mqa__dense_activation_relu__prompt_kernel<<<256, 512, 0, stream>>>(d_in[0], d_in[1], d_in[2], d_in[3], d_in[4], d_in[6], d_in[7], d_out, d_ws);

}
```
